```python
import torch

def _softmax(x, axis=-1):
    return torch.softmax(x, dim=axis)
def _gelu(x):
    return torch.nn.functional.gelu(x, approximate="tanh")
def _silu(x):
    return torch.nn.functional.silu(x)
def _sigmoid(x):
    return torch.sigmoid(x)
def _layernorm(x, axis=-1, eps=1e-5):
    m = x.mean(dim=axis, keepdim=True)
    v = x.var(dim=axis, keepdim=True, unbiased=False)
    return (x - m) / torch.sqrt(v + eps)
def _scatter_add(tgt, idx, src, axis=0):
    return tgt.clone().index_add_(axis, idx, src)
def _segment_sum(src, seg, n):
    out = torch.zeros((n,) + src.shape[1:], dtype=src.dtype)
    return out.index_add_(0, seg, src)
def _pad(x, pw):
    flat = tuple(v for pair in reversed(pw) for v in pair)
    return torch.nn.functional.pad(x, flat)

kernel_name = 'gated_mix__ssm_conv_s5__dense_activation_squared_relu__prompt'

def setup_inputs():

    _w = (lambda a: (lambda t: t.to(torch.bfloat16) if t.is_floating_point() else t)(torch.as_tensor(a)))
    def _A_setup():
        B, S, D = 1, 2048, 1024
        P = D
        Lambda_re = -0.5 * (lambda _x: torch.exp(torch.as_tensor(_x)))((lambda *s: torch.randn(*s, dtype=torch.bfloat16) * 0.4)(P))
        Lambda_im = (lambda *s: torch.randn(*s, dtype=torch.bfloat16) * 0.8)(P) * 1.0
        B_re = (lambda *s: torch.randn(*s, dtype=torch.bfloat16) * 0.8)(P, D) * (D ** -0.5)
        B_im = (lambda *s: torch.randn(*s, dtype=torch.bfloat16) * 0.8)(P, D) * (D ** -0.5)
        C_re = (lambda *s: torch.randn(*s, dtype=torch.bfloat16) * 0.8)(D, P) * (P ** -0.5)
        C_im = (lambda *s: torch.randn(*s, dtype=torch.bfloat16) * 0.8)(D, P) * (P ** -0.5)
        log_dt = (lambda *s: torch.randn(*s, dtype=torch.bfloat16) * 0.4)(P) * 0.3 - 2.0
        Dskip = (lambda *s: torch.randn(*s, dtype=torch.bfloat16) * 0.1)(D)
        return {
            "x": _w((lambda *s: torch.randn(*s, dtype=torch.bfloat16) * 0.05)(B, S, D)),
            "Lambda_re": _w(Lambda_re),
            "Lambda_im": _w(Lambda_im),
            "B_re": _w(B_re),
            "B_im": _w(B_im),
            "C_re": _w(C_re),
            "C_im": _w(C_im),
            "log_dt": _w(log_dt),
            "Dskip": _w(Dskip),
        }
    _A_in = _A_setup()

    def _B_setup():
        B, S, D, F = 1, 2048, 1024, 3584
        return {
            "x": _w((lambda *s: torch.randn(*s, dtype=torch.bfloat16) * 0.05)(B, S, D)),
            "w1": _w((lambda *s: torch.randn(*s, dtype=torch.bfloat16) * 0.05)(D, F)),
            "b1": _w((lambda *s: torch.randn(*s, dtype=torch.bfloat16) * 0.05)(F)),
            "w2": _w((lambda *s: torch.randn(*s, dtype=torch.bfloat16) * 0.05)(F, D)),
            "b2": _w((lambda *s: torch.randn(*s, dtype=torch.bfloat16) * 0.05)(D)),
        }
    _B_in = _B_setup()

    return {
        "A_x": _A_in["x"],
        "A_Lambda_re": _A_in["Lambda_re"],
        "A_Lambda_im": _A_in["Lambda_im"],
        "A_B_re": _A_in["B_re"],
        "A_B_im": _A_in["B_im"],
        "A_C_re": _A_in["C_re"],
        "A_C_im": _A_in["C_im"],
        "A_log_dt": _A_in["log_dt"],
        "A_Dskip": _A_in["Dskip"],
        "B_x": _B_in["x"],
        "B_w1": _B_in["w1"],
        "B_b1": _B_in["b1"],
        "B_w2": _B_in["w2"],
        "B_b2": _B_in["b2"],
        "gate_w": _w((lambda *s: torch.randn(*s, dtype=torch.bfloat16) * 0.05)(1024) * 16.0),
    }

def reference(A_x, A_Lambda_re, A_Lambda_im, A_B_re, A_B_im, A_C_re, A_C_im, A_log_dt, A_Dskip, B_x, B_w1, B_b1, B_w2, B_b2, gate_w):
    def _A_ref(x, Lambda_re, Lambda_im, B_re, B_im, C_re, C_im, log_dt, Dskip):
        Bsz, L, H = x.shape
        Lam = Lambda_re + 1j * Lambda_im
        Bmat = B_re + 1j * B_im
        Cmat = C_re + 1j * C_im
        dt = torch.exp(log_dt)
        Lam_bar = torch.exp(dt * Lam)
        B_bar = ((Lam_bar - 1.0) / Lam)[:, None] * Bmat
        Bu = torch.einsum("bld,pd->blp", x + 0j, B_bar)
        h = torch.zeros_like(Bu[:, 0, :])
        hs = []
        for t in range(L):
            h = Lam_bar[None, :] * h + Bu[:, t, :]
            hs.append(h)
        hseq = torch.stack(hs, 1)
        y = 2.0 * torch.einsum("blp,dp->bld", hseq, Cmat).real + x * Dskip[None, None, :]
        return y

    def _B_ref(x, w1, b1, w2, b2):
        h = torch.einsum("bsd,df->bsf", x, w1) + b1
        zero = torch.zeros_like(h)
        r = torch.where(h > zero, h, zero)
        h = r * r
        out = torch.einsum("bsf,fd->bsd", h, w2) + b2
        return out

    x = A_x
    g = _sigmoid((x * gate_w).sum(-1, keepdims=True))
    a = _A_ref(x, A_Lambda_re, A_Lambda_im, A_B_re, A_B_im, A_C_re, A_C_im, A_log_dt, A_Dskip)
    b = _B_ref(x, B_w1, B_b1, B_w2, B_b2)
    return g * a + (1 - g) * b
```

```cpp
#include <hip/hip_runtime.h>
#include <hip/hip_bf16.h>
#define ROUND_DT 1
#define ROUND_G 0
typedef unsigned short u16;
__device__ __forceinline__ float bf2f(u16 v){ return __uint_as_float(((unsigned)v)<<16); }
__device__ __forceinline__ float rbf(float f){ unsigned u=__float_as_uint(f); u = (u + 0x7fffu + ((u>>16)&1)) & 0xffff0000u; return __uint_as_float(u);}
constexpr int L=2048, D=1024, P=1024, F=3584;

__global__ void k_gate(const u16* x, const u16* gw, float* g){
  int row=blockIdx.x; int t=threadIdx.x; float s=0;
  for(int i=t;i<D;i+=64){ float p=bf2f(x[row*D+i])*bf2f(gw[i]); if(ROUND_G) p=rbf(p); s+=p; }
  for(int o=32;o;o>>=1) s+=__shfl_xor(s,o);
  if(ROUND_G) s=rbf(s);
  float gg=1.f/(1.f+__expf(-s)); if(ROUND_G) gg=rbf(gg);
  if(t==0) g[row]=gg;
}
__global__ void k_gemm(const u16* A, const float* Af, int lda, const u16* B, int sbn, int sbk, float* C, int ldc, int K, float alpha, int accum){
  __shared__ float As[16][17], Bs[16][17];
  int tx=threadIdx.x&15, ty=threadIdx.x>>4; int m0=blockIdx.y*16, n0=blockIdx.x*16; float s=0;
  for(int k0=0;k0<K;k0+=16){
    As[ty][tx]= A? bf2f(A[(size_t)(m0+ty)*lda+k0+tx]) : Af[(size_t)(m0+ty)*lda+k0+tx];
    Bs[ty][tx]=bf2f(B[(size_t)(n0+ty)*sbn+(size_t)(k0+tx)*sbk]);
    __syncthreads();
    for(int k=0;k<16;k++) s+=As[ty][k]*Bs[tx][k];
    __syncthreads();
  }
  float* c=&C[(size_t)(m0+ty)*ldc+n0+tx];
  *c = accum? *c+alpha*s : alpha*s;
}
__global__ void k_scan(float* ure, float* uim, const u16* lre, const u16* lim, const u16* ldt){
  int p=blockIdx.x*64+threadIdx.x;
  float dt=expf(bf2f(ldt[p])); if(ROUND_DT) dt=rbf(dt);
  float lr=bf2f(lre[p]), li=bf2f(lim[p]);
  float e=expf(dt*lr); float ar=e*cosf(dt*li), ai=e*sinf(dt*li);
  float nr=ar-1.f, ni=ai; float den=lr*lr+li*li;
  float cr=(nr*lr+ni*li)/den, ci=(ni*lr-nr*li)/den;
  float hr=0,hi=0;
  for(int t=0;t<L;t++){ float ur=ure[t*P+p], ui=uim[t*P+p];
    float br=cr*ur-ci*ui, bi=cr*ui+ci*ur;
    float nr2=ar*hr-ai*hi+br, ni2=ar*hi+ai*hr+bi; hr=nr2; hi=ni2; ure[t*P+p]=hr; uim[t*P+p]=hi; }
}
__global__ void k_act(float* h, const u16* b1){ int i=blockIdx.x*256+threadIdx.x; float v=h[i]+bf2f(b1[i%F]); v=v>0?v:0; h[i]=v*v; }
__global__ void k_fin(float* out, const float* ya, const float* yb, const float* g, const u16* x, const u16* dsk, const u16* b2){
  int i=blockIdx.x*256+threadIdx.x; int r=i/D, c=i%D; float gg=g[r];
  float a=ya[i]+bf2f(x[i])*bf2f(dsk[c]); float b=yb[i]+bf2f(b2[c]);
  out[i]=gg*a+(1.f-gg)*b; }
extern "C" void kernel_launch(void* const* d_in, const int* in_sizes, int n_in, void* d_out, int out_size, void* d_ws, size_t ws_size, hipStream_t s){
  const u16* x=(const u16*)d_in[0]; const u16* lre=(const u16*)d_in[1]; const u16* lim=(const u16*)d_in[2];
  const u16* Bre=(const u16*)d_in[3]; const u16* Bim=(const u16*)d_in[4]; const u16* Cre=(const u16*)d_in[5]; const u16* Cim=(const u16*)d_in[6];
  const u16* ldt=(const u16*)d_in[7]; const u16* dsk=(const u16*)d_in[8];
  const u16* w1=(const u16*)d_in[10]; const u16* b1=(const u16*)d_in[11]; const u16* w2=(const u16*)d_in[12]; const u16* b2=(const u16*)d_in[13]; const u16* gw=(const u16*)d_in[14];
  float* ws=(float*)d_ws; float* g=ws; float* ure=ws+4096; float* uim=ure+L*P; float* h=uim+L*P; float* ya=h+(size_t)L*F; float* yb=ya+L*D;
  k_gate<<<L,64,0,s>>>(x,gw,g);
  k_gemm<<<dim3(P/16,L/16),256,0,s>>>(x,nullptr,D,Bre,D,1,ure,P,D,1.f,0);
  k_gemm<<<dim3(P/16,L/16),256,0,s>>>(x,nullptr,D,Bim,D,1,uim,P,D,1.f,0);
  k_gemm<<<dim3(F/16,L/16),256,0,s>>>(x,nullptr,D,w1,1,F,h,F,D,1.f,0);
  k_scan<<<P/64,64,0,s>>>(ure,uim,lre,lim,ldt);
  k_act<<<L*F/256,256,0,s>>>(h,b1);
  k_gemm<<<dim3(D/16,L/16),256,0,s>>>(nullptr,ure,P,Cre,P,1,ya,D,P,2.f,0);
  k_gemm<<<dim3(D/16,L/16),256,0,s>>>(nullptr,uim,P,Cim,P,1,ya,D,P,-2.f,1);
  k_gemm<<<dim3(D/16,L/16),256,0,s>>>(nullptr,h,F,w2,1,D,yb,D,F,1.f,0);
  k_fin<<<L*D/256,256,0,s>>>((float*)d_out,ya,yb,g,x,dsk,b2);
}
```

```cpp
#include <hip/hip_runtime.h>

__global__ __launch_bounds__(256, 1) __attribute__((aligned(4096))) void gated_mix_s5_ffn_kernel(
    const void* x, unsigned xs, unsigned xf, const void* w1, unsigned w1s, unsigned w1f,
    const void* bre, unsigned bres, unsigned bref, const void* bim, unsigned bims, unsigned bimf,
    const void* w2, unsigned w2s, unsigned w2f, const void* cre, unsigned cres, unsigned cref,
    const void* cim, unsigned cims, unsigned cimf, void* out, unsigned outs, unsigned outf,
    void* ws, unsigned wss, unsigned wsf,
    const void* gw, const void* b1, const void* lre, const void* lim, const void* ldt, const void* dsk, const void* b2) {
  __shared__ __attribute__((aligned(16))) char lds[163840];
  unsigned tid = threadIdx.x;
  asm volatile(
".Lk_start:\n"
"s_load_dwordx16 s[4:19], %1, 0x0\n"
"s_load_dwordx16 s[20:35], %1, 0x40\n"
"s_load_dwordx16 s[36:51], %1, 0x80\n"
"s_load_dwordx2 s[52:53], %1, 0xc0\n"
"s_mov_b32 s2, %2\n"
"v_readfirstlane_b32 s54, v0\n"
"v_and_b32 v1, 63, v0\n"
"s_lshr_b32 s54, s54, 6\n"
"s_lshr_b32 s68, s2, 3\n"
"s_lshl_b32 s68, s68, 2\n"
"s_add_u32 s68, s68, s54\n"
"s_lshl_b32 s68, s68, 9\n"
"v_and_b32 v6, 3, v1\n"
"v_lshl_add_u32 v6, v6, 7, s68\n"
"v_min_u32 v6, .Lk_end-.Lk_pc-4, v6\n"
"s_getpc_b64 s[94:95]\n"
".Lk_pc:\n"
"global_load_dword v199, v6, s[94:95]\n"
"s_and_b32 s67, s2, 1\n"
"s_lshl_b32 s67, s67, 3\n"
"s_bfe_u32 s68, s2, 0x30003\n"
"s_or_b32 s55, s67, s68\n"
"s_bfe_u32 s57, s2, 0x20001\n"
"s_lshr_b32 s69, s2, 6\n"
"s_lshl_b32 s67, s57, 2\n"
"s_or_b32 s56, s67, s69\n"
"s_lshr_b32 s58, s55, 1\n"
"s_and_b32 s67, s55, 1\n"
"s_lshl_b32 s67, s67, 2\n"
"s_or_b32 s59, s67, s69\n"
"s_lshl_b32 s67, s68, 1\n"
"s_and_b32 s68, s2, 1\n"
"s_and_b32 s67, s67, 12\n"
"s_lshl_b32 s68, s68, 1\n"
"s_add_u32 s67, s67, s68\n"
"s_and_b32 s68, s56, 14\n"
"s_add_u32 s60, s67, s68\n"
"s_and_b32 s60, s60, 15\n"
"s_lshl_b32 s63, s54, 12\n"
"s_lshl_b32 s64, s54, 11\n"
"s_mov_b32 s62, 400000\n"
"s_lshl_b32 s65, s60, 7\n"
"s_mul_i32 s66, s65, 3584\n"
"v_lshlrev_b32 v7, 4, v1\n"
"v_lshrrev_b32 v2, 5, v1\n"
"v_bfe_u32 v3, v1, 3, 2\n"
"v_lshl_add_u32 v2, v2, 3, v3\n"
"v_lshrrev_b32 v3, 4, v1\n"
"v_and_b32 v4, 7, v1\n"
"v_xor_b32 v3, v3, v4\n"
"v_lshlrev_b32 v3, 4, v3\n"
"s_lshl_b32 s67, s55, 2\n"
"s_add_u32 s67, s67, s54\n"
"s_lshl_b32 s68, s67, 16\n"
"v_lshl_add_u32 v2, v2, 11, s68\n"
"v_add_u32 v8, v2, v3\n"
"v_xor_b32 v4, 64, v8\n"
"v_add_u32 v9, 31744, v4\n"
"v_add_u32 v10, 6144, v8\n"
"v_add_u32 v11, 37888, v4\n"
"v_lshrrev_b32 v2, 3, v1\n"
"v_lshl_add_u32 v12, v2, 11, v3\n"
"v_xor_b32 v4, 64, v12\n"
"v_add_u32 v13, 15360, v4\n"
"s_mul_i32 s68, s67, 360448\n"
"v_add_u32 v200, s68, v7\n"
"v_lshrrev_b32 v2, 2, v1\n"
"v_mul_u32_u24 v2, 7168, v2\n"
"v_lshrrev_b32 v4, 5, v1\n"
"v_lshlrev_b32 v4, 1, v4\n"
"v_and_b32 v5, 3, v1\n"
"v_xor_b32 v4, v4, v5\n"
"v_lshl_add_u32 v201, v4, 4, v2\n"
"v_add_u32 v205, s65, v7\n"
"v_and_b32 v205, 2047, v205\n"
"v_xor_b32 v206, 1024, v205\n"
"v_and_b32 v209, 15, v1\n"
"v_lshrrev_b32 v210, 4, v1\n"
"s_lshr_b32 s92, s56, 1\n"
"s_lshl_b32 s92, s92, 2\n"
"s_and_b32 s93, s56, 1\n"
"s_add_u32 s92, s92, s93\n"
"s_lshl_b32 s92, s92, 5\n"
"v_and_b32 v2, 32, v1\n"
"v_add3_u32 v2, v2, v1, s92\n"
"v_lshlrev_b32 v208, 1, v2\n"
"v_cmp_eq_u32 vcc, 3, v210\n"
"s_lshr_b32 s68, s56, 1\n"
"v_mov_b32 v3, s68\n"
"v_lshl_add_u32 v2, s93, 3, v3\n"
"v_cndmask_b32 v2, v2, v3, vcc\n"
"v_lshl_add_u32 v2, v210, 4, v2\n"
"v_mul_u32_u24 v211, 37, v2\n"
"v_lshrrev_b32 v211, 8, v211\n"
"v_mul_u32_u24 v3, 7, v211\n"
"v_sub_u32 v2, v2, v3\n"
"s_lshl_b32 s68, s93, 3\n"
"v_and_or_b32 v3, v1, 7, s68\n"
"v_cndmask_b32 v3, v209, v3, vcc\n"
"v_lshlrev_b32 v3, 3, v3\n"
"v_lshl_add_u32 v207, v2, 10, v3\n"
"s_lshl_b32 s92, s92, 11\n"
"s_mul_i32 s93, s54, 114688\n"
"s_lshl_b32 s88, s55, 18\n"
"s_mov_b32 s89, 40370176\n"
"s_mul_i32 s90, s55, 1441792\n"
"s_lshl_b32 s84, s58, 20\n"
"s_mul_i32 s85, s58, 1572864\n"
"s_add_u32 s85, s85, 25165824\n"
"s_waitcnt lgkmcnt(0)\n"
"s_load_dword s0, s[42:43], 0x0\n"
"s_load_dword s0, s[44:45], 0x0\n"
"s_load_dword s0, s[46:47], 0x0\n"
"s_load_dword s0, s[48:49], 0x0\n"
"s_load_dword s0, s[40:41], 0x0\n"
"s_load_dword s0, s[4:5], s88\n"
"s_load_dword s0, s[8:9], s66\n"
"s_load_dword s0, s[36:37], s89\n"
"s_lshr_b32 s91, s8, 7\n"
"s_sub_u32 s91, 3, s91\n"
"v_add_u32 v211, s91, v211\n"
"v_and_b32 v211, 7, v211\n"
"v_lshl_add_u32 v207, v211, 7, v207\n"
"global_load_dwordx2 v[212:213], v207, s[42:43]\n"
"global_load_ushort v216, v208, s[44:45]\n"
"global_load_ushort v217, v208, s[46:47]\n"
"global_load_ushort v218, v208, s[48:49]\n"
"v_readlane_b32 s68, v207, 0\n"
"v_readlane_b32 s1, v207, 16\n"
"s_add_u32 s93, s93, s68\n"
"s_sub_u32 s1, s1, s68\n"
"s_mov_b32 m0, 147456\n"
"s_nop 0\n"
"global_load_lds_dwordx4 v205, s[40:41]\n"
"s_mov_b32 m0, 148480\n"
"s_nop 0\n"
"global_load_lds_dwordx4 v206, s[40:41]\n"
"s_mov_b32 s42, s4\n"
"s_and_b32 s68, s4, 0x7fffffff\n"
"s_and_b32 s100, s4, 0x80000000\n"
"s_add_u32 s6, s6, s68\n"
"s_add_u32 s4, s100, s65\n"
"s_add_u32 m0, s63, 114688\n"
"v_add_u32 v8, s68, v8\n"
"buffer_load_dwordx4 v8, s[4:7], 0 offen offset:0 lds\n"
"v_add_u32 v9, s68, v9\n"
"buffer_load_dwordx4 v9, s[4:7], 0 offen offset:1024 lds\n"
"v_add_u32 v10, s68, v10\n"
"buffer_load_dwordx4 v10, s[4:7], 0 offen offset:2048 lds\n"
"v_add_u32 v11, s68, v11\n"
"buffer_load_dwordx4 v11, s[4:7], 0 offen offset:3072 lds\n"
"s_sub_u32 s8, s8, 4096\n"
"s_subb_u32 s9, s9, 0\n"
"s_and_b32 s68, s8, 0x7fffffff\n"
"s_and_b32 s101, s8, 0x80000000\n"
"s_add_u32 s10, s10, s68\n"
"s_add_u32 s68, s68, 4032\n"
"s_add_u32 s101, s101, s93\n"
"s_add_u32 s101, s101, 64\n"
"v_add_u32 v201, s68, v201\n"
"s_add_u32 s8, s101, s66\n"
"s_add_u32 m0, s64, 0\n"
"s_nop 0\n"
"buffer_load_dwordx4 v201, s[8:11], 0 offen offset:0 lds\n"
"v_subrev_u32 v202, 960, v201\n"
"buffer_load_dwordx4 v202, s[8:11], 0 offen offset:1024 lds\n"
"s_add_u32 s8, s8, s1\n"
"s_add_u32 m0, s64, 8192\n"
"s_nop 0\n"
"buffer_load_dwordx4 v201, s[8:11], 0 offen offset:0 lds\n"
"s_bitcmp1_b32 s54, 0\n"
"s_cselect_b64 s[12:13], s[16:17], s[12:13]\n"
"s_sub_u32 s12, s12, 2048\n"
"s_subb_u32 s13, s13, 0\n"
"s_add_u32 s14, s14, 2048\n"
"s_and_b32 s68, s12, 0x7fffffff\n"
"s_and_b32 s3, s12, 0x80000000\n"
"s_add_u32 s14, s14, s68\n"
"s_add_u32 s68, s68, s92\n"
"v_add_u32 v14, s68, v12\n"
"v_add_u32 v15, s68, v13\n"
"buffer_load_dwordx4 v202, s[8:11], 0 offen offset:1024 lds\n"
"v_readlane_b32 s92, v207, 32\n"
"v_readlane_b32 s68, v207, 16\n"
"s_nop 0\n"
"s_sub_u32 s92, s92, s68\n"
"s_add_u32 s8, s8, s92\n"
"s_add_u32 m0, s64, 16384\n"
"s_nop 0\n"
"buffer_load_dwordx4 v201, s[8:11], 0 offen offset:0 lds\n"
"buffer_load_dwordx4 v202, s[8:11], 0 offen offset:1024 lds\n"
"s_mov_b32 s68, 40370176\n"
"buffer_load_dword v219, off, s[36:39], s68 sc1\n"
"v_lshrrev_b32 v220, 5, v1\n"
"v_lshlrev_b32 v220, 30, v220\n"
"v_lshl_add_u32 v220, v1, 2, v220\n"
"v_add_u32 v203, 112640, v201\n"
"v_add_u32 v204, 112640, v202\n"
"v_lshrrev_b32 v2, 1, v209\n"
"v_xor_b32 v2, v2, v210\n"
"v_lshlrev_b32 v2, 4, v2\n"
"v_lshl_add_u32 v221, v209, 7, v2\n"
"v_xor_b32 v222, 64, v221\n"
"v_add_u32 v223, 65536, v221\n"
"v_add_u32 v224, 65536, v222\n"
"s_add_u32 s68, s63, 114688\n"
"v_add_u32 v225, s68, v221\n"
"v_add_u32 v226, s68, v222\n"
"v_lshrrev_b32 v2, 5, v1\n"
"v_lshlrev_b32 v2, 11, v2\n"
"v_and_b32 v3, 1, v210\n"
"v_mul_u32_u24 v3, 544, v3\n"
"v_bfe_u32 v4, v1, 2, 2\n"
"v_lshlrev_b32 v4, 6, v4\n"
"v_add3_u32 v2, v2, v3, v4\n"
"v_lshl_add_u32 v227, v5, 3, v2\n"
"s_and_b32 s68, s54, 1\n"
"v_lshl_add_u32 v227, s68, 10, v227\n"
"v_xor_b32 v228, 32, v227\n"
"v_add_u32 v229, 65536, v227\n"
"v_add_u32 v230, 65536, v228\n"
"v_lshlrev_b32 v231, 4, v210\n"
"v_add_u32 v231, 147456, v231\n"
"s_sub_u32 s20, s20, 4096\n"
"s_subb_u32 s21, s21, 0\n"
"s_add_u32 s22, s22, 4096\n"
"s_bitcmp1_b32 s54, 0\n"
"s_cselect_b64 s[24:25], s[28:29], s[24:25]\n"
"s_mov_b64 s[28:29], s[36:37]\n"
"s_mov_b32 s30, s38\n"
"s_add_u32 s65, s65, 128\n"
"s_and_b32 s65, s65, 2047\n"
"s_mul_i32 s66, s65, 3584\n"
"s_add_u32 s4, s100, s65\n"
"s_add_u32 m0, s63, 131072\n"
"s_nop 0\n"
"buffer_load_dwordx4 v8, s[4:7], 0 offen offset:0 lds\n"
"s_and_b32 s84, s20, 0x7fffffff\n"
"s_and_b32 s40, s20, 0x80000000\n"
"s_add_u32 s22, s22, s84\n"
"s_and_b32 s85, s24, 0x7fffffff\n"
"s_and_b32 s41, s24, 0x80000000\n"
"s_add_u32 s26, s26, s85\n"
"s_and_b32 s86, s28, 0x7fffffff\n"
"s_and_b32 s87, s28, 0x80000000\n"
"s_add_u32 s30, s30, s86\n"
"v_add_u32 v12, s85, v12\n"
"v_add_u32 v13, s85, v13\n"
"s_mul_i32 s88, s58, 1\n"
"s_mul_i32 s89, s59, 0\n"
"s_add_u32 s88, s88, s89\n"
"s_lshl_b32 s89, s57, 2\n"
"s_lshl_b32 s90, s57, 1\n"
"s_and_b32 s88, s58, 3\n"
"s_mul_i32 s88, s88, 3\n"
"s_lshr_b32 s89, s58, 2\n"
"s_add_u32 s88, s88, s89\n"
"v_add_u32 v16, s88, v1\n"
"v_subrev_u32 v17, 12, v16\n"
"v_min_u32 v17, v16, v17\n"
"v_cmp_le_u32 vcc, 12, v1\n"
"s_nop 1\n"
"v_cndmask_b32 v19, v17, v1, vcc\n"
"v_cndmask_b32 v16, v17, v16, vcc\n"
"v_bfe_u32 v17, v16, 1, 1\n"
"v_and_b32 v18, 1, v16\n"
"v_lshlrev_b32 v18, 3, v18\n"
"v_add3_u32 v17, v17, v18, s90\n"
"v_and_or_b32 v16, v16, 1, s90\n"
"v_cndmask_b32 v16, v17, v16, vcc\n"
"v_bfe_u32 v17, v19, 2, 2\n"
"v_lshl_add_u32 v16, v17, 4, v16\n"
"v_mul_u32_u24 v17, 37, v16\n"
"v_lshrrev_b32 v17, 8, v17\n"
"v_mul_u32_u24 v18, 7, v17\n"
"v_sub_u32 v16, v16, v18\n"
"v_add_u32 v17, s91, v17\n"
"v_and_b32 v17, 7, v17\n"
"v_lshl_add_u32 v16, v16, 3, v17\n"
"v_lshl_add_u32 v16, v16, 12, s87\n"
"s_nop 0\n"
"v_readlane_b32 s70, v16, 0\n"
"v_readlane_b32 s71, v16, 1\n"
"buffer_load_dwordx4 v9, s[4:7], 0 offen offset:1024 lds\n"
"v_readlane_b32 s72, v16, 2\n"
"v_readlane_b32 s73, v16, 3\n"
"v_readlane_b32 s74, v16, 4\n"
"v_readlane_b32 s75, v16, 5\n"
"v_readlane_b32 s76, v16, 6\n"
"v_readlane_b32 s77, v16, 7\n"
"v_readlane_b32 s78, v16, 8\n"
"v_readlane_b32 s79, v16, 9\n"
"v_readlane_b32 s80, v16, 10\n"
"v_readlane_b32 s81, v16, 11\n"
"v_readlane_b32 s82, v16, 12\n"
"v_readlane_b32 s83, v16, 13\n"
"v_lshrrev_b32 v236, 2, v1\n"
"v_lshlrev_b32 v236, 11, v236\n"
"v_and_b32 v237, 1, v210\n"
"v_lshlrev_b32 v237, 1, v237\n"
"v_xor_b32 v237, v237, v1\n"
"v_and_b32 v237, 3, v237\n"
"v_lshl_add_u32 v236, v237, 4, v236\n"
"s_lshl_b32 s89, s54, 15\n"
"s_lshl_b32 s90, s59, 8\n"
"s_add_u32 s89, s89, s90\n"
"s_add_u32 s89, s89, s84\n"
"s_add_u32 s88, s57, 1\n"
"s_and_b32 s88, s88, 3\n"
"s_lshl_b32 s88, s88, 6\n"
"s_add_u32 s88, s88, s89\n"
"s_add_u32 s88, s88, 4096\n"
"v_add_u32 v238, s88, v236\n"
"s_add_u32 s88, s57, 2\n"
"s_and_b32 s88, s88, 3\n"
"buffer_load_dwordx4 v10, s[4:7], 0 offen offset:2048 lds\n"
"s_lshl_b32 s88, s88, 6\n"
"s_add_u32 s88, s88, s89\n"
"s_add_u32 s88, s88, 3072\n"
"v_add_u32 v239, s88, v236\n"
"s_add_u32 s88, s57, 3\n"
"s_and_b32 s88, s88, 3\n"
"s_lshl_b32 s88, s88, 6\n"
"s_add_u32 s88, s88, s89\n"
"s_add_u32 s88, s88, 2048\n"
"v_add_u32 v240, s88, v236\n"
"s_add_u32 s88, s57, 4\n"
"s_and_b32 s88, s88, 3\n"
"s_lshl_b32 s88, s88, 6\n"
"s_add_u32 s88, s88, s89\n"
"s_add_u32 s88, s88, 1024\n"
"v_add_u32 v241, s88, v236\n"
"v_lshrrev_b32 v236, 2, v1\n"
"v_lshlrev_b32 v236, 6, v236\n"
"v_and_b32 v237, 1, v210\n"
"v_lshl_add_u32 v236, v237, 5, v236\n"
"v_and_b32 v237, 3, v1\n"
"v_lshl_add_u32 v242, v237, 3, v236\n"
"v_xor_b32 v243, 32, v242\n"
"v_add_u32 v244, 65536, v242\n"
"v_add_u32 v245, 65536, v243\n"
"s_lshl_b32 s88, s58, 2\n"
"s_add_u32 s88, s88, s54\n"
"s_mul_i32 s88, s88, 720896\n"
"s_add_u32 s88, s88, s86\n"
"v_add_u32 v246, s88, v7\n"
"v_add_u32 v247, 360448, v246\n"
"v_bfe_u32 v233, v1, 3, 2\n"
"v_lshrrev_b32 v236, 5, v1\n"
"v_lshl_add_u32 v233, v236, 4, v233\n"
"v_lshlrev_b32 v233, 7, v233\n"
"s_add_u32 s88, s63, 114688\n"
"v_lshlrev_b32 v234, 9, v210\n"
"v_lshl_add_u32 v234, v209, 3, v234\n"
"v_add_u32 v234, s88, v234\n"
"v_lshlrev_b32 v235, 4, v209\n"
"v_add_u32 v235, 150528, v235\n"
"s_lshl_b32 s88, s2, 2\n"
"s_add_u32 s88, s88, s54\n"
"buffer_load_dwordx4 v11, s[4:7], 0 offen offset:3072 lds\n"
"s_mul_i32 s89, s88, 7168\n"
"s_add_u32 s89, s89, s84\n"
"s_add_u32 s89, s89, 4096\n"
"s_lshr_b32 s88, s88, 1\n"
"s_lshl_b32 s88, s88, 12\n"
"s_add_u32 s88, s88, s85\n"
"v_min_u32 v236, 55, v1\n"
"v_lshl_add_u32 v44, v236, 7, s89\n"
"v_lshrrev_b32 v236, 1, v1\n"
"s_bfe_u32 s19, s2, 65539\n"
"v_lshl_add_u32 v45, v236, 7, s88\n"
"s_mov_b32 s20, s40\n"
"s_mov_b32 s24, s41\n"
"s_add_u32 s8, s101, s66\n"
"s_add_u32 m0, s64, 24576\n"
"s_nop 0\n"
"buffer_load_dwordx4 v201, s[8:11], 0 offen offset:0 lds\n"
"v_add_u32 v2, 147456, v7\n"
"s_waitcnt vmcnt(21)\n"
"v_and_b32 v215, 0xffff0000, v213\n"
"v_lshlrev_b32 v214, 16, v213\n"
"v_and_b32 v213, 0xffff0000, v212\n"
"v_lshlrev_b32 v212, 16, v212\n"
"ds_write_b128 v2, v[212:215] offset:2048\n"
"s_and_b32 s16, s54, 1\n"
"v_lshl_add_u32 v152, s16, 7, v231\n"
"ds_read_b128 v[68:71], v152 offset:2048\n"
"ds_read_b128 v[76:79], v152 offset:2112\n"
"ds_read_b128 v[100:103], v152 offset:2304\n"
"ds_read_b128 v[108:111], v152 offset:2368\n"
"ds_read_b128 v[132:135], v152 offset:2560\n"
"ds_read_b128 v[140:143], v152 offset:2624\n"
"s_waitcnt vmcnt(20)\n"
"v_lshlrev_b32 v216, 16, v216\n"
"s_waitcnt vmcnt(19)\n"
"v_lshlrev_b32 v217, 16, v217\n"
"s_waitcnt vmcnt(18)\n"
"v_lshlrev_b32 v218, 16, v218\n"
"v_mul_f32 v218, 0x3fb8aa3b, v218\n"
"v_exp_f32 v218, v218\n"
"s_nop 0\n"
"v_cvt_pk_bf16_f32 v218, v218, v218\n"
"v_lshlrev_b32 v218, 16, v218\n"
"v_mul_f32 v25, v218, v216\n"
"v_mul_f32 v21, v218, v217\n"
"v_mul_f32 v25, 0x3fb8aa3b, v25\n"
"v_exp_f32 v25, v25\n"
"v_mul_f32 v20, 0x3f22f983, v21\n"
"v_rndne_f32 v20, v20\n"
"v_fmamk_f32 v21, v20, 0xbfc90fdb, v21\n"
"v_fmamk_f32 v21, v20, 0x333bbd2e, v21\n"
"v_cvt_i32_f32 v26, v20\n"
"v_mul_f32 v22, v21, v21\n"
"v_mov_b32 v23, 0x3638ef1d\n"
"v_fmaak_f32 v23, v23, v22, 0xb9500d01\n"
"v_fmaak_f32 v23, v23, v22, 0x3c088888\n"
"v_fmaak_f32 v23, v23, v22, 0xbe2aaaab\n"
"v_mul_f32 v27, v21, v22\n"
"v_fma_f32 v23, v23, v27, v21\n"
"v_mov_b32 v24, 0xb493f27e\n"
"buffer_load_dwordx4 v202, s[8:11], 0 offen offset:1024 lds\n"
"v_fmaak_f32 v24, v24, v22, 0x37d00d01\n"
"v_fmaak_f32 v24, v24, v22, 0xbab60b61\n"
"v_fmaak_f32 v24, v24, v22, 0x3d2aaaab\n"
"v_fmaak_f32 v24, v24, v22, 0xbf000000\n"
"v_fma_f32 v24, v24, v22, 1.0\n"
"v_and_b32 v27, 1, v26\n"
"v_cmp_eq_u32 vcc, 1, v27\n"
"v_lshlrev_b32 v27, 30, v26\n"
"v_add_u32 v28, 0x40000000, v27\n"
"v_and_b32 v27, 0x80000000, v27\n"
"v_and_b32 v28, 0x80000000, v28\n"
"v_cndmask_b32 v17, v23, v24, vcc\n"
"v_cndmask_b32 v16, v24, v23, vcc\n"
"v_xor_b32 v17, v27, v17\n"
"v_xor_b32 v16, v28, v16\n"
"v_mul_f32 v16, v25, v16\n"
"v_mul_f32 v17, v25, v17\n"
"v_mul_f32 v27, v216, v216\n"
"v_fmac_f32 v27, v217, v217\n"
"v_rcp_f32 v27, v27\n"
"v_add_f32 v28, -1.0, v16\n"
"v_add_f32 v27, v27, v27\n"
"v_mul_f32 v18, v28, v216\n"
"v_mul_f32 v19, v17, v216\n"
"v_fmac_f32 v18, v17, v217\n"
"v_fma_f32 v19, -v28, v217, v19\n"
"v_mul_f32 v18, v27, v18\n"
"v_mul_f32 v19, v27, v19\n"
"v_add_u32 v46, 147456, v7\n"
"ds_write_b128 v46, v[16:19] offset:3072\n"
"v_mul_f32 v20, v16, v16\n"
"v_mul_f32 v21, v16, v17\n"
"v_fma_f32 v20, -v17, v17, v20\n"
"v_add_f32 v21, v21, v21\n"
"v_mul_f32 v22, v20, v20\n"
"v_mul_f32 v23, v20, v21\n"
"v_fma_f32 v22, -v21, v21, v22\n"
"v_add_f32 v23, v23, v23\n"
"v_mul_f32 v24, v22, v22\n"
"v_mul_f32 v25, v22, v23\n"
"v_fma_f32 v24, -v23, v23, v24\n"
"v_add_f32 v25, v25, v25\n"
"v_mul_f32 v20, v24, v24\n"
"s_add_u32 s8, s8, s1\n"
"s_add_u32 m0, s64, 32768\n"
"s_nop 0\n"
"buffer_load_dwordx4 v201, s[8:11], 0 offen offset:0 lds\n"
"v_mul_f32 v21, v24, v25\n"
"v_fma_f32 v20, -v25, v25, v20\n"
"v_add_f32 v21, v21, v21\n"
"v_mul_f32 v26, v20, v20\n"
"v_mul_f32 v27, v20, v21\n"
"v_fma_f32 v26, -v21, v21, v26\n"
"v_add_f32 v27, v27, v27\n"
"ds_write_b128 v46, v[24:27] offset:4096\n"
"v_mul_f32 v28, v26, v26\n"
"v_mul_f32 v29, v26, v27\n"
"v_fma_f32 v28, -v27, v27, v28\n"
"v_add_f32 v29, v29, v29\n"
"v_mul_f32 v30, v28, v28\n"
"v_mul_f32 v31, v28, v29\n"
"v_fma_f32 v30, -v29, v29, v30\n"
"v_add_f32 v31, v31, v31\n"
"ds_write_b64 v46, v[30:31] offset:5120\n"
"v_mul_f32 v20, v30, v30\n"
"v_mul_f32 v21, v30, v31\n"
"v_fma_f32 v20, -v31, v31, v20\n"
"v_add_f32 v21, v21, v21\n"
"v_mul_f32 v32, v20, v20\n"
"v_mul_f32 v33, v20, v21\n"
"v_fma_f32 v32, -v21, v21, v32\n"
"v_add_f32 v33, v33, v33\n"
"v_mul_f32 v34, v32, v32\n"
"v_mul_f32 v35, v32, v33\n"
"v_fma_f32 v34, -v33, v33, v34\n"
"v_add_f32 v35, v35, v35\n"
"s_bitcmp1_b32 s54, 0\n"
"s_cselect_b64 s[94:95], -1, 0\n"
"s_bitcmp1_b32 s54, 1\n"
"s_cselect_b64 s[96:97], -1, 0\n"
"v_cndmask_b32 v40, 1.0, v26, s[94:95]\n"
"v_cndmask_b32 v41, 0, v27, s[94:95]\n"
"v_cndmask_b32 v42, 1.0, v28, s[96:97]\n"
"v_cndmask_b32 v43, 0, v29, s[96:97]\n"
"v_mul_f32 v36, v40, v42\n"
"v_mul_f32 v37, v40, v43\n"
"v_fma_f32 v36, -v41, v43, v36\n"
"v_fmac_f32 v37, v41, v42\n"
"v_cndmask_b32 v40, 1.0, v32, s[94:95]\n"
"v_cndmask_b32 v41, 0, v33, s[94:95]\n"
"v_cndmask_b32 v42, 1.0, v34, s[96:97]\n"
"v_cndmask_b32 v43, 0, v35, s[96:97]\n"
"buffer_load_dwordx4 v202, s[8:11], 0 offen offset:1024 lds\n"
"v_mul_f32 v38, v40, v42\n"
"v_mul_f32 v39, v40, v43\n"
"v_fma_f32 v38, -v41, v43, v38\n"
"v_fmac_f32 v39, v41, v42\n"
"v_lshl_add_u32 v46, s54, 10, v46\n"
"ds_write_b128 v46, v[36:39] offset:6144\n"
"s_add_u32 s8, s8, s92\n"
"s_add_u32 m0, s64, 40960\n"
"s_nop 0\n"
"buffer_load_dwordx4 v201, s[8:11], 0 offen offset:0 lds\n"
"buffer_load_dwordx4 v202, s[8:11], 0 offen offset:1024 lds\n"
"s_mov_b64 exec, 0\n"
"s_add_u32 s17, s54, 1\n"
"s_mov_b32 s16, s54\n"
".Lk_hend:\n"
"s_cmp_eq_u32 s16, s17\n"
"s_cbranch_scc1 .Lk_hdone\n"
"s_and_b32 s18, s16, 3\n"
"s_add_u32 s16, s16, 1\n"
"s_cmp_eq_u32 s18, 0\n"
"s_cbranch_scc1 .Lk_h0\n"
"s_cmp_eq_u32 s18, 1\n"
"s_cbranch_scc1 .Lk_h1\n"
"s_cmp_eq_u32 s18, 2\n"
"s_cbranch_scc1 .Lk_h2\n"
"s_branch .Lk_h3\n"
".Lk_hdone:\n"
"s_mov_b64 exec, -1\n"
"v_xor_b32 v32, 4096, v225\n"
"v_xor_b32 v33, 4096, v226\n"
"s_sub_u32 s16, s22, 7340032\n"
"v_mov_b32 v43, s16\n"
"s_waitcnt vmcnt(10)\n"
"v_readfirstlane_b32 s61, v219\n"
"s_nop 0\n"
"s_mul_i32 s61, s61, 0x9E3779B1\n"
"s_add_u32 s61, s61, 0x7F4A7C15\n"
"v_mov_b32 v219, s61\n"
"s_barrier\n"
"ds_read_b128 a[0:3], v225 offset:0\n"
"ds_read_b128 a[8:11], v226 offset:0\n"
"ds_read_b128 a[4:7], v225 offset:2048\n"
"ds_read_b128 a[12:15], v226 offset:2048\n"
"ds_read_b128 v[16:19], v32 offset:0\n"
"ds_read_b128 v[24:27], v33 offset:0\n"
"ds_read_b128 v[20:23], v32 offset:2048\n"
"ds_read_b128 v[28:31], v33 offset:2048\n"
"ds_read_b64_tr_b16 v[152:153], v227 offset:0\n"
"ds_read_b64_tr_b16 v[154:155], v227 offset:256\n"
"ds_read_b64_tr_b16 v[156:157], v227 offset:4096\n"
"ds_read_b64_tr_b16 v[158:159], v227 offset:4352\n"
"ds_read_b64_tr_b16 v[160:161], v228 offset:0\n"
"ds_read_b64_tr_b16 v[162:163], v228 offset:256\n"
"ds_read_b64_tr_b16 v[164:165], v228 offset:4096\n"
"ds_read_b64_tr_b16 v[166:167], v228 offset:4352\n"
"s_waitcnt lgkmcnt(4)\n"
"v_mfma_f32_16x16x32_bf16 v[48:51], v[152:155], a[0:3], v[68:71]\n"
"ds_read_b64_tr_b16 v[168:169], v227 offset:8192\n"
"s_add_u32 s65, s65, 128\n"
"v_mfma_f32_16x16x32_bf16 v[48:51], v[156:159], a[8:11], v[48:51]\n"
"ds_read_b64_tr_b16 v[170:171], v227 offset:8448\n"
"s_and_b32 s65, s65, 2047\n"
"v_mfma_f32_16x16x32_bf16 v[52:55], v[152:155], a[4:7], v[68:71]\n"
"ds_read_b64 v[250:251], v235 offset:0\n"
"s_mul_i32 s66, s65, 3584\n"
"v_mfma_f32_16x16x32_bf16 v[52:55], v[156:159], a[12:15], v[52:55]\n"
"ds_read_b64 v[236:237], v235 offset:8\n"
"s_add_u32 s8, s101, s66\n"
"v_mfma_f32_16x16x32_bf16 v[64:67], v[152:155], v[16:19], v[68:71]\n"
"ds_read_b64_tr_b16 v[172:173], v227 offset:12288\n"
"s_add_u32 m0, s64, 49152\n"
"v_mfma_f32_16x16x32_bf16 v[64:67], v[156:159], v[24:27], v[64:67]\n"
"ds_read_b64 v[252:253], v235 offset:256\n"
"buffer_load_dwordx4 v201, s[8:11], 0 offen offset:0 lds\n"
"v_mfma_f32_16x16x32_bf16 v[68:71], v[152:155], v[20:23], v[68:71]\n"
"ds_read_b64_tr_b16 v[174:175], v227 offset:12544\n"
"s_lshl_b32 s43, s55, 2\n"
"v_mfma_f32_16x16x32_bf16 v[68:71], v[156:159], v[28:31], v[68:71]\n"
"ds_read_b64_tr_b16 v[176:177], v228 offset:8192\n"
"s_waitcnt lgkmcnt(8)\n"
"v_mfma_f32_16x16x32_bf16 v[56:59], v[160:163], a[0:3], v[76:79]\n"
"ds_read_b64 v[254:255], v235 offset:512\n"
"s_add_u32 s43, s43, s54\n"
"v_mfma_f32_16x16x32_bf16 v[56:59], v[164:167], a[8:11], v[56:59]\n"
"ds_read_b64_tr_b16 v[178:179], v228 offset:8448\n"
"buffer_load_dwordx4 v202, s[8:11], 0 offen offset:1024 lds\n"
"v_mfma_f32_16x16x32_bf16 v[60:63], v[160:163], a[4:7], v[76:79]\n"
"ds_read_b64_tr_b16 v[180:181], v228 offset:12288\n"
"s_add_u32 s8, s8, s1\n"
"v_mfma_f32_16x16x32_bf16 v[60:63], v[164:167], a[12:15], v[60:63]\n"
"ds_read_b64 v[4:5], v235 offset:768\n"
"s_add_u32 m0, s64, 57344\n"
"v_mfma_f32_16x16x32_bf16 v[72:75], v[160:163], v[16:19], v[76:79]\n"
"ds_read_b64_tr_b16 v[182:183], v228 offset:12544\n"
"s_lshl_b32 s43, s43, 4\n"
"v_mfma_f32_16x16x32_bf16 v[72:75], v[164:167], v[24:27], v[72:75]\n"
"buffer_load_dwordx4 v201, s[8:11], 0 offen offset:0 lds\n"
"s_add_u32 s43, s43, s56\n"
"v_mfma_f32_16x16x32_bf16 v[76:79], v[160:163], v[20:23], v[76:79]\n"
"s_lshl_b32 s43, s43, 7\n"
"s_add_u32 s43, s43, 39190528\n"
"v_mfma_f32_16x16x32_bf16 v[76:79], v[164:167], v[28:31], v[76:79]\n"
"ds_read_b64_tr_b16 v[152:153], v227 offset:16384\n"
"s_waitcnt lgkmcnt(7)\n"
"v_mfma_f32_16x16x32_bf16 v[80:83], v[168:171], a[0:3], v[100:103]\n"
"ds_read_b64_tr_b16 v[154:155], v227 offset:16640\n"
"s_barrier\n"
"v_mfma_f32_16x16x32_bf16 v[80:83], v[172:175], a[8:11], v[80:83]\n"
"ds_read_b64_tr_b16 v[156:157], v227 offset:20480\n"
"buffer_load_dwordx4 v202, s[8:11], 0 offen offset:1024 lds\n"
"v_mfma_f32_16x16x32_bf16 v[84:87], v[168:171], a[4:7], v[100:103]\n"
"ds_read_b64_tr_b16 v[158:159], v227 offset:20736\n"
"s_add_u32 s8, s8, s92\n"
"v_mfma_f32_16x16x32_bf16 v[84:87], v[172:175], a[12:15], v[84:87]\n"
"s_add_u32 m0, s64, 65536\n"
"s_lshl_b32 s67, s58, 2\n"
"v_mfma_f32_16x16x32_bf16 v[96:99], v[168:171], v[16:19], v[100:103]\n"
"s_add_u32 s67, s67, s54\n"
"s_lshl_b32 s67, s67, 3\n"
"v_mfma_f32_16x16x32_bf16 v[96:99], v[172:175], v[24:27], v[96:99]\n"
"buffer_load_dwordx4 v201, s[8:11], 0 offen offset:0 lds\n"
"s_add_u32 s67, s67, s57\n"
"v_mfma_f32_16x16x32_bf16 v[100:103], v[168:171], v[20:23], v[100:103]\n"
"s_lshl_b32 s67, s67, 9\n"
"s_add_u32 s67, s67, 39190528\n"
"v_mfma_f32_16x16x32_bf16 v[100:103], v[172:175], v[28:31], v[100:103]\n"
"ds_read_b64_tr_b16 v[160:161], v228 offset:16384\n"
"s_waitcnt lgkmcnt(5)\n"
"s_nop 4\n"
"v_mfma_f32_16x16x32_bf16 v[88:91], v[176:179], a[0:3], v[108:111]\n"
"ds_read_b64_tr_b16 v[162:163], v228 offset:16640\n"
"s_lshr_b32 s88, s54, 1\n"
"v_mfma_f32_16x16x32_bf16 v[88:91], v[180:183], a[8:11], v[88:91]\n"
"ds_read_b64_tr_b16 v[164:165], v228 offset:20480\n"
"buffer_load_dwordx4 v202, s[8:11], 0 offen offset:1024 lds\n"
"s_nop 4\n"
"v_mfma_f32_16x16x32_bf16 v[92:95], v[176:179], a[4:7], v[108:111]\n"
"ds_read_b64_tr_b16 v[166:167], v228 offset:20736\n"
"s_add_u32 s4, s100, s65\n"
"v_mfma_f32_16x16x32_bf16 v[92:95], v[180:183], a[12:15], v[92:95]\n"
"s_waitcnt vmcnt(6)\n"
"s_barrier\n"
"s_nop 4\n"
"v_mfma_f32_16x16x32_bf16 v[104:107], v[176:179], v[16:19], v[108:111]\n"
"ds_read_b128 a[16:19], v225 offset:16384\n"
"s_add_u32 m0, s63, 114688\n"
"v_mfma_f32_16x16x32_bf16 v[104:107], v[180:183], v[24:27], v[104:107]\n"
"ds_read_b128 a[24:27], v226 offset:16384\n"
"buffer_load_dwordx4 v8, s[4:7], 0 offen offset:0 lds\n"
"s_nop 4\n"
"v_mfma_f32_16x16x32_bf16 v[108:111], v[176:179], v[20:23], v[108:111]\n"
"ds_read_b128 a[20:23], v225 offset:18432\n"
"s_add_u32 s88, s88, s60\n"
"v_mfma_f32_16x16x32_bf16 v[108:111], v[180:183], v[28:31], v[108:111]\n"
"ds_read_b128 a[28:31], v226 offset:18432\n"
"s_waitcnt lgkmcnt(8)\n"
"s_nop 4\n"
"v_mfma_f32_16x16x32_bf16 v[112:115], v[152:155], a[0:3], v[132:135]\n"
"ds_read_b128 v[184:187], v32 offset:16384\n"
"s_and_b32 s90, s54, 1\n"
"v_mfma_f32_16x16x32_bf16 v[112:115], v[156:159], a[8:11], v[112:115]\n"
"ds_read_b128 v[192:195], v33 offset:16384\n"
"buffer_load_dwordx4 v9, s[4:7], 0 offen offset:1024 lds\n"
"s_nop 4\n"
"v_mfma_f32_16x16x32_bf16 v[116:119], v[152:155], a[4:7], v[132:135]\n"
"ds_read_b128 v[188:191], v32 offset:18432\n"
"s_mul_i32 s90, s90, 229376\n"
"v_mfma_f32_16x16x32_bf16 v[116:119], v[156:159], a[12:15], v[116:119]\n"
"ds_read_b128 v[196:199], v33 offset:18432\n"
"ds_read_b64_tr_b16 v[168:169], v227 offset:24576\n"
"s_nop 4\n"
"v_mfma_f32_16x16x32_bf16 v[128:131], v[152:155], v[16:19], v[132:135]\n"
"ds_read_b64_tr_b16 v[170:171], v227 offset:24832\n"
"s_add_u32 s89, s88, 0\n"
"v_mfma_f32_16x16x32_bf16 v[128:131], v[156:159], v[24:27], v[128:131]\n"
"ds_read_b64_tr_b16 v[172:173], v227 offset:28672\n"
"buffer_load_dwordx4 v10, s[4:7], 0 offen offset:2048 lds\n"
"s_nop 4\n"
"v_mfma_f32_16x16x32_bf16 v[132:135], v[152:155], v[20:23], v[132:135]\n"
"ds_read_b64_tr_b16 v[174:175], v227 offset:28928\n"
"s_and_b32 s89, s89, 15\n"
"v_mfma_f32_16x16x32_bf16 v[132:135], v[156:159], v[28:31], v[132:135]\n"
".Lk_h0:\n"
"s_cbranch_execz .Lk_h4\n"
"ds_read_b64_tr_b16 v[176:177], v228 offset:24576\n"
"s_waitcnt lgkmcnt(13)\n"
"s_nop 4\n"
"v_mfma_f32_16x16x32_bf16 v[120:123], v[160:163], a[0:3], v[140:143]\n"
"ds_read_b64_tr_b16 v[178:179], v228 offset:24832\n"
"s_mul_i32 s89, s89, 458752\n"
"v_mfma_f32_16x16x32_bf16 v[120:123], v[164:167], a[8:11], v[120:123]\n"
"ds_read_b64_tr_b16 v[180:181], v228 offset:28672\n"
"buffer_load_dwordx4 v11, s[4:7], 0 offen offset:3072 lds\n"
"s_nop 4\n"
"v_mfma_f32_16x16x32_bf16 v[124:127], v[160:163], a[4:7], v[140:143]\n"
"ds_read_b64_tr_b16 v[182:183], v228 offset:28928\n"
"s_add_u32 s65, s65, 128\n"
"v_mfma_f32_16x16x32_bf16 v[124:127], v[164:167], a[12:15], v[124:127]\n"
"s_and_b32 s65, s65, 2047\n"
"s_mul_i32 s66, s65, 3584\n"
"s_nop 4\n"
"v_mfma_f32_16x16x32_bf16 v[136:139], v[160:163], v[16:19], v[140:143]\n"
"s_waitcnt lgkmcnt(8)\n"
"s_barrier\n"
"v_mfma_f32_16x16x32_bf16 v[136:139], v[164:167], v[24:27], v[136:139]\n"
"s_add_u32 s8, s101, s66\n"
"s_add_u32 m0, s64, 73728\n"
"s_nop 4\n"
"v_mfma_f32_16x16x32_bf16 v[140:143], v[160:163], v[20:23], v[140:143]\n"
"buffer_load_dwordx4 v201, s[8:11], 0 offen offset:0 lds\n"
"s_add_u32 s84, s89, s90\n"
"v_mfma_f32_16x16x32_bf16 v[140:143], v[164:167], v[28:31], v[140:143]\n"
"ds_read_b64_tr_b16 v[152:153], v227 offset:32768\n"
"s_waitcnt lgkmcnt(5)\n"
"s_nop 4\n"
"v_mfma_f32_16x16x32_bf16 v[48:51], v[168:171], a[16:19], v[48:51]\n"
"ds_read_b64_tr_b16 v[154:155], v227 offset:33024\n"
"s_add_u32 s89, s88, 2\n"
"v_mfma_f32_16x16x32_bf16 v[48:51], v[172:175], a[24:27], v[48:51]\n"
"ds_read_b64_tr_b16 v[156:157], v227 offset:36864\n"
"s_and_b32 s89, s89, 15\n"
"s_nop 4\n"
"v_mfma_f32_16x16x32_bf16 v[52:55], v[168:171], a[20:23], v[52:55]\n"
"ds_read_b64_tr_b16 v[158:159], v227 offset:37120\n"
"buffer_load_dwordx4 v202, s[8:11], 0 offen offset:1024 lds\n"
"v_mfma_f32_16x16x32_bf16 v[52:55], v[172:175], a[28:31], v[52:55]\n"
"s_add_u32 s8, s8, s1\n"
"s_add_u32 m0, s64, 81920\n"
"s_nop 4\n"
"v_mfma_f32_16x16x32_bf16 v[64:67], v[168:171], v[184:187], v[64:67]\n"
"s_mul_i32 s89, s89, 458752\n"
"s_add_u32 s85, s89, s90\n"
"v_mfma_f32_16x16x32_bf16 v[64:67], v[172:175], v[192:195], v[64:67]\n"
"s_add_u32 s89, s88, 4\n"
"s_and_b32 s89, s89, 15\n"
"s_nop 4\n"
"v_mfma_f32_16x16x32_bf16 v[68:71], v[168:171], v[188:191], v[68:71]\n"
"buffer_load_dwordx4 v201, s[8:11], 0 offen offset:0 lds\n"
"s_mul_i32 s89, s89, 458752\n"
"v_mfma_f32_16x16x32_bf16 v[68:71], v[172:175], v[196:199], v[68:71]\n"
"ds_read_b64_tr_b16 v[160:161], v228 offset:32768\n"
"s_waitcnt lgkmcnt(5)\n"
"s_nop 4\n"
"v_mfma_f32_16x16x32_bf16 v[56:59], v[176:179], a[16:19], v[56:59]\n"
"ds_read_b64_tr_b16 v[162:163], v228 offset:33024\n"
"s_add_u32 s86, s89, s90\n"
"v_mfma_f32_16x16x32_bf16 v[56:59], v[180:183], a[24:27], v[56:59]\n"
"ds_read_b64_tr_b16 v[164:165], v228 offset:36864\n"
"s_add_u32 s89, s88, 6\n"
"s_nop 4\n"
"v_mfma_f32_16x16x32_bf16 v[60:63], v[176:179], a[20:23], v[60:63]\n"
"ds_read_b64_tr_b16 v[166:167], v228 offset:37120\n"
"buffer_load_dwordx4 v202, s[8:11], 0 offen offset:1024 lds\n"
"v_mfma_f32_16x16x32_bf16 v[60:63], v[180:183], a[28:31], v[60:63]\n"
"s_add_u32 s8, s8, s92\n"
"s_add_u32 m0, s64, 90112\n"
"s_nop 4\n"
"v_mfma_f32_16x16x32_bf16 v[72:75], v[176:179], v[184:187], v[72:75]\n"
"s_and_b32 s89, s89, 15\n"
"s_mul_i32 s89, s89, 458752\n"
"v_mfma_f32_16x16x32_bf16 v[72:75], v[180:183], v[192:195], v[72:75]\n"
"s_add_u32 s87, s89, s90\n"
"s_add_u32 s89, s88, 8\n"
"s_nop 4\n"
"v_mfma_f32_16x16x32_bf16 v[76:79], v[176:179], v[188:191], v[76:79]\n"
"buffer_load_dwordx4 v201, s[8:11], 0 offen offset:0 lds\n"
"s_and_b32 s89, s89, 15\n"
"v_mfma_f32_16x16x32_bf16 v[76:79], v[180:183], v[196:199], v[76:79]\n"
"ds_read_b64_tr_b16 v[168:169], v227 offset:40960\n"
"s_waitcnt lgkmcnt(5)\n"
"s_nop 4\n"
"v_mfma_f32_16x16x32_bf16 v[80:83], v[152:155], a[16:19], v[80:83]\n"
"ds_read_b64_tr_b16 v[170:171], v227 offset:41216\n"
"s_mul_i32 s89, s89, 458752\n"
"v_mfma_f32_16x16x32_bf16 v[80:83], v[156:159], a[24:27], v[80:83]\n"
"ds_read_b64_tr_b16 v[172:173], v227 offset:45056\n"
"s_add_u32 s93, s89, s90\n"
"s_nop 4\n"
"v_mfma_f32_16x16x32_bf16 v[84:87], v[152:155], a[20:23], v[84:87]\n"
"ds_read_b64_tr_b16 v[174:175], v227 offset:45312\n"
"buffer_load_dwordx4 v202, s[8:11], 0 offen offset:1024 lds\n"
"v_mfma_f32_16x16x32_bf16 v[84:87], v[156:159], a[28:31], v[84:87]\n"
"s_add_u32 s4, s100, s65\n"
"s_add_u32 m0, s63, 131072\n"
"s_nop 4\n"
"v_mfma_f32_16x16x32_bf16 v[96:99], v[152:155], v[184:187], v[96:99]\n"
"s_add_u32 s89, s88, 10\n"
"s_and_b32 s89, s89, 15\n"
"v_mfma_f32_16x16x32_bf16 v[96:99], v[156:159], v[192:195], v[96:99]\n"
"s_mul_i32 s89, s89, 458752\n"
"s_add_u32 s68, s89, s90\n"
"s_nop 4\n"
"v_mfma_f32_16x16x32_bf16 v[100:103], v[152:155], v[188:191], v[100:103]\n"
"buffer_load_dwordx4 v8, s[4:7], 0 offen offset:0 lds\n"
"s_add_u32 s89, s88, 12\n"
"v_mfma_f32_16x16x32_bf16 v[100:103], v[156:159], v[196:199], v[100:103]\n"
"ds_read_b64_tr_b16 v[176:177], v228 offset:40960\n"
"s_waitcnt lgkmcnt(5)\n"
"s_nop 4\n"
"v_mfma_f32_16x16x32_bf16 v[88:91], v[160:163], a[16:19], v[88:91]\n"
"ds_read_b64_tr_b16 v[178:179], v228 offset:41216\n"
"s_and_b32 s89, s89, 15\n"
"v_mfma_f32_16x16x32_bf16 v[88:91], v[164:167], a[24:27], v[88:91]\n"
"ds_read_b64_tr_b16 v[180:181], v228 offset:45056\n"
"s_mul_i32 s89, s89, 458752\n"
"s_nop 4\n"
"v_mfma_f32_16x16x32_bf16 v[92:95], v[160:163], a[20:23], v[92:95]\n"
"ds_read_b64_tr_b16 v[182:183], v228 offset:45312\n"
"buffer_load_dwordx4 v9, s[4:7], 0 offen offset:1024 lds\n"
"v_mfma_f32_16x16x32_bf16 v[92:95], v[164:167], a[28:31], v[92:95]\n"
"s_waitcnt vmcnt(8)\n"
"s_barrier\n"
"s_nop 4\n"
"v_mfma_f32_16x16x32_bf16 v[104:107], v[160:163], v[184:187], v[104:107]\n"
"ds_read_b128 a[32:35], v225 offset:0\n"
"s_add_u32 s69, s89, s90\n"
"v_mfma_f32_16x16x32_bf16 v[104:107], v[164:167], v[192:195], v[104:107]\n"
"ds_read_b128 a[40:43], v226 offset:0\n"
"s_add_u32 s89, s88, 14\n"
"s_nop 4\n"
"v_mfma_f32_16x16x32_bf16 v[108:111], v[160:163], v[188:191], v[108:111]\n"
"ds_read_b128 a[36:39], v225 offset:2048\n"
"buffer_load_dwordx4 v10, s[4:7], 0 offen offset:2048 lds\n"
"v_mfma_f32_16x16x32_bf16 v[108:111], v[164:167], v[196:199], v[108:111]\n"
"ds_read_b128 a[44:47], v226 offset:2048\n"
"s_waitcnt lgkmcnt(8)\n"
"s_nop 4\n"
"v_mfma_f32_16x16x32_bf16 v[112:115], v[168:171], a[16:19], v[112:115]\n"
"ds_read_b128 v[16:19], v32 offset:0\n"
"s_and_b32 s89, s89, 15\n"
"v_mfma_f32_16x16x32_bf16 v[112:115], v[172:175], a[24:27], v[112:115]\n"
"ds_read_b128 v[24:27], v33 offset:0\n"
"s_mul_i32 s89, s89, 458752\n"
"s_nop 4\n"
"v_mfma_f32_16x16x32_bf16 v[116:119], v[168:171], a[20:23], v[116:119]\n"
"ds_read_b128 v[20:23], v32 offset:2048\n"
"buffer_load_dwordx4 v11, s[4:7], 0 offen offset:3072 lds\n"
"v_mfma_f32_16x16x32_bf16 v[116:119], v[172:175], a[28:31], v[116:119]\n"
"ds_read_b128 v[28:31], v33 offset:2048\n"
"s_add_u32 s65, s65, 128\n"
"s_nop 4\n"
"v_mfma_f32_16x16x32_bf16 v[128:131], v[168:171], v[184:187], v[128:131]\n"
"s_waitcnt lgkmcnt(0)\n"
"s_add_u32 s0, s89, s90\n"
"v_mfma_f32_16x16x32_bf16 v[128:131], v[172:175], v[192:195], v[128:131]\n"
"ds_read_b64_tr_b16 v[152:153], v227 offset:49152\n"
"s_and_b32 s65, s65, 2047\n"
"s_nop 4\n"
"v_mfma_f32_16x16x32_bf16 v[132:135], v[168:171], v[188:191], v[132:135]\n"
"ds_read_b64_tr_b16 v[154:155], v227 offset:49408\n"
"s_mul_i32 s66, s65, 3584\n"
"v_mfma_f32_16x16x32_bf16 v[132:135], v[172:175], v[196:199], v[132:135]\n"
"ds_read_b64_tr_b16 v[156:157], v227 offset:53248\n"
"s_add_u32 s8, s101, s66\n"
"s_nop 4\n"
"v_mfma_f32_16x16x32_bf16 v[120:123], v[176:179], a[16:19], v[120:123]\n"
"ds_read_b64_tr_b16 v[158:159], v227 offset:53504\n"
"s_add_u32 m0, s64, 0\n"
"v_mfma_f32_16x16x32_bf16 v[120:123], v[180:183], a[24:27], v[120:123]\n"
"ds_read_b64_tr_b16 v[160:161], v228 offset:49152\n"
"buffer_load_dwordx4 v201, s[8:11], 0 offen offset:0 lds\n"
"s_nop 4\n"
"v_mfma_f32_16x16x32_bf16 v[124:127], v[176:179], a[20:23], v[124:127]\n"
"ds_read_b64_tr_b16 v[162:163], v228 offset:49408\n"
"s_barrier\n"
"v_mfma_f32_16x16x32_bf16 v[124:127], v[180:183], a[28:31], v[124:127]\n"
"ds_read_b64_tr_b16 v[164:165], v228 offset:53248\n"
"v_lshlrev_b32 v249, 3, v1\n"
"s_nop 4\n"
"v_mfma_f32_16x16x32_bf16 v[136:139], v[176:179], v[184:187], v[136:139]\n"
"ds_read_b64_tr_b16 v[166:167], v228 offset:53504\n"
"s_add_u32 s88, s63, 114688\n"
"v_mfma_f32_16x16x32_bf16 v[136:139], v[180:183], v[192:195], v[136:139]\n"
"buffer_load_dwordx4 v202, s[8:11], 0 offen offset:1024 lds\n"
"s_add_u32 s8, s8, s1\n"
"s_nop 4\n"
"v_mfma_f32_16x16x32_bf16 v[140:143], v[176:179], v[188:191], v[140:143]\n"
"s_add_u32 m0, s64, 8192\n"
"v_add_u32 v248, s88, v249\n"
"v_mfma_f32_16x16x32_bf16 v[140:143], v[180:183], v[196:199], v[140:143]\n"
"ds_read_b64_tr_b16 v[168:169], v227 offset:57344\n"
"s_waitcnt lgkmcnt(5)\n"
"s_nop 4\n"
"v_mfma_f32_16x16x32_bf16 v[48:51], v[152:155], a[32:35], v[48:51]\n"
"ds_read_b64_tr_b16 v[170:171], v227 offset:57600\n"
"v_add_u32 v249, 147456, v249\n"
"v_mfma_f32_16x16x32_bf16 v[48:51], v[156:159], a[40:43], v[48:51]\n"
"ds_read_b64_tr_b16 v[172:173], v227 offset:61440\n"
"buffer_load_dwordx4 v201, s[8:11], 0 offen offset:0 lds\n"
"s_nop 4\n"
"v_mfma_f32_16x16x32_bf16 v[52:55], v[152:155], a[36:39], v[52:55]\n"
"ds_read_b64_tr_b16 v[174:175], v227 offset:61696\n"
"v_add_u32 v211, 147456, v7\n"
"v_mfma_f32_16x16x32_bf16 v[52:55], v[156:159], a[44:47], v[52:55]\n"
"ds_read_b128 v[212:215], v211 offset:4096\n"
"s_lshl_b32 s88, s54, 9\n"
"s_nop 4\n"
"v_mfma_f32_16x16x32_bf16 v[64:67], v[152:155], v[16:19], v[64:67]\n"
"s_cmp_eq_u32 s19, 0\n"
"s_cbranch_scc0 .Lk_pf2\n"
"buffer_load_dword v232, v45, s[24:27], 0 offen\n"
".Lk_pf2:\n"
"v_mfma_f32_16x16x32_bf16 v[64:67], v[156:159], v[24:27], v[64:67]\n"
"buffer_load_dwordx4 v202, s[8:11], 0 offen offset:1024 lds\n"
"s_add_u32 s8, s8, s92\n"
"s_nop 4\n"
"v_mfma_f32_16x16x32_bf16 v[68:71], v[152:155], v[20:23], v[68:71]\n"
"s_add_u32 m0, s64, 16384\n"
"v_add_u32 v205, s88, v249\n"
"v_mfma_f32_16x16x32_bf16 v[68:71], v[156:159], v[28:31], v[68:71]\n"
"ds_read_b64_tr_b16 v[176:177], v228 offset:57344\n"
"s_waitcnt lgkmcnt(6)\n"
"s_nop 4\n"
"v_mfma_f32_16x16x32_bf16 v[56:59], v[160:163], a[32:35], v[56:59]\n"
"ds_read_b64_tr_b16 v[178:179], v228 offset:57600\n"
"s_lshl_b32 s89, s55, 4\n"
"v_mfma_f32_16x16x32_bf16 v[56:59], v[164:167], a[40:43], v[56:59]\n"
"ds_read_b64_tr_b16 v[180:181], v228 offset:61440\n"
"buffer_load_dwordx4 v201, s[8:11], 0 offen offset:0 lds\n"
"s_nop 4\n"
"v_mfma_f32_16x16x32_bf16 v[60:63], v[160:163], a[36:39], v[60:63]\n"
"ds_read_b64_tr_b16 v[182:183], v228 offset:61696\n"
"s_add_u32 s89, s89, s56\n"
"v_mfma_f32_16x16x32_bf16 v[60:63], v[164:167], a[44:47], v[60:63]\n"
"s_lshl_b32 s89, s89, 10\n"
"s_add_u32 s89, s89, 38797312\n"
"s_nop 4\n"
"v_mfma_f32_16x16x32_bf16 v[72:75], v[160:163], v[16:19], v[72:75]\n"
"s_cmp_eq_u32 s54, 0\n"
"s_cselect_b32 s90, s89, 1879048192\n"
"v_mfma_f32_16x16x32_bf16 v[72:75], v[164:167], v[24:27], v[72:75]\n"
"buffer_load_dwordx4 v202, s[8:11], 0 offen offset:1024 lds\n"
"s_add_u32 s4, s100, s65\n"
"s_nop 4\n"
"v_mfma_f32_16x16x32_bf16 v[76:79], v[160:163], v[20:23], v[76:79]\n"
"s_add_u32 m0, s63, 114688\n"
"v_mfma_f32_16x16x32_bf16 v[76:79], v[164:167], v[28:31], v[76:79]\n"
"ds_read_b64_tr_b16 v[152:153], v229 offset:0\n"
"s_waitcnt lgkmcnt(6)\n"
"s_nop 4\n"
"v_mfma_f32_16x16x32_bf16 v[80:83], v[168:171], a[32:35], v[80:83]\n"
"s_cmp_eq_u32 s54, 3\n"
"s_cselect_b64 s[98:99], -1, 0\n"
"v_mfma_f32_16x16x32_bf16 v[80:83], v[172:175], a[40:43], v[80:83]\n"
"ds_read_b64_tr_b16 v[154:155], v229 offset:256\n"
"buffer_load_dwordx4 v8, s[4:7], 0 offen offset:0 lds\n"
"s_nop 4\n"
"v_mfma_f32_16x16x32_bf16 v[84:87], v[168:171], a[36:39], v[84:87]\n"
"ds_read_b64_tr_b16 v[156:157], v229 offset:4096\n"
"v_mfma_f32_16x16x32_bf16 v[84:87], v[172:175], a[44:47], v[84:87]\n"
"ds_read_b64_tr_b16 v[158:159], v229 offset:4352\n"
"s_nop 4\n"
"v_mfma_f32_16x16x32_bf16 v[96:99], v[168:171], v[16:19], v[96:99]\n"
"v_mfma_f32_16x16x32_bf16 v[96:99], v[172:175], v[24:27], v[96:99]\n"
"buffer_load_dwordx4 v9, s[4:7], 0 offen offset:1024 lds\n"
"s_nop 4\n"
"v_mfma_f32_16x16x32_bf16 v[100:103], v[168:171], v[20:23], v[100:103]\n"
"v_mfma_f32_16x16x32_bf16 v[100:103], v[172:175], v[28:31], v[100:103]\n"
"ds_read_b64_tr_b16 v[160:161], v230 offset:0\n"
"s_waitcnt lgkmcnt(5)\n"
"s_nop 4\n"
"v_mfma_f32_16x16x32_bf16 v[88:91], v[176:179], a[32:35], v[88:91]\n"
"ds_read_b64_tr_b16 v[162:163], v230 offset:256\n"
"v_mfma_f32_16x16x32_bf16 v[88:91], v[180:183], a[40:43], v[88:91]\n"
"ds_read_b64_tr_b16 v[164:165], v230 offset:4096\n"
"buffer_load_dwordx4 v10, s[4:7], 0 offen offset:2048 lds\n"
"s_nop 4\n"
"v_mfma_f32_16x16x32_bf16 v[92:95], v[176:179], a[36:39], v[92:95]\n"
"ds_read_b64_tr_b16 v[166:167], v230 offset:4352\n"
"v_mfma_f32_16x16x32_bf16 v[92:95], v[180:183], a[44:47], v[92:95]\n"
"s_waitcnt vmcnt(9)\n"
"s_barrier\n"
"s_nop 4\n"
"v_mfma_f32_16x16x32_bf16 v[104:107], v[176:179], v[16:19], v[104:107]\n"
"ds_read_b128 a[48:51], v225 offset:16384\n"
"v_mfma_f32_16x16x32_bf16 v[104:107], v[180:183], v[24:27], v[104:107]\n"
"ds_read_b128 a[56:59], v226 offset:16384\n"
"buffer_load_dwordx4 v11, s[4:7], 0 offen offset:3072 lds\n"
"s_nop 4\n"
"v_mfma_f32_16x16x32_bf16 v[108:111], v[176:179], v[20:23], v[108:111]\n"
"ds_read_b128 a[52:55], v225 offset:18432\n"
"s_add_u32 s65, s65, 128\n"
"v_mfma_f32_16x16x32_bf16 v[108:111], v[180:183], v[28:31], v[108:111]\n"
"ds_read_b128 a[60:63], v226 offset:18432\n"
"s_waitcnt lgkmcnt(8)\n"
"s_nop 4\n"
"v_mfma_f32_16x16x32_bf16 v[112:115], v[152:155], a[32:35], v[112:115]\n"
"ds_read_b128 v[184:187], v32 offset:16384\n"
"s_and_b32 s65, s65, 2047\n"
"v_mfma_f32_16x16x32_bf16 v[112:115], v[156:159], a[40:43], v[112:115]\n"
"ds_read_b128 v[192:195], v33 offset:16384\n"
"s_mul_i32 s66, s65, 3584\n"
"s_nop 4\n"
"v_mfma_f32_16x16x32_bf16 v[116:119], v[152:155], a[36:39], v[116:119]\n"
"ds_read_b128 v[188:191], v32 offset:18432\n"
"s_add_u32 s8, s101, s66\n"
"v_mfma_f32_16x16x32_bf16 v[116:119], v[156:159], a[44:47], v[116:119]\n"
"ds_read_b128 v[196:199], v33 offset:18432\n"
"s_add_u32 m0, s64, 24576\n"
"s_nop 4\n"
"v_mfma_f32_16x16x32_bf16 v[128:131], v[152:155], v[16:19], v[128:131]\n"
"ds_read_b64_tr_b16 v[168:169], v229 offset:8192\n"
"buffer_load_dwordx4 v201, s[8:11], 0 offen offset:0 lds\n"
"v_mfma_f32_16x16x32_bf16 v[128:131], v[156:159], v[24:27], v[128:131]\n"
"ds_read_b64_tr_b16 v[170:171], v229 offset:8448\n"
"ds_read_b64_tr_b16 v[172:173], v229 offset:12288\n"
"s_nop 4\n"
"v_mfma_f32_16x16x32_bf16 v[132:135], v[152:155], v[20:23], v[132:135]\n"
"ds_read_b64_tr_b16 v[174:175], v229 offset:12544\n"
"v_mfma_f32_16x16x32_bf16 v[132:135], v[156:159], v[28:31], v[132:135]\n"
"ds_read_b64_tr_b16 v[176:177], v230 offset:8192\n"
"s_waitcnt lgkmcnt(13)\n"
"s_nop 4\n"
"v_mfma_f32_16x16x32_bf16 v[120:123], v[160:163], a[32:35], v[120:123]\n"
"ds_read_b64_tr_b16 v[178:179], v230 offset:8448\n"
"buffer_load_dwordx4 v202, s[8:11], 0 offen offset:1024 lds\n"
"v_mfma_f32_16x16x32_bf16 v[120:123], v[164:167], a[40:43], v[120:123]\n"
"ds_read_b64_tr_b16 v[180:181], v230 offset:12288\n"
"s_add_u32 s8, s8, s1\n"
"s_nop 4\n"
"v_mfma_f32_16x16x32_bf16 v[124:127], v[160:163], a[36:39], v[124:127]\n"
"ds_read_b64_tr_b16 v[182:183], v230 offset:12544\n"
"s_add_u32 m0, s64, 32768\n"
"v_mfma_f32_16x16x32_bf16 v[124:127], v[164:167], a[44:47], v[124:127]\n"
"s_waitcnt lgkmcnt(8)\n"
"s_barrier\n"
"s_nop 4\n"
"v_mfma_f32_16x16x32_bf16 v[136:139], v[160:163], v[16:19], v[136:139]\n"
"buffer_load_dwordx4 v201, s[8:11], 0 offen offset:0 lds\n"
"v_mfma_f32_16x16x32_bf16 v[136:139], v[164:167], v[24:27], v[136:139]\n"
"s_nop 4\n"
"v_mfma_f32_16x16x32_bf16 v[140:143], v[160:163], v[20:23], v[140:143]\n"
"v_mfma_f32_16x16x32_bf16 v[140:143], v[164:167], v[28:31], v[140:143]\n"
"ds_read_b64_tr_b16 v[152:153], v229 offset:16384\n"
"s_waitcnt lgkmcnt(5)\n"
"s_nop 4\n"
"v_mfma_f32_16x16x32_bf16 v[48:51], v[168:171], a[48:51], v[48:51]\n"
"ds_read_b64_tr_b16 v[154:155], v229 offset:16640\n"
"buffer_load_dwordx4 v202, s[8:11], 0 offen offset:1024 lds\n"
"v_mfma_f32_16x16x32_bf16 v[48:51], v[172:175], a[56:59], v[48:51]\n"
"ds_read_b64_tr_b16 v[156:157], v229 offset:20480\n"
"s_add_u32 s8, s8, s92\n"
"s_nop 4\n"
"v_mfma_f32_16x16x32_bf16 v[52:55], v[168:171], a[52:55], v[52:55]\n"
"ds_read_b64_tr_b16 v[158:159], v229 offset:20736\n"
"s_add_u32 m0, s64, 40960\n"
"v_mfma_f32_16x16x32_bf16 v[52:55], v[172:175], a[60:63], v[52:55]\n"
"s_nop 4\n"
"v_mfma_f32_16x16x32_bf16 v[64:67], v[168:171], v[184:187], v[64:67]\n"
"buffer_load_dwordx4 v201, s[8:11], 0 offen offset:0 lds\n"
"v_mfma_f32_16x16x32_bf16 v[64:67], v[172:175], v[192:195], v[64:67]\n"
"s_nop 4\n"
"v_mfma_f32_16x16x32_bf16 v[68:71], v[168:171], v[188:191], v[68:71]\n"
"v_mfma_f32_16x16x32_bf16 v[68:71], v[172:175], v[196:199], v[68:71]\n"
"ds_read_b64_tr_b16 v[160:161], v230 offset:16384\n"
"s_waitcnt lgkmcnt(5)\n"
"s_nop 4\n"
"v_mfma_f32_16x16x32_bf16 v[56:59], v[176:179], a[48:51], v[56:59]\n"
"ds_read_b64_tr_b16 v[162:163], v230 offset:16640\n"
"buffer_load_dwordx4 v202, s[8:11], 0 offen offset:1024 lds\n"
"v_mfma_f32_16x16x32_bf16 v[56:59], v[180:183], a[56:59], v[56:59]\n"
"ds_read_b64_tr_b16 v[164:165], v230 offset:20480\n"
"s_nop 4\n"
"v_mfma_f32_16x16x32_bf16 v[60:63], v[176:179], a[52:55], v[60:63]\n"
"ds_read_b64_tr_b16 v[166:167], v230 offset:20736\n"
"v_mfma_f32_16x16x32_bf16 v[60:63], v[180:183], a[60:63], v[60:63]\n"
"s_nop 4\n"
"v_mfma_f32_16x16x32_bf16 v[72:75], v[176:179], v[184:187], v[72:75]\n"
"s_add_u32 s4, s100, s65\n"
"s_add_u32 m0, s63, 131072\n"
"v_mfma_f32_16x16x32_bf16 v[72:75], v[180:183], v[192:195], v[72:75]\n"
"buffer_load_dwordx4 v8, s[4:7], 0 offen offset:0 lds\n"
"s_nop 4\n"
"v_mfma_f32_16x16x32_bf16 v[76:79], v[176:179], v[188:191], v[76:79]\n"
"v_mfma_f32_16x16x32_bf16 v[76:79], v[180:183], v[196:199], v[76:79]\n"
"ds_read_b64_tr_b16 v[168:169], v229 offset:24576\n"
"s_waitcnt lgkmcnt(5)\n"
"v_mfma_f32_16x16x32_bf16 v[80:83], v[152:155], a[48:51], v[80:83]\n"
"ds_read_b64_tr_b16 v[170:171], v229 offset:24832\n"
"v_mfma_f32_16x16x32_bf16 v[80:83], v[156:159], a[56:59], v[80:83]\n"
"s_cmp_eq_u32 s19, 1\n"
"s_cbranch_scc0 .Lk_pf3\n"
"buffer_load_dword v232, v45, s[24:27], 0 offen\n"
".Lk_pf3:\n"
"v_mfma_f32_16x16x32_bf16 v[84:87], v[152:155], a[52:55], v[84:87]\n"
"ds_read_b64_tr_b16 v[172:173], v229 offset:28672\n"
"buffer_load_dwordx4 v9, s[4:7], 0 offen offset:1024 lds\n"
"v_mfma_f32_16x16x32_bf16 v[84:87], v[156:159], a[60:63], v[84:87]\n"
"ds_read_b64_tr_b16 v[174:175], v229 offset:28928\n"
"v_readlane_b32 s17, v207, 48\n"
"v_mfma_f32_16x16x32_bf16 v[96:99], v[152:155], v[184:187], v[96:99]\n"
"v_readlane_b32 s18, v201, 0\n"
"s_lshl_b32 s19, s55, 2\n"
"v_mfma_f32_16x16x32_bf16 v[96:99], v[156:159], v[192:195], v[96:99]\n"
"s_add_u32 s19, s19, s54\n"
"s_lshl_b32 s19, s19, 1\n"
"v_mfma_f32_16x16x32_bf16 v[100:103], v[152:155], v[188:191], v[100:103]\n"
"buffer_load_dwordx4 v10, s[4:7], 0 offen offset:2048 lds\n"
"s_and_b32 s16, s56, 1\n"
"v_mfma_f32_16x16x32_bf16 v[100:103], v[156:159], v[196:199], v[100:103]\n"
"ds_read_b64_tr_b16 v[176:177], v230 offset:24576\n"
"s_waitcnt lgkmcnt(5)\n"
"v_mfma_f32_16x16x32_bf16 v[88:91], v[160:163], a[48:51], v[88:91]\n"
"ds_read_b64_tr_b16 v[178:179], v230 offset:24832\n"
"s_add_u32 s19, s19, s16\n"
"v_mfma_f32_16x16x32_bf16 v[88:91], v[164:167], a[56:59], v[88:91]\n"
"ds_read_b64_tr_b16 v[180:181], v230 offset:28672\n"
"s_mul_i32 s19, s19, 57344\n"
"v_mfma_f32_16x16x32_bf16 v[92:95], v[160:163], a[52:55], v[92:95]\n"
"ds_read_b64_tr_b16 v[182:183], v230 offset:28928\n"
"s_add_u32 s18, s18, s19\n"
"v_mfma_f32_16x16x32_bf16 v[92:95], v[164:167], a[60:63], v[92:95]\n"
"s_waitcnt vmcnt(9)\n"
"s_barrier\n"
"v_mfma_f32_16x16x32_bf16 v[104:107], v[160:163], v[184:187], v[104:107]\n"
"ds_read_b128 a[64:67], v225 offset:0\n"
"buffer_load_dwordx4 v11, s[4:7], 0 offen offset:3072 lds\n"
"v_mfma_f32_16x16x32_bf16 v[104:107], v[164:167], v[192:195], v[104:107]\n"
"ds_read_b128 a[72:75], v226 offset:0\n"
"s_add_u32 s65, s65, 128\n"
"v_mfma_f32_16x16x32_bf16 v[108:111], v[160:163], v[188:191], v[108:111]\n"
"ds_read_b128 a[68:71], v225 offset:2048\n"
"s_and_b32 s65, s65, 2047\n"
"v_mfma_f32_16x16x32_bf16 v[108:111], v[164:167], v[196:199], v[108:111]\n"
"ds_read_b128 a[76:79], v226 offset:2048\n"
"s_waitcnt lgkmcnt(8)\n"
"v_mfma_f32_16x16x32_bf16 v[112:115], v[168:171], a[48:51], v[112:115]\n"
"ds_read_b128 v[16:19], v32 offset:0\n"
"s_mul_i32 s66, s65, 3584\n"
"v_mfma_f32_16x16x32_bf16 v[112:115], v[172:175], a[56:59], v[112:115]\n"
"ds_read_b128 v[24:27], v33 offset:0\n"
"s_add_u32 s8, s101, s66\n"
"v_mfma_f32_16x16x32_bf16 v[116:119], v[168:171], a[52:55], v[116:119]\n"
"ds_read_b128 v[20:23], v32 offset:2048\n"
"s_add_u32 m0, s64, 49152\n"
"v_mfma_f32_16x16x32_bf16 v[116:119], v[172:175], a[60:63], v[116:119]\n"
"ds_read_b128 v[28:31], v33 offset:2048\n"
"buffer_load_dwordx4 v201, s[8:11], 0 offen offset:0 lds\n"
"v_mfma_f32_16x16x32_bf16 v[128:131], v[168:171], v[184:187], v[128:131]\n"
"ds_read_b64_tr_b16 v[152:153], v227 offset:0\n"
"ds_read_b64_tr_b16 v[154:155], v227 offset:256\n"
"v_mfma_f32_16x16x32_bf16 v[128:131], v[172:175], v[192:195], v[128:131]\n"
"ds_read_b64_tr_b16 v[156:157], v227 offset:4096\n"
"s_add_u32 s18, s18, 64\n"
"v_mfma_f32_16x16x32_bf16 v[132:135], v[168:171], v[188:191], v[132:135]\n"
"ds_read_b64_tr_b16 v[158:159], v227 offset:4352\n"
"s_add_u32 s17, s17, s18\n"
"v_mfma_f32_16x16x32_bf16 v[132:135], v[172:175], v[196:199], v[132:135]\n"
"ds_read_b64_tr_b16 v[160:161], v228 offset:0\n"
"s_waitcnt lgkmcnt(13)\n"
"v_mfma_f32_16x16x32_bf16 v[120:123], v[176:179], a[48:51], v[120:123]\n"
"ds_read_b64_tr_b16 v[162:163], v228 offset:256\n"
"buffer_load_dwordx4 v202, s[8:11], 0 offen offset:1024 lds\n"
"v_mfma_f32_16x16x32_bf16 v[120:123], v[180:183], a[56:59], v[120:123]\n"
"ds_read_b64_tr_b16 v[164:165], v228 offset:4096\n"
"s_add_u32 s8, s8, s1\n"
"v_mfma_f32_16x16x32_bf16 v[124:127], v[176:179], a[52:55], v[124:127]\n"
"ds_read_b64_tr_b16 v[166:167], v228 offset:4352\n"
"s_add_u32 m0, s64, 57344\n"
"v_mfma_f32_16x16x32_bf16 v[124:127], v[180:183], a[60:63], v[124:127]\n"
"s_waitcnt lgkmcnt(8)\n"
"s_barrier\n"
"v_mfma_f32_16x16x32_bf16 v[136:139], v[176:179], v[184:187], v[136:139]\n"
"buffer_load_dwordx4 v201, s[8:11], 0 offen offset:0 lds\n"
"v_lshrrev_b32 v44, 3, v1\n"
"v_mfma_f32_16x16x32_bf16 v[136:139], v[180:183], v[192:195], v[136:139]\n"
"v_mul_u32_u24 v44, 7168, v44\n"
"v_add_u32 v44, s17, v44\n"
"v_mfma_f32_16x16x32_bf16 v[140:143], v[176:179], v[188:191], v[140:143]\n"
"v_readlane_b32 s18, v14, 0\n"
"s_lshr_b32 s19, s54, 1\n"
"v_mfma_f32_16x16x32_bf16 v[140:143], v[180:183], v[196:199], v[140:143]\n"
"ds_read_b64_tr_b16 v[168:169], v227 offset:8192\n"
"s_waitcnt lgkmcnt(5)\n"
"v_mfma_f32_16x16x32_bf16 v[48:51], v[152:155], a[64:67], v[48:51]\n"
"ds_read_b64_tr_b16 v[170:171], v227 offset:8448\n"
"buffer_load_dwordx4 v202, s[8:11], 0 offen offset:1024 lds\n"
"v_mfma_f32_16x16x32_bf16 v[48:51], v[156:159], a[72:75], v[48:51]\n"
"ds_read_b64_tr_b16 v[172:173], v227 offset:12288\n"
"s_add_u32 s8, s8, s92\n"
"v_mfma_f32_16x16x32_bf16 v[52:55], v[152:155], a[68:71], v[52:55]\n"
"ds_read_b64_tr_b16 v[174:175], v227 offset:12544\n"
"s_add_u32 m0, s64, 65536\n"
"v_mfma_f32_16x16x32_bf16 v[52:55], v[156:159], a[76:79], v[52:55]\n"
"s_lshl_b32 s19, s19, 15\n"
"s_add_u32 s18, s18, s19\n"
"v_mfma_f32_16x16x32_bf16 v[64:67], v[152:155], v[16:19], v[64:67]\n"
"buffer_load_dwordx4 v201, s[8:11], 0 offen offset:0 lds\n"
"s_lshl_b32 s19, s55, 7\n"
"v_mfma_f32_16x16x32_bf16 v[64:67], v[156:159], v[24:27], v[64:67]\n"
"s_add_u32 s18, s18, s19\n"
"s_add_u32 s18, s18, 133120\n"
"v_mfma_f32_16x16x32_bf16 v[68:71], v[152:155], v[20:23], v[68:71]\n"
"v_lshrrev_b32 v45, 2, v1\n"
"v_lshl_add_u32 v45, v45, 11, s18\n"
"v_mfma_f32_16x16x32_bf16 v[68:71], v[156:159], v[28:31], v[68:71]\n"
"ds_read_b64_tr_b16 v[176:177], v228 offset:8192\n"
"s_waitcnt lgkmcnt(5)\n"
"v_mfma_f32_16x16x32_bf16 v[56:59], v[160:163], a[64:67], v[56:59]\n"
"ds_read_b64_tr_b16 v[178:179], v228 offset:8448\n"
"buffer_load_dwordx4 v202, s[8:11], 0 offen offset:1024 lds\n"
"v_mfma_f32_16x16x32_bf16 v[56:59], v[164:167], a[72:75], v[56:59]\n"
"ds_read_b64_tr_b16 v[180:181], v228 offset:12288\n"
"s_lshl_b32 s16, s2, 2\n"
"v_mfma_f32_16x16x32_bf16 v[60:63], v[160:163], a[68:71], v[60:63]\n"
"ds_read_b64_tr_b16 v[182:183], v228 offset:12544\n"
"s_add_u32 s16, s16, s54\n"
"v_mfma_f32_16x16x32_bf16 v[60:63], v[164:167], a[76:79], v[60:63]\n"
"s_mul_i32 s16, s16, 1536\n"
"s_add_u32 s16, s16, 38797312\n"
"v_mfma_f32_16x16x32_bf16 v[72:75], v[160:163], v[16:19], v[72:75]\n"
"s_add_u32 s4, s100, s65\n"
"s_add_u32 m0, s63, 114688\n"
"v_mfma_f32_16x16x32_bf16 v[72:75], v[164:167], v[24:27], v[72:75]\n"
"buffer_load_dwordx4 v8, s[4:7], 0 offen offset:0 lds\n"
"v_lshrrev_b32 v47, 2, v1\n"
"v_mfma_f32_16x16x32_bf16 v[76:79], v[160:163], v[20:23], v[76:79]\n"
"v_min_u32 v47, 11, v47\n"
"v_lshl_add_u32 v47, v47, 7, s16\n"
"v_mfma_f32_16x16x32_bf16 v[76:79], v[164:167], v[28:31], v[76:79]\n"
"ds_read_b64_tr_b16 v[152:153], v227 offset:16384\n"
"s_waitcnt lgkmcnt(5)\n"
"v_mfma_f32_16x16x32_bf16 v[80:83], v[168:171], a[64:67], v[80:83]\n"
"ds_read_b64_tr_b16 v[154:155], v227 offset:16640\n"
"v_readlane_b32 s16, v43, 0\n"
"v_mfma_f32_16x16x32_bf16 v[80:83], v[172:175], a[72:75], v[80:83]\n"
"ds_read_b64_tr_b16 v[156:157], v227 offset:20480\n"
"buffer_load_dwordx4 v9, s[4:7], 0 offen offset:1024 lds\n"
"v_mfma_f32_16x16x32_bf16 v[84:87], v[168:171], a[68:71], v[84:87]\n"
"ds_read_b64_tr_b16 v[158:159], v227 offset:20736\n"
"s_lshl_b32 s17, s82, 5\n"
"v_mfma_f32_16x16x32_bf16 v[84:87], v[172:175], a[76:79], v[84:87]\n"
"s_lshl_b32 s18, s83, 5\n"
"s_sub_u32 s18, s18, s17\n"
"v_mfma_f32_16x16x32_bf16 v[96:99], v[168:171], v[16:19], v[96:99]\n"
"s_add_u32 s16, s16, s17\n"
"s_and_b32 s17, s56, 3\n"
"v_mfma_f32_16x16x32_bf16 v[96:99], v[172:175], v[24:27], v[96:99]\n"
"s_lshl2_add_u32 s17, s55, s17\n"
"s_lshl2_add_u32 s17, s17, s54\n"
"v_mfma_f32_16x16x32_bf16 v[100:103], v[168:171], v[20:23], v[100:103]\n"
"buffer_load_dwordx4 v10, s[4:7], 0 offen offset:2048 lds\n"
"s_lshl_b32 s17, s17, 9\n"
"v_mfma_f32_16x16x32_bf16 v[100:103], v[172:175], v[28:31], v[100:103]\n"
"ds_read_b64_tr_b16 v[160:161], v228 offset:16384\n"
"s_waitcnt lgkmcnt(5)\n"
"v_mfma_f32_16x16x32_bf16 v[88:91], v[176:179], a[64:67], v[88:91]\n"
"ds_read_b64_tr_b16 v[162:163], v228 offset:16640\n"
"s_add_u32 s16, s16, s17\n"
"v_mfma_f32_16x16x32_bf16 v[88:91], v[180:183], a[72:75], v[88:91]\n"
"ds_read_b64_tr_b16 v[164:165], v228 offset:20480\n"
"v_lshrrev_b32 v43, 5, v1\n"
"v_mfma_f32_16x16x32_bf16 v[92:95], v[176:179], a[68:71], v[92:95]\n"
"ds_read_b64_tr_b16 v[166:167], v228 offset:20736\n"
"v_mul_lo_u32 v43, v43, s18\n"
"v_mfma_f32_16x16x32_bf16 v[92:95], v[180:183], a[76:79], v[92:95]\n"
"s_waitcnt vmcnt(9)\n"
"s_barrier\n"
"v_mfma_f32_16x16x32_bf16 v[104:107], v[176:179], v[16:19], v[104:107]\n"
"ds_read_b128 a[80:83], v225 offset:16384\n"
"buffer_load_dwordx4 v11, s[4:7], 0 offen offset:3072 lds\n"
"v_mfma_f32_16x16x32_bf16 v[104:107], v[180:183], v[24:27], v[104:107]\n"
"ds_read_b128 a[88:91], v226 offset:16384\n"
"s_add_u32 s65, s65, 128\n"
"v_mfma_f32_16x16x32_bf16 v[108:111], v[176:179], v[20:23], v[108:111]\n"
"ds_read_b128 a[84:87], v225 offset:18432\n"
"s_and_b32 s65, s65, 2047\n"
"v_mfma_f32_16x16x32_bf16 v[108:111], v[180:183], v[28:31], v[108:111]\n"
"ds_read_b128 a[92:95], v226 offset:18432\n"
"s_waitcnt lgkmcnt(8)\n"
"v_mfma_f32_16x16x32_bf16 v[112:115], v[152:155], a[64:67], v[112:115]\n"
"ds_read_b128 v[184:187], v32 offset:16384\n"
"s_mul_i32 s66, s65, 3584\n"
"v_mfma_f32_16x16x32_bf16 v[112:115], v[156:159], a[72:75], v[112:115]\n"
"ds_read_b128 v[192:195], v33 offset:16384\n"
"s_add_u32 s8, s101, s66\n"
"v_mfma_f32_16x16x32_bf16 v[116:119], v[152:155], a[68:71], v[116:119]\n"
"ds_read_b128 v[188:191], v32 offset:18432\n"
"s_add_u32 m0, s64, 73728\n"
"v_mfma_f32_16x16x32_bf16 v[116:119], v[156:159], a[76:79], v[116:119]\n"
"ds_read_b128 v[196:199], v33 offset:18432\n"
"buffer_load_dwordx4 v201, s[8:11], 0 offen offset:0 lds\n"
"v_mfma_f32_16x16x32_bf16 v[128:131], v[152:155], v[16:19], v[128:131]\n"
"ds_read_b64_tr_b16 v[168:169], v227 offset:24576\n"
"ds_read_b64_tr_b16 v[170:171], v227 offset:24832\n"
"v_mfma_f32_16x16x32_bf16 v[128:131], v[156:159], v[24:27], v[128:131]\n"
"ds_read_b64_tr_b16 v[172:173], v227 offset:28672\n"
"v_bfe_u32 v42, v1, 3, 2\n"
"v_mfma_f32_16x16x32_bf16 v[132:135], v[152:155], v[20:23], v[132:135]\n"
".Lk_h1:\n"
"s_cbranch_execz .Lk_h5\n"
"ds_read_b64_tr_b16 v[174:175], v227 offset:28928\n"
"v_lshl_add_u32 v42, v42, 7, s16\n"
"v_mfma_f32_16x16x32_bf16 v[132:135], v[156:159], v[28:31], v[132:135]\n"
"ds_read_b64_tr_b16 v[176:177], v228 offset:24576\n"
"s_waitcnt lgkmcnt(13)\n"
"v_mfma_f32_16x16x32_bf16 v[120:123], v[160:163], a[64:67], v[120:123]\n"
"ds_read_b64_tr_b16 v[178:179], v228 offset:24832\n"
"buffer_load_dwordx4 v202, s[8:11], 0 offen offset:1024 lds\n"
"v_mfma_f32_16x16x32_bf16 v[120:123], v[164:167], a[72:75], v[120:123]\n"
"ds_read_b64_tr_b16 v[180:181], v228 offset:28672\n"
"s_add_u32 s8, s8, s1\n"
"v_mfma_f32_16x16x32_bf16 v[124:127], v[160:163], a[68:71], v[124:127]\n"
"ds_read_b64_tr_b16 v[182:183], v228 offset:28928\n"
"s_add_u32 m0, s64, 81920\n"
"v_mfma_f32_16x16x32_bf16 v[124:127], v[164:167], a[76:79], v[124:127]\n"
"s_waitcnt lgkmcnt(8)\n"
"s_barrier\n"
"v_mfma_f32_16x16x32_bf16 v[136:139], v[160:163], v[16:19], v[136:139]\n"
"buffer_load_dwordx4 v201, s[8:11], 0 offen offset:0 lds\n"
"v_add_u32 v43, v42, v43\n"
"v_mfma_f32_16x16x32_bf16 v[136:139], v[164:167], v[24:27], v[136:139]\n"
"v_mfma_f32_16x16x32_bf16 v[140:143], v[160:163], v[20:23], v[140:143]\n"
"v_mfma_f32_16x16x32_bf16 v[140:143], v[164:167], v[28:31], v[140:143]\n"
"ds_read_b64_tr_b16 v[152:153], v227 offset:32768\n"
"s_waitcnt lgkmcnt(5)\n"
"v_mfma_f32_16x16x32_bf16 v[48:51], v[168:171], a[80:83], v[48:51]\n"
"ds_read_b64_tr_b16 v[154:155], v227 offset:33024\n"
"buffer_load_dwordx4 v202, s[8:11], 0 offen offset:1024 lds\n"
"v_mfma_f32_16x16x32_bf16 v[48:51], v[172:175], a[88:91], v[48:51]\n"
"ds_read_b64_tr_b16 v[156:157], v227 offset:36864\n"
"s_add_u32 s8, s8, s92\n"
"v_mfma_f32_16x16x32_bf16 v[52:55], v[168:171], a[84:87], v[52:55]\n"
"ds_read_b64_tr_b16 v[158:159], v227 offset:37120\n"
"s_add_u32 m0, s64, 90112\n"
"v_mfma_f32_16x16x32_bf16 v[52:55], v[172:175], a[92:95], v[52:55]\n"
"v_mfma_f32_16x16x32_bf16 v[64:67], v[168:171], v[184:187], v[64:67]\n"
"buffer_load_dwordx4 v201, s[8:11], 0 offen offset:0 lds\n"
"v_mfma_f32_16x16x32_bf16 v[64:67], v[172:175], v[192:195], v[64:67]\n"
"v_mfma_f32_16x16x32_bf16 v[68:71], v[168:171], v[188:191], v[68:71]\n"
"v_mfma_f32_16x16x32_bf16 v[68:71], v[172:175], v[196:199], v[68:71]\n"
"ds_read_b64_tr_b16 v[160:161], v228 offset:32768\n"
"s_waitcnt lgkmcnt(5)\n"
"v_mfma_f32_16x16x32_bf16 v[56:59], v[176:179], a[80:83], v[56:59]\n"
"ds_read_b64_tr_b16 v[162:163], v228 offset:33024\n"
"buffer_load_dwordx4 v202, s[8:11], 0 offen offset:1024 lds\n"
"v_mfma_f32_16x16x32_bf16 v[56:59], v[180:183], a[88:91], v[56:59]\n"
"ds_read_b64_tr_b16 v[164:165], v228 offset:36864\n"
"v_mfma_f32_16x16x32_bf16 v[60:63], v[176:179], a[84:87], v[60:63]\n"
"ds_read_b64_tr_b16 v[166:167], v228 offset:37120\n"
"v_mfma_f32_16x16x32_bf16 v[60:63], v[180:183], a[92:95], v[60:63]\n"
"v_mfma_f32_16x16x32_bf16 v[72:75], v[176:179], v[184:187], v[72:75]\n"
"s_add_u32 s4, s100, s65\n"
"s_add_u32 m0, s63, 131072\n"
"v_mfma_f32_16x16x32_bf16 v[72:75], v[180:183], v[192:195], v[72:75]\n"
"buffer_load_dwordx4 v8, s[4:7], 0 offen offset:0 lds\n"
"v_mfma_f32_16x16x32_bf16 v[76:79], v[176:179], v[188:191], v[76:79]\n"
"v_mfma_f32_16x16x32_bf16 v[76:79], v[180:183], v[196:199], v[76:79]\n"
"ds_read_b64_tr_b16 v[168:169], v227 offset:40960\n"
"s_waitcnt lgkmcnt(5)\n"
"v_mfma_f32_16x16x32_bf16 v[80:83], v[152:155], a[80:83], v[80:83]\n"
"ds_read_b64_tr_b16 v[170:171], v227 offset:41216\n"
"v_mfma_f32_16x16x32_bf16 v[80:83], v[156:159], a[88:91], v[80:83]\n"
"ds_read_b64_tr_b16 v[172:173], v227 offset:45056\n"
"buffer_load_dwordx4 v9, s[4:7], 0 offen offset:1024 lds\n"
"v_mfma_f32_16x16x32_bf16 v[84:87], v[152:155], a[84:87], v[84:87]\n"
"ds_read_b64_tr_b16 v[174:175], v227 offset:45312\n"
"v_mfma_f32_16x16x32_bf16 v[84:87], v[156:159], a[92:95], v[84:87]\n"
"v_mfma_f32_16x16x32_bf16 v[96:99], v[152:155], v[184:187], v[96:99]\n"
"v_mfma_f32_16x16x32_bf16 v[96:99], v[156:159], v[192:195], v[96:99]\n"
"v_mfma_f32_16x16x32_bf16 v[100:103], v[152:155], v[188:191], v[100:103]\n"
"buffer_load_dwordx4 v10, s[4:7], 0 offen offset:2048 lds\n"
"v_mfma_f32_16x16x32_bf16 v[100:103], v[156:159], v[196:199], v[100:103]\n"
"ds_read_b64_tr_b16 v[176:177], v228 offset:40960\n"
"s_waitcnt lgkmcnt(5)\n"
"v_mfma_f32_16x16x32_bf16 v[88:91], v[160:163], a[80:83], v[88:91]\n"
"ds_read_b64_tr_b16 v[178:179], v228 offset:41216\n"
"v_mfma_f32_16x16x32_bf16 v[88:91], v[164:167], a[88:91], v[88:91]\n"
"ds_read_b64_tr_b16 v[180:181], v228 offset:45056\n"
"v_mfma_f32_16x16x32_bf16 v[92:95], v[160:163], a[84:87], v[92:95]\n"
"ds_read_b64_tr_b16 v[182:183], v228 offset:45312\n"
"v_mfma_f32_16x16x32_bf16 v[92:95], v[164:167], a[92:95], v[92:95]\n"
"s_waitcnt vmcnt(9)\n"
"s_barrier\n"
"v_mfma_f32_16x16x32_bf16 v[104:107], v[160:163], v[184:187], v[104:107]\n"
"ds_read_b128 a[96:99], v225 offset:0\n"
"buffer_load_dwordx4 v11, s[4:7], 0 offen offset:3072 lds\n"
"v_mfma_f32_16x16x32_bf16 v[104:107], v[164:167], v[192:195], v[104:107]\n"
"ds_read_b128 a[104:107], v226 offset:0\n"
"s_add_u32 s65, s65, 128\n"
"v_mfma_f32_16x16x32_bf16 v[108:111], v[160:163], v[188:191], v[108:111]\n"
"ds_read_b128 a[100:103], v225 offset:2048\n"
"s_and_b32 s65, s65, 2047\n"
"v_mfma_f32_16x16x32_bf16 v[108:111], v[164:167], v[196:199], v[108:111]\n"
"ds_read_b128 a[108:111], v226 offset:2048\n"
"s_waitcnt lgkmcnt(8)\n"
"v_mfma_f32_16x16x32_bf16 v[112:115], v[168:171], a[80:83], v[112:115]\n"
"ds_read_b128 v[16:19], v32 offset:0\n"
"s_mul_i32 s66, s65, 3584\n"
"v_mfma_f32_16x16x32_bf16 v[112:115], v[172:175], a[88:91], v[112:115]\n"
"ds_read_b128 v[24:27], v33 offset:0\n"
"s_add_u32 s8, s101, s66\n"
"v_mfma_f32_16x16x32_bf16 v[116:119], v[168:171], a[84:87], v[116:119]\n"
"ds_read_b128 v[20:23], v32 offset:2048\n"
"s_add_u32 m0, s64, 0\n"
"v_mfma_f32_16x16x32_bf16 v[116:119], v[172:175], a[92:95], v[116:119]\n"
"ds_read_b128 v[28:31], v33 offset:2048\n"
"buffer_load_dwordx4 v201, s[8:11], 0 offen offset:0 lds\n"
"v_mfma_f32_16x16x32_bf16 v[128:131], v[168:171], v[184:187], v[128:131]\n"
"ds_read_b64_tr_b16 v[152:153], v227 offset:49152\n"
"ds_read_b64_tr_b16 v[154:155], v227 offset:49408\n"
"v_mfma_f32_16x16x32_bf16 v[128:131], v[172:175], v[192:195], v[128:131]\n"
"ds_read_b64_tr_b16 v[156:157], v227 offset:53248\n"
"v_mfma_f32_16x16x32_bf16 v[132:135], v[168:171], v[188:191], v[132:135]\n"
"ds_read_b64_tr_b16 v[158:159], v227 offset:53504\n"
"v_mfma_f32_16x16x32_bf16 v[132:135], v[172:175], v[196:199], v[132:135]\n"
"ds_read_b64_tr_b16 v[160:161], v228 offset:49152\n"
"s_waitcnt lgkmcnt(13)\n"
"v_mfma_f32_16x16x32_bf16 v[120:123], v[176:179], a[80:83], v[120:123]\n"
"ds_read_b64_tr_b16 v[162:163], v228 offset:49408\n"
"buffer_load_dwordx4 v202, s[8:11], 0 offen offset:1024 lds\n"
"v_mfma_f32_16x16x32_bf16 v[120:123], v[180:183], a[88:91], v[120:123]\n"
"ds_read_b64_tr_b16 v[164:165], v228 offset:53248\n"
"s_add_u32 s8, s8, s1\n"
"v_mfma_f32_16x16x32_bf16 v[124:127], v[176:179], a[84:87], v[124:127]\n"
"ds_read_b64_tr_b16 v[166:167], v228 offset:53504\n"
"s_add_u32 m0, s64, 8192\n"
"v_mfma_f32_16x16x32_bf16 v[124:127], v[180:183], a[92:95], v[124:127]\n"
"s_waitcnt lgkmcnt(8)\n"
"s_barrier\n"
"v_mfma_f32_16x16x32_bf16 v[136:139], v[176:179], v[184:187], v[136:139]\n"
"buffer_load_dwordx4 v201, s[8:11], 0 offen offset:0 lds\n"
"v_mfma_f32_16x16x32_bf16 v[136:139], v[180:183], v[192:195], v[136:139]\n"
"v_mfma_f32_16x16x32_bf16 v[140:143], v[176:179], v[188:191], v[140:143]\n"
"v_mfma_f32_16x16x32_bf16 v[140:143], v[180:183], v[196:199], v[140:143]\n"
"ds_read_b64_tr_b16 v[168:169], v227 offset:57344\n"
"s_waitcnt lgkmcnt(5)\n"
"v_mfma_f32_16x16x32_bf16 v[48:51], v[152:155], a[96:99], v[48:51]\n"
"ds_read_b64_tr_b16 v[170:171], v227 offset:57600\n"
"buffer_load_dwordx4 v202, s[8:11], 0 offen offset:1024 lds\n"
"v_mfma_f32_16x16x32_bf16 v[48:51], v[156:159], a[104:107], v[48:51]\n"
"ds_read_b64_tr_b16 v[172:173], v227 offset:61440\n"
"s_add_u32 s8, s8, s92\n"
"v_mfma_f32_16x16x32_bf16 v[52:55], v[152:155], a[100:103], v[52:55]\n"
"ds_read_b64_tr_b16 v[174:175], v227 offset:61696\n"
"s_add_u32 m0, s64, 16384\n"
"v_mfma_f32_16x16x32_bf16 v[52:55], v[156:159], a[108:111], v[52:55]\n"
"v_mfma_f32_16x16x32_bf16 v[64:67], v[152:155], v[16:19], v[64:67]\n"
"buffer_load_dwordx4 v201, s[8:11], 0 offen offset:0 lds\n"
"v_mfma_f32_16x16x32_bf16 v[64:67], v[156:159], v[24:27], v[64:67]\n"
"v_mfma_f32_16x16x32_bf16 v[68:71], v[152:155], v[20:23], v[68:71]\n"
"v_mfma_f32_16x16x32_bf16 v[68:71], v[156:159], v[28:31], v[68:71]\n"
"ds_read_b64_tr_b16 v[176:177], v228 offset:57344\n"
"s_waitcnt lgkmcnt(5)\n"
"v_mfma_f32_16x16x32_bf16 v[56:59], v[160:163], a[96:99], v[56:59]\n"
"ds_read_b64_tr_b16 v[178:179], v228 offset:57600\n"
"buffer_load_dwordx4 v202, s[8:11], 0 offen offset:1024 lds\n"
"v_mfma_f32_16x16x32_bf16 v[56:59], v[164:167], a[104:107], v[56:59]\n"
"ds_read_b64_tr_b16 v[180:181], v228 offset:61440\n"
"v_mfma_f32_16x16x32_bf16 v[60:63], v[160:163], a[100:103], v[60:63]\n"
"ds_read_b64_tr_b16 v[182:183], v228 offset:61696\n"
"v_mfma_f32_16x16x32_bf16 v[60:63], v[164:167], a[108:111], v[60:63]\n"
"v_mfma_f32_16x16x32_bf16 v[72:75], v[160:163], v[16:19], v[72:75]\n"
"s_add_u32 s4, s100, s65\n"
"s_add_u32 m0, s63, 114688\n"
"v_mfma_f32_16x16x32_bf16 v[72:75], v[164:167], v[24:27], v[72:75]\n"
"buffer_load_dwordx4 v8, s[4:7], 0 offen offset:0 lds\n"
"v_mfma_f32_16x16x32_bf16 v[76:79], v[160:163], v[20:23], v[76:79]\n"
"v_mfma_f32_16x16x32_bf16 v[76:79], v[164:167], v[28:31], v[76:79]\n"
"ds_read_b64_tr_b16 v[152:153], v229 offset:0\n"
"s_waitcnt lgkmcnt(5)\n"
"v_mfma_f32_16x16x32_bf16 v[80:83], v[168:171], a[96:99], v[80:83]\n"
"ds_read_b64_tr_b16 v[154:155], v229 offset:256\n"
"v_mfma_f32_16x16x32_bf16 v[80:83], v[172:175], a[104:107], v[80:83]\n"
"ds_read_b64_tr_b16 v[156:157], v229 offset:4096\n"
"buffer_load_dwordx4 v9, s[4:7], 0 offen offset:1024 lds\n"
"v_mfma_f32_16x16x32_bf16 v[84:87], v[168:171], a[100:103], v[84:87]\n"
"ds_read_b64_tr_b16 v[158:159], v229 offset:4352\n"
"v_mfma_f32_16x16x32_bf16 v[84:87], v[172:175], a[108:111], v[84:87]\n"
"v_mfma_f32_16x16x32_bf16 v[96:99], v[168:171], v[16:19], v[96:99]\n"
"v_mfma_f32_16x16x32_bf16 v[96:99], v[172:175], v[24:27], v[96:99]\n"
"v_mfma_f32_16x16x32_bf16 v[100:103], v[168:171], v[20:23], v[100:103]\n"
"buffer_load_dwordx4 v10, s[4:7], 0 offen offset:2048 lds\n"
"v_mfma_f32_16x16x32_bf16 v[100:103], v[172:175], v[28:31], v[100:103]\n"
"ds_read_b64_tr_b16 v[160:161], v230 offset:0\n"
"s_waitcnt lgkmcnt(5)\n"
"v_mfma_f32_16x16x32_bf16 v[88:91], v[176:179], a[96:99], v[88:91]\n"
"ds_read_b64_tr_b16 v[162:163], v230 offset:256\n"
"v_mfma_f32_16x16x32_bf16 v[88:91], v[180:183], a[104:107], v[88:91]\n"
"ds_read_b64_tr_b16 v[164:165], v230 offset:4096\n"
"v_mfma_f32_16x16x32_bf16 v[92:95], v[176:179], a[100:103], v[92:95]\n"
"ds_read_b64_tr_b16 v[166:167], v230 offset:4352\n"
"v_mfma_f32_16x16x32_bf16 v[92:95], v[180:183], a[108:111], v[92:95]\n"
"s_waitcnt vmcnt(9)\n"
"s_barrier\n"
"v_mfma_f32_16x16x32_bf16 v[104:107], v[176:179], v[16:19], v[104:107]\n"
"ds_read_b128 a[112:115], v225 offset:16384\n"
"buffer_load_dwordx4 v11, s[4:7], 0 offen offset:3072 lds\n"
"v_mfma_f32_16x16x32_bf16 v[104:107], v[180:183], v[24:27], v[104:107]\n"
"ds_read_b128 a[120:123], v226 offset:16384\n"
"s_add_u32 s65, s65, 128\n"
"v_mfma_f32_16x16x32_bf16 v[108:111], v[176:179], v[20:23], v[108:111]\n"
"ds_read_b128 a[116:119], v225 offset:18432\n"
"s_and_b32 s65, s65, 2047\n"
"v_mfma_f32_16x16x32_bf16 v[108:111], v[180:183], v[28:31], v[108:111]\n"
"ds_read_b128 a[124:127], v226 offset:18432\n"
"s_waitcnt lgkmcnt(8)\n"
"v_mfma_f32_16x16x32_bf16 v[112:115], v[152:155], a[96:99], v[112:115]\n"
"ds_read_b128 v[184:187], v32 offset:16384\n"
"s_mul_i32 s66, s65, 3584\n"
"v_mfma_f32_16x16x32_bf16 v[112:115], v[156:159], a[104:107], v[112:115]\n"
"ds_read_b128 v[192:195], v33 offset:16384\n"
"s_add_u32 s8, s101, s66\n"
"v_mfma_f32_16x16x32_bf16 v[116:119], v[152:155], a[100:103], v[116:119]\n"
"ds_read_b128 v[188:191], v32 offset:18432\n"
"s_add_u32 m0, s64, 24576\n"
"v_mfma_f32_16x16x32_bf16 v[116:119], v[156:159], a[108:111], v[116:119]\n"
"ds_read_b128 v[196:199], v33 offset:18432\n"
"buffer_load_dwordx4 v201, s[8:11], 0 offen offset:0 lds\n"
"v_mfma_f32_16x16x32_bf16 v[128:131], v[152:155], v[16:19], v[128:131]\n"
"ds_read_b64_tr_b16 v[168:169], v229 offset:8192\n"
"ds_read_b64_tr_b16 v[170:171], v229 offset:8448\n"
"v_mfma_f32_16x16x32_bf16 v[128:131], v[156:159], v[24:27], v[128:131]\n"
"ds_read_b64_tr_b16 v[172:173], v229 offset:12288\n"
"v_mfma_f32_16x16x32_bf16 v[132:135], v[152:155], v[20:23], v[132:135]\n"
"ds_read_b64_tr_b16 v[174:175], v229 offset:12544\n"
"v_mfma_f32_16x16x32_bf16 v[132:135], v[156:159], v[28:31], v[132:135]\n"
"ds_read_b64_tr_b16 v[176:177], v230 offset:8192\n"
"s_waitcnt lgkmcnt(13)\n"
"v_mfma_f32_16x16x32_bf16 v[120:123], v[160:163], a[96:99], v[120:123]\n"
"ds_read_b64_tr_b16 v[178:179], v230 offset:8448\n"
"buffer_load_dwordx4 v202, s[8:11], 0 offen offset:1024 lds\n"
"v_mfma_f32_16x16x32_bf16 v[120:123], v[164:167], a[104:107], v[120:123]\n"
"ds_read_b64_tr_b16 v[180:181], v230 offset:12288\n"
"s_add_u32 s8, s8, s1\n"
"v_mfma_f32_16x16x32_bf16 v[124:127], v[160:163], a[100:103], v[124:127]\n"
"ds_read_b64_tr_b16 v[182:183], v230 offset:12544\n"
"s_add_u32 m0, s64, 32768\n"
"v_mfma_f32_16x16x32_bf16 v[124:127], v[164:167], a[108:111], v[124:127]\n"
"s_waitcnt lgkmcnt(8)\n"
"s_barrier\n"
"v_mfma_f32_16x16x32_bf16 v[136:139], v[160:163], v[16:19], v[136:139]\n"
"buffer_load_dwordx4 v201, s[8:11], 0 offen offset:0 lds\n"
"v_mfma_f32_16x16x32_bf16 v[136:139], v[164:167], v[24:27], v[136:139]\n"
"v_mfma_f32_16x16x32_bf16 v[140:143], v[160:163], v[20:23], v[140:143]\n"
"v_mfma_f32_16x16x32_bf16 v[140:143], v[164:167], v[28:31], v[140:143]\n"
"ds_read_b64_tr_b16 v[152:153], v229 offset:16384\n"
"s_waitcnt lgkmcnt(5)\n"
"v_mfma_f32_16x16x32_bf16 v[48:51], v[168:171], a[112:115], v[48:51]\n"
"ds_read_b64_tr_b16 v[154:155], v229 offset:16640\n"
"buffer_load_dwordx4 v202, s[8:11], 0 offen offset:1024 lds\n"
"v_mfma_f32_16x16x32_bf16 v[48:51], v[172:175], a[120:123], v[48:51]\n"
"ds_read_b64_tr_b16 v[156:157], v229 offset:20480\n"
"s_add_u32 s8, s8, s92\n"
"v_mfma_f32_16x16x32_bf16 v[52:55], v[168:171], a[116:119], v[52:55]\n"
"ds_read_b64_tr_b16 v[158:159], v229 offset:20736\n"
"s_add_u32 m0, s64, 40960\n"
"v_mfma_f32_16x16x32_bf16 v[52:55], v[172:175], a[124:127], v[52:55]\n"
"v_mfma_f32_16x16x32_bf16 v[64:67], v[168:171], v[184:187], v[64:67]\n"
"buffer_load_dwordx4 v201, s[8:11], 0 offen offset:0 lds\n"
"v_mfma_f32_16x16x32_bf16 v[64:67], v[172:175], v[192:195], v[64:67]\n"
"v_mfma_f32_16x16x32_bf16 v[68:71], v[168:171], v[188:191], v[68:71]\n"
"v_mfma_f32_16x16x32_bf16 v[68:71], v[172:175], v[196:199], v[68:71]\n"
"ds_read_b64_tr_b16 v[160:161], v230 offset:16384\n"
"s_waitcnt lgkmcnt(5)\n"
"v_mfma_f32_16x16x32_bf16 v[56:59], v[176:179], a[112:115], v[56:59]\n"
"ds_read_b64_tr_b16 v[162:163], v230 offset:16640\n"
"buffer_load_dwordx4 v202, s[8:11], 0 offen offset:1024 lds\n"
"v_mfma_f32_16x16x32_bf16 v[56:59], v[180:183], a[120:123], v[56:59]\n"
"ds_read_b64_tr_b16 v[164:165], v230 offset:20480\n"
"v_mfma_f32_16x16x32_bf16 v[60:63], v[176:179], a[116:119], v[60:63]\n"
"ds_read_b64_tr_b16 v[166:167], v230 offset:20736\n"
"v_mfma_f32_16x16x32_bf16 v[60:63], v[180:183], a[124:127], v[60:63]\n"
"v_mfma_f32_16x16x32_bf16 v[72:75], v[176:179], v[184:187], v[72:75]\n"
"s_add_u32 s4, s100, s65\n"
"s_add_u32 m0, s63, 131072\n"
"v_mfma_f32_16x16x32_bf16 v[72:75], v[180:183], v[192:195], v[72:75]\n"
"buffer_load_dwordx4 v8, s[4:7], 0 offen offset:0 lds\n"
"v_mfma_f32_16x16x32_bf16 v[76:79], v[176:179], v[188:191], v[76:79]\n"
"v_mfma_f32_16x16x32_bf16 v[76:79], v[180:183], v[196:199], v[76:79]\n"
"ds_read_b64_tr_b16 v[168:169], v229 offset:24576\n"
"s_waitcnt lgkmcnt(5)\n"
"v_mfma_f32_16x16x32_bf16 v[80:83], v[152:155], a[112:115], v[80:83]\n"
"ds_read_b64_tr_b16 v[170:171], v229 offset:24832\n"
"v_mfma_f32_16x16x32_bf16 v[80:83], v[156:159], a[120:123], v[80:83]\n"
"ds_read_b64_tr_b16 v[172:173], v229 offset:28672\n"
"buffer_load_dwordx4 v9, s[4:7], 0 offen offset:1024 lds\n"
"v_mfma_f32_16x16x32_bf16 v[84:87], v[152:155], a[116:119], v[84:87]\n"
"ds_read_b64_tr_b16 v[174:175], v229 offset:28928\n"
"v_mfma_f32_16x16x32_bf16 v[84:87], v[156:159], a[124:127], v[84:87]\n"
"v_mfma_f32_16x16x32_bf16 v[96:99], v[152:155], v[184:187], v[96:99]\n"
"v_mfma_f32_16x16x32_bf16 v[96:99], v[156:159], v[192:195], v[96:99]\n"
"v_mfma_f32_16x16x32_bf16 v[100:103], v[152:155], v[188:191], v[100:103]\n"
"buffer_load_dwordx4 v10, s[4:7], 0 offen offset:2048 lds\n"
"v_mfma_f32_16x16x32_bf16 v[100:103], v[156:159], v[196:199], v[100:103]\n"
"ds_read_b64_tr_b16 v[176:177], v230 offset:24576\n"
"s_waitcnt lgkmcnt(5)\n"
"v_mfma_f32_16x16x32_bf16 v[88:91], v[160:163], a[112:115], v[88:91]\n"
"ds_read_b64_tr_b16 v[178:179], v230 offset:24832\n"
"v_mfma_f32_16x16x32_bf16 v[88:91], v[164:167], a[120:123], v[88:91]\n"
"ds_read_b64_tr_b16 v[180:181], v230 offset:28672\n"
"v_mfma_f32_16x16x32_bf16 v[92:95], v[160:163], a[116:119], v[92:95]\n"
"ds_read_b64_tr_b16 v[182:183], v230 offset:28928\n"
"v_mfma_f32_16x16x32_bf16 v[92:95], v[164:167], a[124:127], v[92:95]\n"
"s_waitcnt vmcnt(9)\n"
"s_barrier\n"
"v_mfma_f32_16x16x32_bf16 v[104:107], v[160:163], v[184:187], v[104:107]\n"
"ds_read_b128 a[128:131], v225 offset:0\n"
"buffer_load_dwordx4 v11, s[4:7], 0 offen offset:3072 lds\n"
"v_mfma_f32_16x16x32_bf16 v[104:107], v[164:167], v[192:195], v[104:107]\n"
"ds_read_b128 a[136:139], v226 offset:0\n"
"s_add_u32 s65, s65, 128\n"
"v_mfma_f32_16x16x32_bf16 v[108:111], v[160:163], v[188:191], v[108:111]\n"
"ds_read_b128 a[132:135], v225 offset:2048\n"
"s_and_b32 s65, s65, 2047\n"
"v_mfma_f32_16x16x32_bf16 v[108:111], v[164:167], v[196:199], v[108:111]\n"
"ds_read_b128 a[140:143], v226 offset:2048\n"
"s_waitcnt lgkmcnt(8)\n"
"v_mfma_f32_16x16x32_bf16 v[112:115], v[168:171], a[112:115], v[112:115]\n"
"ds_read_b128 v[16:19], v32 offset:0\n"
"s_mul_i32 s66, s65, 3584\n"
"v_mfma_f32_16x16x32_bf16 v[112:115], v[172:175], a[120:123], v[112:115]\n"
"ds_read_b128 v[24:27], v33 offset:0\n"
"s_add_u32 s8, s101, s66\n"
"v_mfma_f32_16x16x32_bf16 v[116:119], v[168:171], a[116:119], v[116:119]\n"
"ds_read_b128 v[20:23], v32 offset:2048\n"
"s_add_u32 m0, s64, 49152\n"
"v_mfma_f32_16x16x32_bf16 v[116:119], v[172:175], a[124:127], v[116:119]\n"
"ds_read_b128 v[28:31], v33 offset:2048\n"
"buffer_load_dwordx4 v201, s[8:11], 0 offen offset:0 lds\n"
"v_mfma_f32_16x16x32_bf16 v[128:131], v[168:171], v[184:187], v[128:131]\n"
"ds_read_b64_tr_b16 v[152:153], v227 offset:0\n"
"ds_read_b64_tr_b16 v[154:155], v227 offset:256\n"
"v_mfma_f32_16x16x32_bf16 v[128:131], v[172:175], v[192:195], v[128:131]\n"
"ds_read_b64_tr_b16 v[156:157], v227 offset:4096\n"
"v_mfma_f32_16x16x32_bf16 v[132:135], v[168:171], v[188:191], v[132:135]\n"
"ds_read_b64_tr_b16 v[158:159], v227 offset:4352\n"
"v_mfma_f32_16x16x32_bf16 v[132:135], v[172:175], v[196:199], v[132:135]\n"
"ds_read_b64_tr_b16 v[160:161], v228 offset:0\n"
"s_waitcnt lgkmcnt(13)\n"
"v_mfma_f32_16x16x32_bf16 v[120:123], v[176:179], a[112:115], v[120:123]\n"
"ds_read_b64_tr_b16 v[162:163], v228 offset:256\n"
"buffer_load_dwordx4 v202, s[8:11], 0 offen offset:1024 lds\n"
"v_mfma_f32_16x16x32_bf16 v[120:123], v[180:183], a[120:123], v[120:123]\n"
"ds_read_b64_tr_b16 v[164:165], v228 offset:4096\n"
"s_add_u32 s8, s8, s1\n"
"v_mfma_f32_16x16x32_bf16 v[124:127], v[176:179], a[116:119], v[124:127]\n"
"ds_read_b64_tr_b16 v[166:167], v228 offset:4352\n"
"s_add_u32 m0, s64, 57344\n"
"v_mfma_f32_16x16x32_bf16 v[124:127], v[180:183], a[124:127], v[124:127]\n"
"s_waitcnt lgkmcnt(8)\n"
"s_barrier\n"
"v_mfma_f32_16x16x32_bf16 v[136:139], v[176:179], v[184:187], v[136:139]\n"
"buffer_load_dwordx4 v201, s[8:11], 0 offen offset:0 lds\n"
"v_mfma_f32_16x16x32_bf16 v[136:139], v[180:183], v[192:195], v[136:139]\n"
"v_mfma_f32_16x16x32_bf16 v[140:143], v[176:179], v[188:191], v[140:143]\n"
"v_mfma_f32_16x16x32_bf16 v[140:143], v[180:183], v[196:199], v[140:143]\n"
"ds_read_b64_tr_b16 v[168:169], v227 offset:8192\n"
"s_waitcnt lgkmcnt(5)\n"
"v_mfma_f32_16x16x32_bf16 v[48:51], v[152:155], a[128:131], v[48:51]\n"
"ds_read_b64_tr_b16 v[170:171], v227 offset:8448\n"
"buffer_load_dwordx4 v202, s[8:11], 0 offen offset:1024 lds\n"
"v_mfma_f32_16x16x32_bf16 v[48:51], v[156:159], a[136:139], v[48:51]\n"
"ds_read_b64_tr_b16 v[172:173], v227 offset:12288\n"
"s_add_u32 s8, s8, s92\n"
"v_mfma_f32_16x16x32_bf16 v[52:55], v[152:155], a[132:135], v[52:55]\n"
"ds_read_b64_tr_b16 v[174:175], v227 offset:12544\n"
"s_add_u32 m0, s64, 65536\n"
"v_mfma_f32_16x16x32_bf16 v[52:55], v[156:159], a[140:143], v[52:55]\n"
"v_mfma_f32_16x16x32_bf16 v[64:67], v[152:155], v[16:19], v[64:67]\n"
"buffer_load_dwordx4 v201, s[8:11], 0 offen offset:0 lds\n"
"v_mfma_f32_16x16x32_bf16 v[64:67], v[156:159], v[24:27], v[64:67]\n"
"v_mfma_f32_16x16x32_bf16 v[68:71], v[152:155], v[20:23], v[68:71]\n"
"v_mfma_f32_16x16x32_bf16 v[68:71], v[156:159], v[28:31], v[68:71]\n"
"ds_read_b64_tr_b16 v[176:177], v228 offset:8192\n"
"s_waitcnt lgkmcnt(5)\n"
"v_mfma_f32_16x16x32_bf16 v[56:59], v[160:163], a[128:131], v[56:59]\n"
"ds_read_b64_tr_b16 v[178:179], v228 offset:8448\n"
"buffer_load_dwordx4 v202, s[8:11], 0 offen offset:1024 lds\n"
"v_mfma_f32_16x16x32_bf16 v[56:59], v[164:167], a[136:139], v[56:59]\n"
"ds_read_b64_tr_b16 v[180:181], v228 offset:12288\n"
"v_mfma_f32_16x16x32_bf16 v[60:63], v[160:163], a[132:135], v[60:63]\n"
"ds_read_b64_tr_b16 v[182:183], v228 offset:12544\n"
"v_mfma_f32_16x16x32_bf16 v[60:63], v[164:167], a[140:143], v[60:63]\n"
"v_mfma_f32_16x16x32_bf16 v[72:75], v[160:163], v[16:19], v[72:75]\n"
"s_add_u32 s4, s100, s65\n"
"s_add_u32 m0, s63, 114688\n"
"v_mfma_f32_16x16x32_bf16 v[72:75], v[164:167], v[24:27], v[72:75]\n"
"buffer_load_dwordx4 v8, s[4:7], 0 offen offset:0 lds\n"
"v_mfma_f32_16x16x32_bf16 v[76:79], v[160:163], v[20:23], v[76:79]\n"
"v_mfma_f32_16x16x32_bf16 v[76:79], v[164:167], v[28:31], v[76:79]\n"
"ds_read_b64_tr_b16 v[152:153], v227 offset:16384\n"
"s_waitcnt lgkmcnt(5)\n"
"v_mfma_f32_16x16x32_bf16 v[80:83], v[168:171], a[128:131], v[80:83]\n"
"ds_read_b64_tr_b16 v[154:155], v227 offset:16640\n"
"v_mfma_f32_16x16x32_bf16 v[80:83], v[172:175], a[136:139], v[80:83]\n"
"ds_read_b64_tr_b16 v[156:157], v227 offset:20480\n"
"buffer_load_dwordx4 v9, s[4:7], 0 offen offset:1024 lds\n"
"v_mfma_f32_16x16x32_bf16 v[84:87], v[168:171], a[132:135], v[84:87]\n"
"ds_read_b64_tr_b16 v[158:159], v227 offset:20736\n"
"v_mfma_f32_16x16x32_bf16 v[84:87], v[172:175], a[140:143], v[84:87]\n"
"v_mfma_f32_16x16x32_bf16 v[96:99], v[168:171], v[16:19], v[96:99]\n"
"v_mfma_f32_16x16x32_bf16 v[96:99], v[172:175], v[24:27], v[96:99]\n"
"v_mfma_f32_16x16x32_bf16 v[100:103], v[168:171], v[20:23], v[100:103]\n"
"buffer_load_dwordx4 v10, s[4:7], 0 offen offset:2048 lds\n"
"v_mfma_f32_16x16x32_bf16 v[100:103], v[172:175], v[28:31], v[100:103]\n"
"ds_read_b64_tr_b16 v[160:161], v228 offset:16384\n"
"s_waitcnt lgkmcnt(5)\n"
"v_mfma_f32_16x16x32_bf16 v[88:91], v[176:179], a[128:131], v[88:91]\n"
"ds_read_b64_tr_b16 v[162:163], v228 offset:16640\n"
"v_mfma_f32_16x16x32_bf16 v[88:91], v[180:183], a[136:139], v[88:91]\n"
"ds_read_b64_tr_b16 v[164:165], v228 offset:20480\n"
"v_mfma_f32_16x16x32_bf16 v[92:95], v[176:179], a[132:135], v[92:95]\n"
"ds_read_b64_tr_b16 v[166:167], v228 offset:20736\n"
"v_mfma_f32_16x16x32_bf16 v[92:95], v[180:183], a[140:143], v[92:95]\n"
"s_waitcnt vmcnt(9)\n"
"s_barrier\n"
"v_mfma_f32_16x16x32_bf16 v[104:107], v[176:179], v[16:19], v[104:107]\n"
"ds_read_b128 a[144:147], v225 offset:16384\n"
"buffer_load_dwordx4 v11, s[4:7], 0 offen offset:3072 lds\n"
"v_mfma_f32_16x16x32_bf16 v[104:107], v[180:183], v[24:27], v[104:107]\n"
"ds_read_b128 a[152:155], v226 offset:16384\n"
"s_add_u32 s65, s65, 128\n"
"v_mfma_f32_16x16x32_bf16 v[108:111], v[176:179], v[20:23], v[108:111]\n"
"ds_read_b128 a[148:151], v225 offset:18432\n"
"s_and_b32 s65, s65, 2047\n"
"v_mfma_f32_16x16x32_bf16 v[108:111], v[180:183], v[28:31], v[108:111]\n"
"ds_read_b128 a[156:159], v226 offset:18432\n"
"s_waitcnt lgkmcnt(8)\n"
"v_mfma_f32_16x16x32_bf16 v[112:115], v[152:155], a[128:131], v[112:115]\n"
"ds_read_b128 v[184:187], v32 offset:16384\n"
"s_mul_i32 s66, s65, 3584\n"
"v_mfma_f32_16x16x32_bf16 v[112:115], v[156:159], a[136:139], v[112:115]\n"
"ds_read_b128 v[192:195], v33 offset:16384\n"
"s_add_u32 s8, s101, s66\n"
"v_mfma_f32_16x16x32_bf16 v[116:119], v[152:155], a[132:135], v[116:119]\n"
"ds_read_b128 v[188:191], v32 offset:18432\n"
"s_add_u32 m0, s64, 73728\n"
"v_mfma_f32_16x16x32_bf16 v[116:119], v[156:159], a[140:143], v[116:119]\n"
"ds_read_b128 v[196:199], v33 offset:18432\n"
"buffer_load_dwordx4 v201, s[8:11], 0 offen offset:0 lds\n"
"v_mfma_f32_16x16x32_bf16 v[128:131], v[152:155], v[16:19], v[128:131]\n"
"ds_read_b64_tr_b16 v[168:169], v227 offset:24576\n"
"ds_read_b64_tr_b16 v[170:171], v227 offset:24832\n"
"v_mfma_f32_16x16x32_bf16 v[128:131], v[156:159], v[24:27], v[128:131]\n"
"ds_read_b64_tr_b16 v[172:173], v227 offset:28672\n"
"v_mfma_f32_16x16x32_bf16 v[132:135], v[152:155], v[20:23], v[132:135]\n"
"ds_read_b64_tr_b16 v[174:175], v227 offset:28928\n"
"v_mfma_f32_16x16x32_bf16 v[132:135], v[156:159], v[28:31], v[132:135]\n"
"ds_read_b64_tr_b16 v[176:177], v228 offset:24576\n"
"s_waitcnt lgkmcnt(13)\n"
"v_mfma_f32_16x16x32_bf16 v[120:123], v[160:163], a[128:131], v[120:123]\n"
"ds_read_b64_tr_b16 v[178:179], v228 offset:24832\n"
"buffer_load_dwordx4 v202, s[8:11], 0 offen offset:1024 lds\n"
"v_mfma_f32_16x16x32_bf16 v[120:123], v[164:167], a[136:139], v[120:123]\n"
"ds_read_b64_tr_b16 v[180:181], v228 offset:28672\n"
"s_add_u32 s8, s8, s1\n"
"v_mfma_f32_16x16x32_bf16 v[124:127], v[160:163], a[132:135], v[124:127]\n"
"ds_read_b64_tr_b16 v[182:183], v228 offset:28928\n"
"s_add_u32 m0, s64, 81920\n"
"v_mfma_f32_16x16x32_bf16 v[124:127], v[164:167], a[140:143], v[124:127]\n"
"s_waitcnt lgkmcnt(8)\n"
"s_barrier\n"
"v_mfma_f32_16x16x32_bf16 v[136:139], v[160:163], v[16:19], v[136:139]\n"
"buffer_load_dwordx4 v201, s[8:11], 0 offen offset:0 lds\n"
"v_mfma_f32_16x16x32_bf16 v[136:139], v[164:167], v[24:27], v[136:139]\n"
"v_mfma_f32_16x16x32_bf16 v[140:143], v[160:163], v[20:23], v[140:143]\n"
"v_mfma_f32_16x16x32_bf16 v[140:143], v[164:167], v[28:31], v[140:143]\n"
"ds_read_b64_tr_b16 v[152:153], v227 offset:32768\n"
"s_waitcnt lgkmcnt(5)\n"
"v_mfma_f32_16x16x32_bf16 v[48:51], v[168:171], a[144:147], v[48:51]\n"
"ds_read_b64_tr_b16 v[154:155], v227 offset:33024\n"
"buffer_load_dwordx4 v202, s[8:11], 0 offen offset:1024 lds\n"
"v_mfma_f32_16x16x32_bf16 v[48:51], v[172:175], a[152:155], v[48:51]\n"
"ds_read_b64_tr_b16 v[156:157], v227 offset:36864\n"
"s_add_u32 s8, s8, s92\n"
"v_mfma_f32_16x16x32_bf16 v[52:55], v[168:171], a[148:151], v[52:55]\n"
"ds_read_b64_tr_b16 v[158:159], v227 offset:37120\n"
"s_add_u32 m0, s64, 90112\n"
"v_mfma_f32_16x16x32_bf16 v[52:55], v[172:175], a[156:159], v[52:55]\n"
"v_mfma_f32_16x16x32_bf16 v[64:67], v[168:171], v[184:187], v[64:67]\n"
"buffer_load_dwordx4 v201, s[8:11], 0 offen offset:0 lds\n"
"v_mfma_f32_16x16x32_bf16 v[64:67], v[172:175], v[192:195], v[64:67]\n"
"v_mfma_f32_16x16x32_bf16 v[68:71], v[168:171], v[188:191], v[68:71]\n"
"v_mfma_f32_16x16x32_bf16 v[68:71], v[172:175], v[196:199], v[68:71]\n"
"ds_read_b64_tr_b16 v[160:161], v228 offset:32768\n"
"s_waitcnt lgkmcnt(5)\n"
"v_mfma_f32_16x16x32_bf16 v[56:59], v[176:179], a[144:147], v[56:59]\n"
"ds_read_b64_tr_b16 v[162:163], v228 offset:33024\n"
"buffer_load_dwordx4 v202, s[8:11], 0 offen offset:1024 lds\n"
"v_mfma_f32_16x16x32_bf16 v[56:59], v[180:183], a[152:155], v[56:59]\n"
"ds_read_b64_tr_b16 v[164:165], v228 offset:36864\n"
"v_mfma_f32_16x16x32_bf16 v[60:63], v[176:179], a[148:151], v[60:63]\n"
"ds_read_b64_tr_b16 v[166:167], v228 offset:37120\n"
"v_mfma_f32_16x16x32_bf16 v[60:63], v[180:183], a[156:159], v[60:63]\n"
"v_mfma_f32_16x16x32_bf16 v[72:75], v[176:179], v[184:187], v[72:75]\n"
"s_add_u32 s4, s100, s65\n"
"s_add_u32 m0, s63, 131072\n"
"v_mfma_f32_16x16x32_bf16 v[72:75], v[180:183], v[192:195], v[72:75]\n"
"buffer_load_dwordx4 v8, s[4:7], 0 offen offset:0 lds\n"
"v_mfma_f32_16x16x32_bf16 v[76:79], v[176:179], v[188:191], v[76:79]\n"
"v_mfma_f32_16x16x32_bf16 v[76:79], v[180:183], v[196:199], v[76:79]\n"
"ds_read_b64_tr_b16 v[168:169], v227 offset:40960\n"
"s_waitcnt lgkmcnt(5)\n"
"v_mfma_f32_16x16x32_bf16 v[80:83], v[152:155], a[144:147], v[80:83]\n"
"ds_read_b64_tr_b16 v[170:171], v227 offset:41216\n"
"v_mfma_f32_16x16x32_bf16 v[80:83], v[156:159], a[152:155], v[80:83]\n"
"ds_read_b64_tr_b16 v[172:173], v227 offset:45056\n"
"buffer_load_dwordx4 v9, s[4:7], 0 offen offset:1024 lds\n"
"v_mfma_f32_16x16x32_bf16 v[84:87], v[152:155], a[148:151], v[84:87]\n"
"ds_read_b64_tr_b16 v[174:175], v227 offset:45312\n"
"v_mfma_f32_16x16x32_bf16 v[84:87], v[156:159], a[156:159], v[84:87]\n"
"v_mfma_f32_16x16x32_bf16 v[96:99], v[152:155], v[184:187], v[96:99]\n"
"v_mfma_f32_16x16x32_bf16 v[96:99], v[156:159], v[192:195], v[96:99]\n"
"v_mfma_f32_16x16x32_bf16 v[100:103], v[152:155], v[188:191], v[100:103]\n"
"buffer_load_dwordx4 v10, s[4:7], 0 offen offset:2048 lds\n"
"v_mfma_f32_16x16x32_bf16 v[100:103], v[156:159], v[196:199], v[100:103]\n"
"ds_read_b64_tr_b16 v[176:177], v228 offset:40960\n"
"s_waitcnt lgkmcnt(5)\n"
"v_mfma_f32_16x16x32_bf16 v[88:91], v[160:163], a[144:147], v[88:91]\n"
"ds_read_b64_tr_b16 v[178:179], v228 offset:41216\n"
"v_mfma_f32_16x16x32_bf16 v[88:91], v[164:167], a[152:155], v[88:91]\n"
"ds_read_b64_tr_b16 v[180:181], v228 offset:45056\n"
"v_mfma_f32_16x16x32_bf16 v[92:95], v[160:163], a[148:151], v[92:95]\n"
"ds_read_b64_tr_b16 v[182:183], v228 offset:45312\n"
"v_mfma_f32_16x16x32_bf16 v[92:95], v[164:167], a[156:159], v[92:95]\n"
"s_waitcnt vmcnt(9)\n"
"s_barrier\n"
"v_mfma_f32_16x16x32_bf16 v[104:107], v[160:163], v[184:187], v[104:107]\n"
"ds_read_b128 a[160:163], v225 offset:0\n"
"buffer_load_dwordx4 v11, s[4:7], 0 offen offset:3072 lds\n"
"v_mfma_f32_16x16x32_bf16 v[104:107], v[164:167], v[192:195], v[104:107]\n"
"ds_read_b128 a[168:171], v226 offset:0\n"
"s_add_u32 s65, s65, 128\n"
"v_mfma_f32_16x16x32_bf16 v[108:111], v[160:163], v[188:191], v[108:111]\n"
"ds_read_b128 a[164:167], v225 offset:2048\n"
"s_and_b32 s65, s65, 2047\n"
"v_mfma_f32_16x16x32_bf16 v[108:111], v[164:167], v[196:199], v[108:111]\n"
"ds_read_b128 a[172:175], v226 offset:2048\n"
"s_waitcnt lgkmcnt(8)\n"
"v_mfma_f32_16x16x32_bf16 v[112:115], v[168:171], a[144:147], v[112:115]\n"
"ds_read_b128 v[16:19], v32 offset:0\n"
"s_mul_i32 s66, s65, 3584\n"
"v_mfma_f32_16x16x32_bf16 v[112:115], v[172:175], a[152:155], v[112:115]\n"
"ds_read_b128 v[24:27], v33 offset:0\n"
"s_add_u32 s8, s101, s66\n"
"v_mfma_f32_16x16x32_bf16 v[116:119], v[168:171], a[148:151], v[116:119]\n"
"ds_read_b128 v[20:23], v32 offset:2048\n"
".Lk_h2:\n"
"s_cbranch_execz .Lk_h6\n"
"s_add_u32 m0, s64, 0\n"
"v_mfma_f32_16x16x32_bf16 v[116:119], v[172:175], a[156:159], v[116:119]\n"
"ds_read_b128 v[28:31], v33 offset:2048\n"
"buffer_load_dwordx4 v201, s[8:11], 0 offen offset:0 lds\n"
"v_mfma_f32_16x16x32_bf16 v[128:131], v[168:171], v[184:187], v[128:131]\n"
"ds_read_b64_tr_b16 v[152:153], v227 offset:49152\n"
"ds_read_b64_tr_b16 v[154:155], v227 offset:49408\n"
"v_mfma_f32_16x16x32_bf16 v[128:131], v[172:175], v[192:195], v[128:131]\n"
"ds_read_b64_tr_b16 v[156:157], v227 offset:53248\n"
"v_mfma_f32_16x16x32_bf16 v[132:135], v[168:171], v[188:191], v[132:135]\n"
"ds_read_b64_tr_b16 v[158:159], v227 offset:53504\n"
"v_mfma_f32_16x16x32_bf16 v[132:135], v[172:175], v[196:199], v[132:135]\n"
"ds_read_b64_tr_b16 v[160:161], v228 offset:49152\n"
"s_waitcnt lgkmcnt(13)\n"
"v_mfma_f32_16x16x32_bf16 v[120:123], v[176:179], a[144:147], v[120:123]\n"
"ds_read_b64_tr_b16 v[162:163], v228 offset:49408\n"
"buffer_load_dwordx4 v202, s[8:11], 0 offen offset:1024 lds\n"
"v_mfma_f32_16x16x32_bf16 v[120:123], v[180:183], a[152:155], v[120:123]\n"
"ds_read_b64_tr_b16 v[164:165], v228 offset:53248\n"
"s_add_u32 s8, s8, s1\n"
"v_mfma_f32_16x16x32_bf16 v[124:127], v[176:179], a[148:151], v[124:127]\n"
"ds_read_b64_tr_b16 v[166:167], v228 offset:53504\n"
"s_add_u32 m0, s64, 8192\n"
"v_mfma_f32_16x16x32_bf16 v[124:127], v[180:183], a[156:159], v[124:127]\n"
"s_waitcnt lgkmcnt(8)\n"
"s_barrier\n"
"v_mfma_f32_16x16x32_bf16 v[136:139], v[176:179], v[184:187], v[136:139]\n"
"buffer_load_dwordx4 v201, s[8:11], 0 offen offset:0 lds\n"
"v_mfma_f32_16x16x32_bf16 v[136:139], v[180:183], v[192:195], v[136:139]\n"
"v_mfma_f32_16x16x32_bf16 v[140:143], v[176:179], v[188:191], v[140:143]\n"
"v_mfma_f32_16x16x32_bf16 v[140:143], v[180:183], v[196:199], v[140:143]\n"
"ds_read_b64_tr_b16 v[168:169], v227 offset:57344\n"
"s_waitcnt lgkmcnt(5)\n"
"v_mfma_f32_16x16x32_bf16 v[48:51], v[152:155], a[160:163], v[48:51]\n"
"ds_read_b64_tr_b16 v[170:171], v227 offset:57600\n"
"buffer_load_dwordx4 v202, s[8:11], 0 offen offset:1024 lds\n"
"v_mfma_f32_16x16x32_bf16 v[48:51], v[156:159], a[168:171], v[48:51]\n"
"ds_read_b64_tr_b16 v[172:173], v227 offset:61440\n"
"s_add_u32 s8, s8, s92\n"
"v_mfma_f32_16x16x32_bf16 v[52:55], v[152:155], a[164:167], v[52:55]\n"
"ds_read_b64_tr_b16 v[174:175], v227 offset:61696\n"
"s_add_u32 m0, s64, 16384\n"
"v_mfma_f32_16x16x32_bf16 v[52:55], v[156:159], a[172:175], v[52:55]\n"
"v_mfma_f32_16x16x32_bf16 v[64:67], v[152:155], v[16:19], v[64:67]\n"
"buffer_load_dwordx4 v201, s[8:11], 0 offen offset:0 lds\n"
"v_mfma_f32_16x16x32_bf16 v[64:67], v[156:159], v[24:27], v[64:67]\n"
"v_mfma_f32_16x16x32_bf16 v[68:71], v[152:155], v[20:23], v[68:71]\n"
"v_mfma_f32_16x16x32_bf16 v[68:71], v[156:159], v[28:31], v[68:71]\n"
"ds_read_b64_tr_b16 v[176:177], v228 offset:57344\n"
"s_waitcnt lgkmcnt(5)\n"
"v_mfma_f32_16x16x32_bf16 v[56:59], v[160:163], a[160:163], v[56:59]\n"
"ds_read_b64_tr_b16 v[178:179], v228 offset:57600\n"
"buffer_load_dwordx4 v202, s[8:11], 0 offen offset:1024 lds\n"
"v_mfma_f32_16x16x32_bf16 v[56:59], v[164:167], a[168:171], v[56:59]\n"
"ds_read_b64_tr_b16 v[180:181], v228 offset:61440\n"
"v_mfma_f32_16x16x32_bf16 v[60:63], v[160:163], a[164:167], v[60:63]\n"
"ds_read_b64_tr_b16 v[182:183], v228 offset:61696\n"
"v_mfma_f32_16x16x32_bf16 v[60:63], v[164:167], a[172:175], v[60:63]\n"
"v_mfma_f32_16x16x32_bf16 v[72:75], v[160:163], v[16:19], v[72:75]\n"
"s_add_u32 s4, s100, s65\n"
"s_add_u32 m0, s63, 114688\n"
"v_mfma_f32_16x16x32_bf16 v[72:75], v[164:167], v[24:27], v[72:75]\n"
"buffer_load_dwordx4 v8, s[4:7], 0 offen offset:0 lds\n"
"v_mfma_f32_16x16x32_bf16 v[76:79], v[160:163], v[20:23], v[76:79]\n"
"v_mfma_f32_16x16x32_bf16 v[76:79], v[164:167], v[28:31], v[76:79]\n"
"ds_read_b64_tr_b16 v[152:153], v229 offset:0\n"
"s_waitcnt lgkmcnt(5)\n"
"v_mfma_f32_16x16x32_bf16 v[80:83], v[168:171], a[160:163], v[80:83]\n"
"ds_read_b64_tr_b16 v[154:155], v229 offset:256\n"
"v_mfma_f32_16x16x32_bf16 v[80:83], v[172:175], a[168:171], v[80:83]\n"
"ds_read_b64_tr_b16 v[156:157], v229 offset:4096\n"
"buffer_load_dwordx4 v9, s[4:7], 0 offen offset:1024 lds\n"
"v_mfma_f32_16x16x32_bf16 v[84:87], v[168:171], a[164:167], v[84:87]\n"
"ds_read_b64_tr_b16 v[158:159], v229 offset:4352\n"
"v_mfma_f32_16x16x32_bf16 v[84:87], v[172:175], a[172:175], v[84:87]\n"
"v_mfma_f32_16x16x32_bf16 v[96:99], v[168:171], v[16:19], v[96:99]\n"
"v_mfma_f32_16x16x32_bf16 v[96:99], v[172:175], v[24:27], v[96:99]\n"
"v_mfma_f32_16x16x32_bf16 v[100:103], v[168:171], v[20:23], v[100:103]\n"
"buffer_load_dwordx4 v10, s[4:7], 0 offen offset:2048 lds\n"
"v_mfma_f32_16x16x32_bf16 v[100:103], v[172:175], v[28:31], v[100:103]\n"
"ds_read_b64_tr_b16 v[160:161], v230 offset:0\n"
"s_waitcnt lgkmcnt(5)\n"
"v_mfma_f32_16x16x32_bf16 v[88:91], v[176:179], a[160:163], v[88:91]\n"
"ds_read_b64_tr_b16 v[162:163], v230 offset:256\n"
"v_mfma_f32_16x16x32_bf16 v[88:91], v[180:183], a[168:171], v[88:91]\n"
"ds_read_b64_tr_b16 v[164:165], v230 offset:4096\n"
"v_mfma_f32_16x16x32_bf16 v[92:95], v[176:179], a[164:167], v[92:95]\n"
"ds_read_b64_tr_b16 v[166:167], v230 offset:4352\n"
"v_mfma_f32_16x16x32_bf16 v[92:95], v[180:183], a[172:175], v[92:95]\n"
"s_waitcnt vmcnt(9)\n"
"s_barrier\n"
"v_mfma_f32_16x16x32_bf16 v[104:107], v[176:179], v[16:19], v[104:107]\n"
"ds_read_b128 a[176:179], v225 offset:16384\n"
"buffer_load_dwordx4 v11, s[4:7], 0 offen offset:3072 lds\n"
"v_mfma_f32_16x16x32_bf16 v[104:107], v[180:183], v[24:27], v[104:107]\n"
"ds_read_b128 a[184:187], v226 offset:16384\n"
"s_add_u32 s65, s65, 128\n"
"v_mfma_f32_16x16x32_bf16 v[108:111], v[176:179], v[20:23], v[108:111]\n"
"ds_read_b128 a[180:183], v225 offset:18432\n"
"s_and_b32 s65, s65, 2047\n"
"v_mfma_f32_16x16x32_bf16 v[108:111], v[180:183], v[28:31], v[108:111]\n"
"ds_read_b128 a[188:191], v226 offset:18432\n"
"s_waitcnt lgkmcnt(8)\n"
"v_mfma_f32_16x16x32_bf16 v[112:115], v[152:155], a[160:163], v[112:115]\n"
"ds_read_b128 v[184:187], v32 offset:16384\n"
"s_mul_i32 s66, s65, 3584\n"
"v_mfma_f32_16x16x32_bf16 v[112:115], v[156:159], a[168:171], v[112:115]\n"
"ds_read_b128 v[192:195], v33 offset:16384\n"
"s_add_u32 s8, s101, s66\n"
"v_mfma_f32_16x16x32_bf16 v[116:119], v[152:155], a[164:167], v[116:119]\n"
"ds_read_b128 v[188:191], v32 offset:18432\n"
"s_add_u32 m0, s64, 24576\n"
"v_mfma_f32_16x16x32_bf16 v[116:119], v[156:159], a[172:175], v[116:119]\n"
"ds_read_b128 v[196:199], v33 offset:18432\n"
"buffer_load_dwordx4 v201, s[8:11], 0 offen offset:0 lds\n"
"v_mfma_f32_16x16x32_bf16 v[128:131], v[152:155], v[16:19], v[128:131]\n"
"ds_read_b64_tr_b16 v[168:169], v229 offset:8192\n"
"ds_read_b64_tr_b16 v[170:171], v229 offset:8448\n"
"v_mfma_f32_16x16x32_bf16 v[128:131], v[156:159], v[24:27], v[128:131]\n"
"ds_read_b64_tr_b16 v[172:173], v229 offset:12288\n"
"v_mfma_f32_16x16x32_bf16 v[132:135], v[152:155], v[20:23], v[132:135]\n"
"ds_read_b64_tr_b16 v[174:175], v229 offset:12544\n"
"v_mfma_f32_16x16x32_bf16 v[132:135], v[156:159], v[28:31], v[132:135]\n"
"ds_read_b64_tr_b16 v[176:177], v230 offset:8192\n"
"s_waitcnt lgkmcnt(13)\n"
"v_mfma_f32_16x16x32_bf16 v[120:123], v[160:163], a[160:163], v[120:123]\n"
"ds_read_b64_tr_b16 v[178:179], v230 offset:8448\n"
"buffer_load_dwordx4 v202, s[8:11], 0 offen offset:1024 lds\n"
"v_mfma_f32_16x16x32_bf16 v[120:123], v[164:167], a[168:171], v[120:123]\n"
"ds_read_b64_tr_b16 v[180:181], v230 offset:12288\n"
"s_add_u32 s8, s8, s1\n"
"v_mfma_f32_16x16x32_bf16 v[124:127], v[160:163], a[164:167], v[124:127]\n"
"ds_read_b64_tr_b16 v[182:183], v230 offset:12544\n"
"s_add_u32 m0, s64, 32768\n"
"v_mfma_f32_16x16x32_bf16 v[124:127], v[164:167], a[172:175], v[124:127]\n"
"s_waitcnt lgkmcnt(8)\n"
"s_barrier\n"
"v_mfma_f32_16x16x32_bf16 v[136:139], v[160:163], v[16:19], v[136:139]\n"
"buffer_load_dwordx4 v201, s[8:11], 0 offen offset:0 lds\n"
"v_mfma_f32_16x16x32_bf16 v[136:139], v[164:167], v[24:27], v[136:139]\n"
"v_mfma_f32_16x16x32_bf16 v[140:143], v[160:163], v[20:23], v[140:143]\n"
"v_mfma_f32_16x16x32_bf16 v[140:143], v[164:167], v[28:31], v[140:143]\n"
"ds_read_b64_tr_b16 v[152:153], v229 offset:16384\n"
"s_waitcnt lgkmcnt(5)\n"
"v_mfma_f32_16x16x32_bf16 v[48:51], v[168:171], a[176:179], v[48:51]\n"
"ds_read_b64_tr_b16 v[154:155], v229 offset:16640\n"
"buffer_load_dwordx4 v202, s[8:11], 0 offen offset:1024 lds\n"
"v_mfma_f32_16x16x32_bf16 v[48:51], v[172:175], a[184:187], v[48:51]\n"
"ds_read_b64_tr_b16 v[156:157], v229 offset:20480\n"
"s_add_u32 s8, s8, s92\n"
"v_mfma_f32_16x16x32_bf16 v[52:55], v[168:171], a[180:183], v[52:55]\n"
"ds_read_b64_tr_b16 v[158:159], v229 offset:20736\n"
"s_add_u32 m0, s64, 40960\n"
"v_mfma_f32_16x16x32_bf16 v[52:55], v[172:175], a[188:191], v[52:55]\n"
"v_mfma_f32_16x16x32_bf16 v[64:67], v[168:171], v[184:187], v[64:67]\n"
"buffer_load_dwordx4 v201, s[8:11], 0 offen offset:0 lds\n"
"v_mfma_f32_16x16x32_bf16 v[64:67], v[172:175], v[192:195], v[64:67]\n"
"v_mfma_f32_16x16x32_bf16 v[68:71], v[168:171], v[188:191], v[68:71]\n"
"v_mfma_f32_16x16x32_bf16 v[68:71], v[172:175], v[196:199], v[68:71]\n"
"ds_read_b64_tr_b16 v[160:161], v230 offset:16384\n"
"s_waitcnt lgkmcnt(5)\n"
"v_mfma_f32_16x16x32_bf16 v[56:59], v[176:179], a[176:179], v[56:59]\n"
"ds_read_b64_tr_b16 v[162:163], v230 offset:16640\n"
"buffer_load_dwordx4 v202, s[8:11], 0 offen offset:1024 lds\n"
"v_mfma_f32_16x16x32_bf16 v[56:59], v[180:183], a[184:187], v[56:59]\n"
"ds_read_b64_tr_b16 v[164:165], v230 offset:20480\n"
"v_mfma_f32_16x16x32_bf16 v[60:63], v[176:179], a[180:183], v[60:63]\n"
"ds_read_b64_tr_b16 v[166:167], v230 offset:20736\n"
"v_mfma_f32_16x16x32_bf16 v[60:63], v[180:183], a[188:191], v[60:63]\n"
"v_mfma_f32_16x16x32_bf16 v[72:75], v[176:179], v[184:187], v[72:75]\n"
"s_add_u32 s4, s100, s65\n"
"s_add_u32 m0, s63, 131072\n"
"v_mfma_f32_16x16x32_bf16 v[72:75], v[180:183], v[192:195], v[72:75]\n"
"buffer_load_dwordx4 v8, s[4:7], 0 offen offset:0 lds\n"
"v_mfma_f32_16x16x32_bf16 v[76:79], v[176:179], v[188:191], v[76:79]\n"
"v_mfma_f32_16x16x32_bf16 v[76:79], v[180:183], v[196:199], v[76:79]\n"
"ds_read_b64_tr_b16 v[168:169], v229 offset:24576\n"
"s_waitcnt lgkmcnt(5)\n"
"v_mfma_f32_16x16x32_bf16 v[80:83], v[152:155], a[176:179], v[80:83]\n"
"ds_read_b64_tr_b16 v[170:171], v229 offset:24832\n"
"v_mfma_f32_16x16x32_bf16 v[80:83], v[156:159], a[184:187], v[80:83]\n"
"ds_read_b64_tr_b16 v[172:173], v229 offset:28672\n"
"buffer_load_dwordx4 v9, s[4:7], 0 offen offset:1024 lds\n"
"v_mfma_f32_16x16x32_bf16 v[84:87], v[152:155], a[180:183], v[84:87]\n"
"ds_read_b64_tr_b16 v[174:175], v229 offset:28928\n"
"v_mfma_f32_16x16x32_bf16 v[84:87], v[156:159], a[188:191], v[84:87]\n"
"v_mfma_f32_16x16x32_bf16 v[96:99], v[152:155], v[184:187], v[96:99]\n"
"v_mfma_f32_16x16x32_bf16 v[96:99], v[156:159], v[192:195], v[96:99]\n"
"v_mfma_f32_16x16x32_bf16 v[100:103], v[152:155], v[188:191], v[100:103]\n"
"buffer_load_dwordx4 v10, s[4:7], 0 offen offset:2048 lds\n"
"v_mfma_f32_16x16x32_bf16 v[100:103], v[156:159], v[196:199], v[100:103]\n"
"ds_read_b64_tr_b16 v[176:177], v230 offset:24576\n"
"s_waitcnt lgkmcnt(5)\n"
"v_mfma_f32_16x16x32_bf16 v[88:91], v[160:163], a[176:179], v[88:91]\n"
"ds_read_b64_tr_b16 v[178:179], v230 offset:24832\n"
"v_mfma_f32_16x16x32_bf16 v[88:91], v[164:167], a[184:187], v[88:91]\n"
"ds_read_b64_tr_b16 v[180:181], v230 offset:28672\n"
"v_mfma_f32_16x16x32_bf16 v[92:95], v[160:163], a[180:183], v[92:95]\n"
"ds_read_b64_tr_b16 v[182:183], v230 offset:28928\n"
"v_mfma_f32_16x16x32_bf16 v[92:95], v[164:167], a[188:191], v[92:95]\n"
"s_waitcnt vmcnt(9)\n"
"s_barrier\n"
"v_mfma_f32_16x16x32_bf16 v[104:107], v[160:163], v[184:187], v[104:107]\n"
"ds_read_b128 a[192:195], v225 offset:0\n"
"buffer_load_dwordx4 v11, s[4:7], 0 offen offset:3072 lds\n"
"v_mfma_f32_16x16x32_bf16 v[104:107], v[164:167], v[192:195], v[104:107]\n"
"ds_read_b128 a[200:203], v226 offset:0\n"
"s_add_u32 s65, s65, 128\n"
"v_mfma_f32_16x16x32_bf16 v[108:111], v[160:163], v[188:191], v[108:111]\n"
"ds_read_b128 a[196:199], v225 offset:2048\n"
"s_and_b32 s65, s65, 2047\n"
"v_mfma_f32_16x16x32_bf16 v[108:111], v[164:167], v[196:199], v[108:111]\n"
"ds_read_b128 a[204:207], v226 offset:2048\n"
"s_waitcnt lgkmcnt(8)\n"
"v_mfma_f32_16x16x32_bf16 v[112:115], v[168:171], a[176:179], v[112:115]\n"
"ds_read_b128 v[16:19], v32 offset:0\n"
"s_mul_i32 s66, s65, 3584\n"
"v_mfma_f32_16x16x32_bf16 v[112:115], v[172:175], a[184:187], v[112:115]\n"
"ds_read_b128 v[24:27], v33 offset:0\n"
"s_add_u32 s8, s101, s66\n"
"v_mfma_f32_16x16x32_bf16 v[116:119], v[168:171], a[180:183], v[116:119]\n"
"ds_read_b128 v[20:23], v32 offset:2048\n"
"s_add_u32 m0, s64, 49152\n"
"v_mfma_f32_16x16x32_bf16 v[116:119], v[172:175], a[188:191], v[116:119]\n"
"ds_read_b128 v[28:31], v33 offset:2048\n"
"buffer_load_dwordx4 v201, s[8:11], 0 offen offset:0 lds\n"
"v_mfma_f32_16x16x32_bf16 v[128:131], v[168:171], v[184:187], v[128:131]\n"
"ds_read_b64_tr_b16 v[152:153], v227 offset:0\n"
"ds_read_b64_tr_b16 v[154:155], v227 offset:256\n"
"v_mfma_f32_16x16x32_bf16 v[128:131], v[172:175], v[192:195], v[128:131]\n"
"ds_read_b64_tr_b16 v[156:157], v227 offset:4096\n"
"v_mfma_f32_16x16x32_bf16 v[132:135], v[168:171], v[188:191], v[132:135]\n"
"ds_read_b64_tr_b16 v[158:159], v227 offset:4352\n"
"v_mfma_f32_16x16x32_bf16 v[132:135], v[172:175], v[196:199], v[132:135]\n"
"ds_read_b64_tr_b16 v[160:161], v228 offset:0\n"
"s_waitcnt lgkmcnt(13)\n"
"v_mfma_f32_16x16x32_bf16 v[120:123], v[176:179], a[176:179], v[120:123]\n"
"ds_read_b64_tr_b16 v[162:163], v228 offset:256\n"
"buffer_load_dwordx4 v202, s[8:11], 0 offen offset:1024 lds\n"
"v_mfma_f32_16x16x32_bf16 v[120:123], v[180:183], a[184:187], v[120:123]\n"
"ds_read_b64_tr_b16 v[164:165], v228 offset:4096\n"
"s_add_u32 s8, s8, s1\n"
"v_mfma_f32_16x16x32_bf16 v[124:127], v[176:179], a[180:183], v[124:127]\n"
"ds_read_b64_tr_b16 v[166:167], v228 offset:4352\n"
"s_add_u32 m0, s64, 57344\n"
"v_mfma_f32_16x16x32_bf16 v[124:127], v[180:183], a[188:191], v[124:127]\n"
"s_waitcnt lgkmcnt(8)\n"
"s_barrier\n"
"v_mfma_f32_16x16x32_bf16 v[136:139], v[176:179], v[184:187], v[136:139]\n"
"buffer_load_dwordx4 v201, s[8:11], 0 offen offset:0 lds\n"
"v_mfma_f32_16x16x32_bf16 v[136:139], v[180:183], v[192:195], v[136:139]\n"
"v_mfma_f32_16x16x32_bf16 v[140:143], v[176:179], v[188:191], v[140:143]\n"
"v_mfma_f32_16x16x32_bf16 v[140:143], v[180:183], v[196:199], v[140:143]\n"
"ds_read_b64_tr_b16 v[168:169], v227 offset:8192\n"
"s_waitcnt lgkmcnt(5)\n"
"v_mfma_f32_16x16x32_bf16 v[48:51], v[152:155], a[192:195], v[48:51]\n"
"ds_read_b64_tr_b16 v[170:171], v227 offset:8448\n"
"buffer_load_dwordx4 v202, s[8:11], 0 offen offset:1024 lds\n"
"v_mfma_f32_16x16x32_bf16 v[48:51], v[156:159], a[200:203], v[48:51]\n"
"ds_read_b64_tr_b16 v[172:173], v227 offset:12288\n"
"s_add_u32 s8, s8, s92\n"
"v_mfma_f32_16x16x32_bf16 v[52:55], v[152:155], a[196:199], v[52:55]\n"
"ds_read_b64_tr_b16 v[174:175], v227 offset:12544\n"
"s_add_u32 m0, s64, 65536\n"
"v_mfma_f32_16x16x32_bf16 v[52:55], v[156:159], a[204:207], v[52:55]\n"
"v_mfma_f32_16x16x32_bf16 v[64:67], v[152:155], v[16:19], v[64:67]\n"
"buffer_load_dwordx4 v201, s[8:11], 0 offen offset:0 lds\n"
"v_mfma_f32_16x16x32_bf16 v[64:67], v[156:159], v[24:27], v[64:67]\n"
"v_mfma_f32_16x16x32_bf16 v[68:71], v[152:155], v[20:23], v[68:71]\n"
"v_mfma_f32_16x16x32_bf16 v[68:71], v[156:159], v[28:31], v[68:71]\n"
"ds_read_b64_tr_b16 v[176:177], v228 offset:8192\n"
"s_waitcnt lgkmcnt(5)\n"
"v_mfma_f32_16x16x32_bf16 v[56:59], v[160:163], a[192:195], v[56:59]\n"
"ds_read_b64_tr_b16 v[178:179], v228 offset:8448\n"
"buffer_load_dwordx4 v202, s[8:11], 0 offen offset:1024 lds\n"
"v_mfma_f32_16x16x32_bf16 v[56:59], v[164:167], a[200:203], v[56:59]\n"
"ds_read_b64_tr_b16 v[180:181], v228 offset:12288\n"
"v_mfma_f32_16x16x32_bf16 v[60:63], v[160:163], a[196:199], v[60:63]\n"
"ds_read_b64_tr_b16 v[182:183], v228 offset:12544\n"
"v_mfma_f32_16x16x32_bf16 v[60:63], v[164:167], a[204:207], v[60:63]\n"
"v_mfma_f32_16x16x32_bf16 v[72:75], v[160:163], v[16:19], v[72:75]\n"
"s_add_u32 s4, s100, s65\n"
"s_add_u32 m0, s63, 114688\n"
"v_mfma_f32_16x16x32_bf16 v[72:75], v[164:167], v[24:27], v[72:75]\n"
"buffer_load_dwordx4 v8, s[4:7], 0 offen offset:0 lds\n"
"v_mfma_f32_16x16x32_bf16 v[76:79], v[160:163], v[20:23], v[76:79]\n"
"v_mfma_f32_16x16x32_bf16 v[76:79], v[164:167], v[28:31], v[76:79]\n"
"ds_read_b64_tr_b16 v[152:153], v227 offset:16384\n"
"s_waitcnt lgkmcnt(5)\n"
"v_mfma_f32_16x16x32_bf16 v[80:83], v[168:171], a[192:195], v[80:83]\n"
"ds_read_b64_tr_b16 v[154:155], v227 offset:16640\n"
"v_mfma_f32_16x16x32_bf16 v[80:83], v[172:175], a[200:203], v[80:83]\n"
"ds_read_b64_tr_b16 v[156:157], v227 offset:20480\n"
"buffer_load_dwordx4 v9, s[4:7], 0 offen offset:1024 lds\n"
"v_mfma_f32_16x16x32_bf16 v[84:87], v[168:171], a[196:199], v[84:87]\n"
"ds_read_b64_tr_b16 v[158:159], v227 offset:20736\n"
"v_mfma_f32_16x16x32_bf16 v[84:87], v[172:175], a[204:207], v[84:87]\n"
"v_mfma_f32_16x16x32_bf16 v[96:99], v[168:171], v[16:19], v[96:99]\n"
"v_mfma_f32_16x16x32_bf16 v[96:99], v[172:175], v[24:27], v[96:99]\n"
"v_mfma_f32_16x16x32_bf16 v[100:103], v[168:171], v[20:23], v[100:103]\n"
"buffer_load_dwordx4 v10, s[4:7], 0 offen offset:2048 lds\n"
"v_mfma_f32_16x16x32_bf16 v[100:103], v[172:175], v[28:31], v[100:103]\n"
"ds_read_b64_tr_b16 v[160:161], v228 offset:16384\n"
"s_waitcnt lgkmcnt(5)\n"
"v_mfma_f32_16x16x32_bf16 v[88:91], v[176:179], a[192:195], v[88:91]\n"
"ds_read_b64_tr_b16 v[162:163], v228 offset:16640\n"
"v_mfma_f32_16x16x32_bf16 v[88:91], v[180:183], a[200:203], v[88:91]\n"
"ds_read_b64_tr_b16 v[164:165], v228 offset:20480\n"
"v_mfma_f32_16x16x32_bf16 v[92:95], v[176:179], a[196:199], v[92:95]\n"
"ds_read_b64_tr_b16 v[166:167], v228 offset:20736\n"
"v_mfma_f32_16x16x32_bf16 v[92:95], v[180:183], a[204:207], v[92:95]\n"
"s_waitcnt vmcnt(9)\n"
"s_barrier\n"
"v_mfma_f32_16x16x32_bf16 v[104:107], v[176:179], v[16:19], v[104:107]\n"
"ds_read_b128 a[208:211], v225 offset:16384\n"
"buffer_load_dwordx4 v11, s[4:7], 0 offen offset:3072 lds\n"
"v_mfma_f32_16x16x32_bf16 v[104:107], v[180:183], v[24:27], v[104:107]\n"
"ds_read_b128 a[216:219], v226 offset:16384\n"
"s_add_u32 s65, s65, 128\n"
"v_mfma_f32_16x16x32_bf16 v[108:111], v[176:179], v[20:23], v[108:111]\n"
"ds_read_b128 a[212:215], v225 offset:18432\n"
"s_and_b32 s65, s65, 2047\n"
"v_mfma_f32_16x16x32_bf16 v[108:111], v[180:183], v[28:31], v[108:111]\n"
"ds_read_b128 a[220:223], v226 offset:18432\n"
"s_waitcnt lgkmcnt(8)\n"
"v_mfma_f32_16x16x32_bf16 v[112:115], v[152:155], a[192:195], v[112:115]\n"
"ds_read_b128 v[184:187], v32 offset:16384\n"
"s_mul_i32 s66, s65, 3584\n"
"v_mfma_f32_16x16x32_bf16 v[112:115], v[156:159], a[200:203], v[112:115]\n"
"ds_read_b128 v[192:195], v33 offset:16384\n"
"s_add_u32 s8, s101, s66\n"
"v_mfma_f32_16x16x32_bf16 v[116:119], v[152:155], a[196:199], v[116:119]\n"
"ds_read_b128 v[188:191], v32 offset:18432\n"
"s_add_u32 m0, s64, 73728\n"
"v_mfma_f32_16x16x32_bf16 v[116:119], v[156:159], a[204:207], v[116:119]\n"
"ds_read_b128 v[196:199], v33 offset:18432\n"
"buffer_load_dwordx4 v201, s[8:11], 0 offen offset:0 lds\n"
"v_mfma_f32_16x16x32_bf16 v[128:131], v[152:155], v[16:19], v[128:131]\n"
"ds_read_b64_tr_b16 v[168:169], v227 offset:24576\n"
"ds_read_b64_tr_b16 v[170:171], v227 offset:24832\n"
"v_mfma_f32_16x16x32_bf16 v[128:131], v[156:159], v[24:27], v[128:131]\n"
"ds_read_b64_tr_b16 v[172:173], v227 offset:28672\n"
"v_mfma_f32_16x16x32_bf16 v[132:135], v[152:155], v[20:23], v[132:135]\n"
"ds_read_b64_tr_b16 v[174:175], v227 offset:28928\n"
"v_mfma_f32_16x16x32_bf16 v[132:135], v[156:159], v[28:31], v[132:135]\n"
"ds_read_b64_tr_b16 v[176:177], v228 offset:24576\n"
"s_waitcnt lgkmcnt(13)\n"
"v_mfma_f32_16x16x32_bf16 v[120:123], v[160:163], a[192:195], v[120:123]\n"
"ds_read_b64_tr_b16 v[178:179], v228 offset:24832\n"
"buffer_load_dwordx4 v202, s[8:11], 0 offen offset:1024 lds\n"
"v_mfma_f32_16x16x32_bf16 v[120:123], v[164:167], a[200:203], v[120:123]\n"
"ds_read_b64_tr_b16 v[180:181], v228 offset:28672\n"
"s_add_u32 s8, s8, s1\n"
"v_mfma_f32_16x16x32_bf16 v[124:127], v[160:163], a[196:199], v[124:127]\n"
"ds_read_b64_tr_b16 v[182:183], v228 offset:28928\n"
"s_add_u32 m0, s64, 81920\n"
"v_mfma_f32_16x16x32_bf16 v[124:127], v[164:167], a[204:207], v[124:127]\n"
"s_waitcnt lgkmcnt(8)\n"
"s_barrier\n"
"v_mfma_f32_16x16x32_bf16 v[136:139], v[160:163], v[16:19], v[136:139]\n"
"buffer_load_dwordx4 v201, s[8:11], 0 offen offset:0 lds\n"
"s_and_b32 s1, s54, 1\n"
"v_mfma_f32_16x16x32_bf16 v[136:139], v[164:167], v[24:27], v[136:139]\n"
"s_mul_i32 s91, s1, -720896\n"
"s_add_u32 s91, s91, 360448\n"
"v_mfma_f32_16x16x32_bf16 v[140:143], v[160:163], v[20:23], v[140:143]\n"
"v_add_u32 v206, s91, v200\n"
"s_lshl_b32 s91, s1, 10\n"
"v_mfma_f32_16x16x32_bf16 v[140:143], v[164:167], v[28:31], v[140:143]\n"
"ds_read_b64_tr_b16 v[152:153], v227 offset:32768\n"
"s_waitcnt lgkmcnt(5)\n"
"v_mfma_f32_16x16x32_bf16 v[48:51], v[168:171], a[208:211], v[48:51]\n"
"ds_read_b64_tr_b16 v[154:155], v227 offset:33024\n"
"buffer_load_dwordx4 v202, s[8:11], 0 offen offset:1024 lds\n"
"v_mfma_f32_16x16x32_bf16 v[48:51], v[172:175], a[216:219], v[48:51]\n"
"ds_read_b64_tr_b16 v[156:157], v227 offset:36864\n"
"s_add_u32 s8, s8, s92\n"
"v_mfma_f32_16x16x32_bf16 v[52:55], v[168:171], a[212:215], v[52:55]\n"
"ds_read_b64_tr_b16 v[158:159], v227 offset:37120\n"
"s_add_u32 m0, s64, 90112\n"
"v_mfma_f32_16x16x32_bf16 v[52:55], v[172:175], a[220:223], v[52:55]\n"
"v_mfma_f32_16x16x32_bf16 v[64:67], v[168:171], v[184:187], v[64:67]\n"
"buffer_load_dwordx4 v201, s[8:11], 0 offen offset:0 lds\n"
"v_mfma_f32_16x16x32_bf16 v[64:67], v[172:175], v[192:195], v[64:67]\n"
"v_mfma_f32_16x16x32_bf16 v[68:71], v[168:171], v[188:191], v[68:71]\n"
"v_mfma_f32_16x16x32_bf16 v[68:71], v[172:175], v[196:199], v[68:71]\n"
"ds_read_b64_tr_b16 v[160:161], v228 offset:32768\n"
"s_waitcnt lgkmcnt(5)\n"
"v_mfma_f32_16x16x32_bf16 v[56:59], v[176:179], a[208:211], v[56:59]\n"
"ds_read_b64_tr_b16 v[162:163], v228 offset:33024\n"
"buffer_load_dwordx4 v202, s[8:11], 0 offen offset:1024 lds\n"
"v_mfma_f32_16x16x32_bf16 v[56:59], v[180:183], a[216:219], v[56:59]\n"
"ds_read_b64_tr_b16 v[164:165], v228 offset:36864\n"
"v_mfma_f32_16x16x32_bf16 v[60:63], v[176:179], a[212:215], v[60:63]\n"
"ds_read_b64_tr_b16 v[166:167], v228 offset:37120\n"
"v_mfma_f32_16x16x32_bf16 v[60:63], v[180:183], a[220:223], v[60:63]\n"
"v_mfma_f32_16x16x32_bf16 v[72:75], v[176:179], v[184:187], v[72:75]\n"
"s_add_u32 s4, s100, s65\n"
"s_add_u32 m0, s63, 131072\n"
"v_mfma_f32_16x16x32_bf16 v[72:75], v[180:183], v[192:195], v[72:75]\n"
"buffer_load_dwordx4 v8, s[4:7], 0 offen offset:0 lds\n"
"v_mfma_f32_16x16x32_bf16 v[76:79], v[176:179], v[188:191], v[76:79]\n"
"v_mfma_f32_16x16x32_bf16 v[76:79], v[180:183], v[196:199], v[76:79]\n"
"ds_read_b64_tr_b16 v[168:169], v227 offset:40960\n"
"s_waitcnt lgkmcnt(5)\n"
"v_mfma_f32_16x16x32_bf16 v[80:83], v[152:155], a[208:211], v[80:83]\n"
"ds_read_b64_tr_b16 v[170:171], v227 offset:41216\n"
"v_mfma_f32_16x16x32_bf16 v[80:83], v[156:159], a[216:219], v[80:83]\n"
"ds_read_b64_tr_b16 v[172:173], v227 offset:45056\n"
"buffer_load_dwordx4 v9, s[4:7], 0 offen offset:1024 lds\n"
"v_mfma_f32_16x16x32_bf16 v[84:87], v[152:155], a[212:215], v[84:87]\n"
"ds_read_b64_tr_b16 v[174:175], v227 offset:45312\n"
"v_mfma_f32_16x16x32_bf16 v[84:87], v[156:159], a[220:223], v[84:87]\n"
"v_mfma_f32_16x16x32_bf16 v[96:99], v[152:155], v[184:187], v[96:99]\n"
"v_mfma_f32_16x16x32_bf16 v[96:99], v[156:159], v[192:195], v[96:99]\n"
"v_mfma_f32_16x16x32_bf16 v[100:103], v[152:155], v[188:191], v[100:103]\n"
"buffer_load_dwordx4 v10, s[4:7], 0 offen offset:2048 lds\n"
"v_mfma_f32_16x16x32_bf16 v[100:103], v[156:159], v[196:199], v[100:103]\n"
"ds_read_b64_tr_b16 v[176:177], v228 offset:40960\n"
"s_waitcnt lgkmcnt(5)\n"
"v_mfma_f32_16x16x32_bf16 v[88:91], v[160:163], a[208:211], v[88:91]\n"
"ds_read_b64_tr_b16 v[178:179], v228 offset:41216\n"
"v_mfma_f32_16x16x32_bf16 v[88:91], v[164:167], a[216:219], v[88:91]\n"
"ds_read_b64_tr_b16 v[180:181], v228 offset:45056\n"
"v_mfma_f32_16x16x32_bf16 v[92:95], v[160:163], a[212:215], v[92:95]\n"
"ds_read_b64_tr_b16 v[182:183], v228 offset:45312\n"
"v_mfma_f32_16x16x32_bf16 v[92:95], v[164:167], a[220:223], v[92:95]\n"
"s_waitcnt vmcnt(9)\n"
"s_barrier\n"
"v_mfma_f32_16x16x32_bf16 v[104:107], v[160:163], v[184:187], v[104:107]\n"
"ds_read_b128 a[224:227], v225 offset:0\n"
"buffer_load_dwordx4 v11, s[4:7], 0 offen offset:3072 lds\n"
"v_mfma_f32_16x16x32_bf16 v[104:107], v[164:167], v[192:195], v[104:107]\n"
"ds_read_b128 a[232:235], v226 offset:0\n"
"s_and_b32 s65, s54, 2\n"
"v_mfma_f32_16x16x32_bf16 v[108:111], v[160:163], v[188:191], v[108:111]\n"
"ds_read_b128 a[228:231], v225 offset:2048\n"
"s_add_u32 s65, s65, s60\n"
"v_mfma_f32_16x16x32_bf16 v[108:111], v[164:167], v[196:199], v[108:111]\n"
"ds_read_b128 a[236:239], v226 offset:2048\n"
"s_waitcnt lgkmcnt(8)\n"
"v_mfma_f32_16x16x32_bf16 v[112:115], v[168:171], a[208:211], v[112:115]\n"
"ds_read_b128 v[16:19], v32 offset:0\n"
"s_lshl_b32 s65, s65, 7\n"
"v_mfma_f32_16x16x32_bf16 v[112:115], v[172:175], a[216:219], v[112:115]\n"
"ds_read_b128 v[24:27], v33 offset:0\n"
"s_sub_u32 s65, s65, 384\n"
"v_mfma_f32_16x16x32_bf16 v[116:119], v[168:171], a[212:215], v[116:119]\n"
"ds_read_b128 v[20:23], v32 offset:2048\n"
"s_mov_b32 s66, s3\n"
"v_mfma_f32_16x16x32_bf16 v[116:119], v[172:175], a[220:223], v[116:119]\n"
"ds_read_b128 v[28:31], v33 offset:2048\n"
"s_add_u32 s3, s3, 2048\n"
"v_mfma_f32_16x16x32_bf16 v[128:131], v[168:171], v[184:187], v[128:131]\n"
"ds_read_b64_tr_b16 v[152:153], v227 offset:49152\n"
"s_add_u32 s65, s65, 384\n"
"v_mfma_f32_16x16x32_bf16 v[128:131], v[172:175], v[192:195], v[128:131]\n"
"ds_read_b64_tr_b16 v[154:155], v227 offset:49408\n"
"s_and_b32 s65, s65, 2047\n"
"v_mfma_f32_16x16x32_bf16 v[132:135], v[168:171], v[188:191], v[132:135]\n"
"ds_read_b64_tr_b16 v[156:157], v227 offset:53248\n"
"s_add_u32 s12, s3, s65\n"
"v_mfma_f32_16x16x32_bf16 v[132:135], v[172:175], v[196:199], v[132:135]\n"
"ds_read_b64_tr_b16 v[158:159], v227 offset:53504\n"
"s_waitcnt lgkmcnt(12)\n"
"v_mfma_f32_16x16x32_bf16 v[120:123], v[176:179], a[208:211], v[120:123]\n"
"ds_read_b64_tr_b16 v[160:161], v228 offset:49152\n"
"s_add_u32 m0, s63, 98304\n"
"v_mfma_f32_16x16x32_bf16 v[120:123], v[180:183], a[216:219], v[120:123]\n"
"ds_read_b64_tr_b16 v[162:163], v228 offset:49408\n"
"buffer_load_dwordx4 v14, s[12:15], 0 offen offset:0 lds\n"
"v_mfma_f32_16x16x32_bf16 v[124:127], v[176:179], a[212:215], v[124:127]\n"
"ds_read_b64_tr_b16 v[164:165], v228 offset:53248\n"
"v_mfma_f32_16x16x32_bf16 v[124:127], v[180:183], a[220:223], v[124:127]\n"
"ds_read_b64_tr_b16 v[166:167], v228 offset:53504\n"
"v_mfma_f32_16x16x32_bf16 v[136:139], v[176:179], v[184:187], v[136:139]\n"
"s_waitcnt lgkmcnt(8)\n"
"s_barrier\n"
"v_mfma_f32_16x16x32_bf16 v[136:139], v[180:183], v[192:195], v[136:139]\n"
"buffer_load_dwordx4 v15, s[12:15], 0 offen offset:1024 lds\n"
"s_add_u32 s65, s65, 128\n"
"v_mfma_f32_16x16x32_bf16 v[140:143], v[176:179], v[188:191], v[140:143]\n"
"s_and_b32 s65, s65, 2047\n"
"s_add_u32 s12, s66, s65\n"
"v_mfma_f32_16x16x32_bf16 v[140:143], v[180:183], v[196:199], v[140:143]\n"
"ds_read_b64_tr_b16 v[168:169], v227 offset:57344\n"
"s_waitcnt lgkmcnt(5)\n"
"v_mfma_f32_16x16x32_bf16 v[48:51], v[152:155], a[224:227], v[48:51]\n"
"ds_read_b64_tr_b16 v[170:171], v227 offset:57600\n"
"v_mfma_f32_16x16x32_bf16 v[48:51], v[156:159], a[232:235], v[48:51]\n"
"ds_read_b64_tr_b16 v[172:173], v227 offset:61440\n"
"buffer_load_dwordx4 v14, s[12:15], 0 offen offset:2048 lds\n"
"v_mfma_f32_16x16x32_bf16 v[52:55], v[152:155], a[228:231], v[52:55]\n"
"ds_read_b64_tr_b16 v[174:175], v227 offset:61696\n"
"v_subrev_u32 v227, s91, v227\n"
"v_mfma_f32_16x16x32_bf16 v[52:55], v[156:159], a[236:239], v[52:55]\n"
"v_mfma_f32_16x16x32_bf16 v[64:67], v[152:155], v[16:19], v[64:67]\n"
"v_mfma_f32_16x16x32_bf16 v[64:67], v[156:159], v[24:27], v[64:67]\n"
"buffer_load_dwordx4 v15, s[12:15], 0 offen offset:3072 lds\n"
"s_and_b32 s8, s101, 0x80000000\n"
"v_mfma_f32_16x16x32_bf16 v[68:71], v[152:155], v[20:23], v[68:71]\n"
"s_and_b32 s12, s3, 0x80000000\n"
"buffer_load_dword v232, v45, s[12:15], 0 offen\n"
"v_mfma_f32_16x16x32_bf16 v[68:71], v[156:159], v[28:31], v[68:71]\n"
"ds_read_b64_tr_b16 v[176:177], v228 offset:57344\n"
"s_waitcnt lgkmcnt(5)\n"
"v_mfma_f32_16x16x32_bf16 v[56:59], v[160:163], a[224:227], v[56:59]\n"
"ds_read_b64_tr_b16 v[178:179], v228 offset:57600\n"
"v_mfma_f32_16x16x32_bf16 v[56:59], v[164:167], a[232:235], v[56:59]\n"
"ds_read_b64_tr_b16 v[180:181], v228 offset:61440\n"
"buffer_load_dword v232, v44, s[8:11], 0 offen\n"
"v_mfma_f32_16x16x32_bf16 v[60:63], v[160:163], a[228:231], v[60:63]\n"
"ds_read_b64_tr_b16 v[182:183], v228 offset:61696\n"
"s_add_u32 s65, s65, 384\n"
"v_mfma_f32_16x16x32_bf16 v[60:63], v[164:167], a[236:239], v[60:63]\n"
"s_and_b32 s65, s65, 2047\n"
"s_add_u32 s12, s3, s65\n"
"v_mfma_f32_16x16x32_bf16 v[72:75], v[160:163], v[16:19], v[72:75]\n"
"s_add_u32 m0, s63, 0\n"
"v_subrev_u32 v228, s91, v228\n"
"v_mfma_f32_16x16x32_bf16 v[72:75], v[164:167], v[24:27], v[72:75]\n"
"v_mfma_f32_16x16x32_bf16 v[76:79], v[160:163], v[20:23], v[76:79]\n"
"v_mfma_f32_16x16x32_bf16 v[76:79], v[164:167], v[28:31], v[76:79]\n"
"ds_read_b64_tr_b16 v[152:153], v229 offset:0\n"
"s_waitcnt lgkmcnt(5)\n"
"v_mfma_f32_16x16x32_bf16 v[80:83], v[168:171], a[224:227], v[80:83]\n"
"ds_read_b64_tr_b16 v[154:155], v229 offset:256\n"
"v_mfma_f32_16x16x32_bf16 v[80:83], v[172:175], a[232:235], v[80:83]\n"
"ds_read_b64_tr_b16 v[156:157], v229 offset:4096\n"
"v_mfma_f32_16x16x32_bf16 v[84:87], v[168:171], a[228:231], v[84:87]\n"
"ds_read_b64_tr_b16 v[158:159], v229 offset:4352\n"
"v_mfma_f32_16x16x32_bf16 v[84:87], v[172:175], a[236:239], v[84:87]\n"
"buffer_load_dwordx4 v14, s[12:15], 0 offen offset:0 lds\n"
"v_mfma_f32_16x16x32_bf16 v[96:99], v[168:171], v[16:19], v[96:99]\n"
"v_mfma_f32_16x16x32_bf16 v[96:99], v[172:175], v[24:27], v[96:99]\n"
"v_mfma_f32_16x16x32_bf16 v[100:103], v[168:171], v[20:23], v[100:103]\n"
"v_mfma_f32_16x16x32_bf16 v[100:103], v[172:175], v[28:31], v[100:103]\n"
"ds_read_b64_tr_b16 v[160:161], v230 offset:0\n"
"s_waitcnt lgkmcnt(5)\n"
"v_mfma_f32_16x16x32_bf16 v[88:91], v[176:179], a[224:227], v[88:91]\n"
"ds_read_b64_tr_b16 v[162:163], v230 offset:256\n"
"buffer_load_dwordx4 v15, s[12:15], 0 offen offset:1024 lds\n"
"v_mfma_f32_16x16x32_bf16 v[88:91], v[180:183], a[232:235], v[88:91]\n"
"ds_read_b64_tr_b16 v[164:165], v230 offset:4096\n"
"s_add_u32 s65, s65, 128\n"
"v_mfma_f32_16x16x32_bf16 v[92:95], v[176:179], a[228:231], v[92:95]\n"
"ds_read_b64_tr_b16 v[166:167], v230 offset:4352\n"
"s_and_b32 s65, s65, 2047\n"
"v_mfma_f32_16x16x32_bf16 v[92:95], v[180:183], a[236:239], v[92:95]\n"
"s_waitcnt vmcnt(8)\n"
".Lk_h3:\n"
"s_cbranch_execz .Lk_h7\n"
"s_barrier\n"
"v_mfma_f32_16x16x32_bf16 v[104:107], v[176:179], v[16:19], v[104:107]\n"
"ds_read_b128 a[240:243], v225 offset:16384\n"
"s_add_u32 s12, s66, s65\n"
"v_mfma_f32_16x16x32_bf16 v[104:107], v[180:183], v[24:27], v[104:107]\n"
"ds_read_b128 a[248:251], v226 offset:16384\n"
"buffer_load_dwordx4 v14, s[12:15], 0 offen offset:2048 lds\n"
"v_mfma_f32_16x16x32_bf16 v[108:111], v[176:179], v[20:23], v[108:111]\n"
"ds_read_b128 a[244:247], v225 offset:18432\n"
"v_mfma_f32_16x16x32_bf16 v[108:111], v[180:183], v[28:31], v[108:111]\n"
"ds_read_b128 a[252:255], v226 offset:18432\n"
"s_waitcnt lgkmcnt(8)\n"
"v_mfma_f32_16x16x32_bf16 v[112:115], v[152:155], a[224:227], v[112:115]\n"
"ds_read_b128 v[184:187], v32 offset:16384\n"
"v_mfma_f32_16x16x32_bf16 v[112:115], v[156:159], a[232:235], v[112:115]\n"
"ds_read_b128 v[192:195], v33 offset:16384\n"
"buffer_load_dwordx4 v15, s[12:15], 0 offen offset:3072 lds\n"
"v_mfma_f32_16x16x32_bf16 v[116:119], v[152:155], a[228:231], v[116:119]\n"
"ds_read_b128 v[188:191], v32 offset:18432\n"
"s_add_u32 s65, s65, 384\n"
"v_mfma_f32_16x16x32_bf16 v[116:119], v[156:159], a[236:239], v[116:119]\n"
"ds_read_b128 v[196:199], v33 offset:18432\n"
"s_and_b32 s65, s65, 2047\n"
"v_mfma_f32_16x16x32_bf16 v[128:131], v[152:155], v[16:19], v[128:131]\n"
"ds_read_b64_tr_b16 v[168:169], v229 offset:8192\n"
"s_add_u32 s12, s3, s65\n"
"v_mfma_f32_16x16x32_bf16 v[128:131], v[156:159], v[24:27], v[128:131]\n"
"ds_read_b64_tr_b16 v[170:171], v229 offset:8448\n"
"s_add_u32 m0, s63, 16384\n"
"v_mfma_f32_16x16x32_bf16 v[132:135], v[152:155], v[20:23], v[132:135]\n"
"ds_read_b64_tr_b16 v[172:173], v229 offset:12288\n"
"buffer_load_dwordx4 v14, s[12:15], 0 offen offset:0 lds\n"
"v_mfma_f32_16x16x32_bf16 v[132:135], v[156:159], v[28:31], v[132:135]\n"
"ds_read_b64_tr_b16 v[174:175], v229 offset:12544\n"
"s_waitcnt lgkmcnt(12)\n"
"v_mfma_f32_16x16x32_bf16 v[120:123], v[160:163], a[224:227], v[120:123]\n"
"ds_read_b64_tr_b16 v[176:177], v230 offset:8192\n"
"v_mfma_f32_16x16x32_bf16 v[120:123], v[164:167], a[232:235], v[120:123]\n"
"ds_read_b64_tr_b16 v[178:179], v230 offset:8448\n"
"v_mfma_f32_16x16x32_bf16 v[124:127], v[160:163], a[228:231], v[124:127]\n"
"ds_read_b64_tr_b16 v[180:181], v230 offset:12288\n"
"buffer_load_dwordx4 v15, s[12:15], 0 offen offset:1024 lds\n"
"v_mfma_f32_16x16x32_bf16 v[124:127], v[164:167], a[236:239], v[124:127]\n"
"ds_read_b64_tr_b16 v[182:183], v230 offset:12544\n"
"s_add_u32 s65, s65, 128\n"
"v_mfma_f32_16x16x32_bf16 v[136:139], v[160:163], v[16:19], v[136:139]\n"
"s_waitcnt lgkmcnt(8)\n"
"s_barrier\n"
"v_mfma_f32_16x16x32_bf16 v[136:139], v[164:167], v[24:27], v[136:139]\n"
"s_and_b32 s65, s65, 2047\n"
"s_add_u32 s12, s66, s65\n"
"v_mfma_f32_16x16x32_bf16 v[140:143], v[160:163], v[20:23], v[140:143]\n"
"buffer_load_dwordx4 v14, s[12:15], 0 offen offset:2048 lds\n"
"v_mfma_f32_16x16x32_bf16 v[140:143], v[164:167], v[28:31], v[140:143]\n"
"ds_read_b64_tr_b16 v[152:153], v229 offset:16384\n"
"s_waitcnt lgkmcnt(5)\n"
"v_mfma_f32_16x16x32_bf16 v[48:51], v[168:171], a[240:243], v[48:51]\n"
"ds_read_b64_tr_b16 v[154:155], v229 offset:16640\n"
"v_mfma_f32_16x16x32_bf16 v[48:51], v[172:175], a[248:251], v[48:51]\n"
"ds_read_b64_tr_b16 v[156:157], v229 offset:20480\n"
"v_mfma_f32_16x16x32_bf16 v[52:55], v[168:171], a[244:247], v[52:55]\n"
"ds_read_b64_tr_b16 v[158:159], v229 offset:20736\n"
"buffer_load_dwordx4 v15, s[12:15], 0 offen offset:3072 lds\n"
"v_mfma_f32_16x16x32_bf16 v[52:55], v[172:175], a[252:255], v[52:55]\n"
"buffer_load_dword v232, v43, s[20:23], 0 offen\n"
"v_mfma_f32_16x16x32_bf16 v[64:67], v[168:171], v[184:187], v[64:67]\n"
"global_load_dword v232, v7, s[50:51]\n"
"v_mul_f32_e64 v48, v48, abs(v48)\n"
"v_mfma_f32_16x16x32_bf16 v[64:67], v[172:175], v[192:195], v[64:67]\n"
"global_load_dword v232, v7, s[50:51] offset:1024\n"
"v_mul_f32_e64 v49, v49, abs(v49)\n"
"v_mfma_f32_16x16x32_bf16 v[68:71], v[168:171], v[188:191], v[68:71]\n"
"global_load_dword v232, v7, s[52:53]\n"
"v_mul_f32_e64 v50, v50, abs(v50)\n"
"v_mfma_f32_16x16x32_bf16 v[68:71], v[172:175], v[196:199], v[68:71]\n"
"ds_read_b64_tr_b16 v[160:161], v230 offset:16384\n"
"s_waitcnt lgkmcnt(5)\n"
"v_mfma_f32_16x16x32_bf16 v[56:59], v[176:179], a[240:243], v[56:59]\n"
"ds_read_b64_tr_b16 v[162:163], v230 offset:16640\n"
"global_load_dword v232, v7, s[52:53] offset:1024\n"
"v_mfma_f32_16x16x32_bf16 v[56:59], v[180:183], a[248:251], v[56:59]\n"
"ds_read_b64_tr_b16 v[164:165], v230 offset:20480\n"
"buffer_load_dword v232, v47, s[36:39], 0 offen sc1\n"
"v_mfma_f32_16x16x32_bf16 v[60:63], v[176:179], a[244:247], v[60:63]\n"
"ds_read_b64_tr_b16 v[166:167], v230 offset:20736\n"
".Lk_pfdone:\n"
"s_add_u32 s65, s65, 384\n"
"v_mfma_f32_16x16x32_bf16 v[60:63], v[180:183], a[252:255], v[60:63]\n"
"s_and_b32 s65, s65, 2047\n"
"s_add_u32 s12, s3, s65\n"
"v_mfma_f32_16x16x32_bf16 v[72:75], v[176:179], v[184:187], v[72:75]\n"
"s_add_u32 m0, s63, 32768\n"
"v_mul_f32_e64 v51, v51, abs(v51)\n"
"v_mfma_f32_16x16x32_bf16 v[72:75], v[180:183], v[192:195], v[72:75]\n"
"v_mul_f32_e64 v56, v56, abs(v56)\n"
"v_mul_f32_e64 v57, v57, abs(v57)\n"
"v_mfma_f32_16x16x32_bf16 v[76:79], v[176:179], v[188:191], v[76:79]\n"
"v_mul_f32_e64 v58, v58, abs(v58)\n"
"v_mul_f32_e64 v59, v59, abs(v59)\n"
"v_mfma_f32_16x16x32_bf16 v[76:79], v[180:183], v[196:199], v[76:79]\n"
"ds_read_b64_tr_b16 v[168:169], v229 offset:24576\n"
"s_waitcnt lgkmcnt(5)\n"
"v_mfma_f32_16x16x32_bf16 v[80:83], v[152:155], a[240:243], v[80:83]\n"
"ds_read_b64_tr_b16 v[170:171], v229 offset:24832\n"
"v_cvt_pk_bf16_f32 v48, v48, v49\n"
"v_mfma_f32_16x16x32_bf16 v[80:83], v[156:159], a[248:251], v[80:83]\n"
"ds_read_b64_tr_b16 v[172:173], v229 offset:28672\n"
"v_cvt_pk_bf16_f32 v49, v50, v51\n"
"v_mfma_f32_16x16x32_bf16 v[84:87], v[152:155], a[244:247], v[84:87]\n"
"ds_read_b64_tr_b16 v[174:175], v229 offset:28928\n"
"v_subrev_u32 v229, s91, v229\n"
"v_mfma_f32_16x16x32_bf16 v[84:87], v[156:159], a[252:255], v[84:87]\n"
"buffer_load_dwordx4 v14, s[12:15], 0 offen offset:0 lds\n"
"v_cvt_pk_bf16_f32 v50, v56, v57\n"
"v_mfma_f32_16x16x32_bf16 v[96:99], v[152:155], v[184:187], v[96:99]\n"
"v_cvt_pk_bf16_f32 v51, v58, v59\n"
"v_pk_max_i16 v48, v48, 0\n"
"v_mfma_f32_16x16x32_bf16 v[96:99], v[156:159], v[192:195], v[96:99]\n"
"v_pk_max_i16 v49, v49, 0\n"
"v_pk_max_i16 v50, v50, 0\n"
"v_mfma_f32_16x16x32_bf16 v[100:103], v[152:155], v[188:191], v[100:103]\n"
"v_pk_max_i16 v51, v51, 0\n"
"v_mul_f32_e64 v52, v52, abs(v52)\n"
"v_mfma_f32_16x16x32_bf16 v[100:103], v[156:159], v[196:199], v[100:103]\n"
"ds_read_b64_tr_b16 v[176:177], v230 offset:24576\n"
"s_waitcnt lgkmcnt(5)\n"
"v_mfma_f32_16x16x32_bf16 v[88:91], v[160:163], a[240:243], v[88:91]\n"
"ds_read_b64_tr_b16 v[178:179], v230 offset:24832\n"
"buffer_load_dwordx4 v15, s[12:15], 0 offen offset:1024 lds\n"
"v_mfma_f32_16x16x32_bf16 v[88:91], v[164:167], a[248:251], v[88:91]\n"
"ds_read_b64_tr_b16 v[180:181], v230 offset:28672\n"
"s_add_u32 s65, s65, 128\n"
"v_mfma_f32_16x16x32_bf16 v[92:95], v[160:163], a[244:247], v[92:95]\n"
"ds_read_b64_tr_b16 v[182:183], v230 offset:28928\n"
"s_and_b32 s65, s65, 2047\n"
"v_mfma_f32_16x16x32_bf16 v[92:95], v[164:167], a[252:255], v[92:95]\n"
"s_waitcnt vmcnt(18)\n"
"s_barrier\n"
"v_mfma_f32_16x16x32_bf16 v[104:107], v[160:163], v[184:187], v[104:107]\n"
"s_add_u32 s12, s66, s65\n"
"buffer_load_dwordx4 v14, s[12:15], 0 offen offset:2048 lds\n"
"v_mfma_f32_16x16x32_bf16 v[104:107], v[164:167], v[192:195], v[104:107]\n"
"ds_read_b128 v[152:155], v223 offset:32768\n"
"v_subrev_u32 v230, s91, v230\n"
"v_mfma_f32_16x16x32_bf16 v[108:111], v[160:163], v[188:191], v[108:111]\n"
"s_lshl_b32 s1, s1, 11\n"
"v_readlane_b32 s91, v207, 0\n"
"v_mfma_f32_16x16x32_bf16 v[108:111], v[164:167], v[196:199], v[108:111]\n"
"ds_read_b128 v[156:159], v224 offset:32768\n"
"s_waitcnt lgkmcnt(6)\n"
"v_mfma_f32_16x16x32_bf16 v[112:115], v[168:171], a[240:243], v[112:115]\n"
"s_lshl_b32 s91, s91, 5\n"
"s_add_u32 s91, s91, s1\n"
"v_mfma_f32_16x16x32_bf16 v[112:115], v[172:175], a[248:251], v[112:115]\n"
"ds_read_b128 v[160:163], v223 offset:34816\n"
"buffer_load_dwordx4 v15, s[12:15], 0 offen offset:3072 lds\n"
"v_mfma_f32_16x16x32_bf16 v[116:119], v[168:171], a[244:247], v[116:119]\n"
"s_add_u32 s3, s3, 32768\n"
"s_add_u32 s66, s66, 32768\n"
"v_mfma_f32_16x16x32_bf16 v[116:119], v[172:175], a[252:255], v[116:119]\n"
"ds_read_b128 v[164:167], v224 offset:34816\n"
"s_add_u32 s65, s65, 384\n"
"v_mfma_f32_16x16x32_bf16 v[128:131], v[168:171], v[184:187], v[128:131]\n"
"s_and_b32 s65, s65, 2047\n"
"s_add_u32 s12, s3, s65\n"
"v_mfma_f32_16x16x32_bf16 v[128:131], v[172:175], v[192:195], v[128:131]\n"
"s_add_u32 m0, s63, 49152\n"
"v_mul_f32_e64 v53, v53, abs(v53)\n"
"v_mfma_f32_16x16x32_bf16 v[132:135], v[168:171], v[188:191], v[132:135]\n"
"ds_read_b128 v[168:171], v223 offset:36864\n"
"buffer_load_dwordx4 v14, s[12:15], 0 offen offset:0 lds\n"
"v_mfma_f32_16x16x32_bf16 v[132:135], v[172:175], v[196:199], v[132:135]\n"
"ds_read_b128 v[172:175], v224 offset:36864\n"
"s_waitcnt lgkmcnt(6)\n"
"v_mfma_f32_16x16x32_bf16 v[120:123], v[176:179], a[240:243], v[120:123]\n"
"v_mul_f32_e64 v54, v54, abs(v54)\n"
"v_mul_f32_e64 v55, v55, abs(v55)\n"
"v_mfma_f32_16x16x32_bf16 v[120:123], v[180:183], a[248:251], v[120:123]\n"
"v_mul_f32_e64 v60, v60, abs(v60)\n"
"v_mul_f32_e64 v61, v61, abs(v61)\n"
"v_mfma_f32_16x16x32_bf16 v[124:127], v[176:179], a[244:247], v[124:127]\n"
"buffer_load_dwordx4 v15, s[12:15], 0 offen offset:1024 lds\n"
"s_add_u32 s65, s65, 128\n"
"v_mfma_f32_16x16x32_bf16 v[124:127], v[180:183], a[252:255], v[124:127]\n"
"s_and_b32 s65, s65, 2047\n"
"s_add_u32 s12, s66, s65\n"
"v_mfma_f32_16x16x32_bf16 v[136:139], v[176:179], v[184:187], v[136:139]\n"
"v_mul_f32_e64 v62, v62, abs(v62)\n"
"v_mul_f32_e64 v63, v63, abs(v63)\n"
"v_mfma_f32_16x16x32_bf16 v[136:139], v[180:183], v[192:195], v[136:139]\n"
"v_cvt_pk_bf16_f32 v52, v52, v53\n"
"v_cvt_pk_bf16_f32 v53, v54, v55\n"
"v_mfma_f32_16x16x32_bf16 v[140:143], v[176:179], v[188:191], v[140:143]\n"
"ds_read_b128 v[176:179], v223 offset:38912\n"
"v_cvt_pk_bf16_f32 v54, v60, v61\n"
"v_mfma_f32_16x16x32_bf16 v[140:143], v[180:183], v[196:199], v[140:143]\n"
"ds_read_b128 v[180:183], v224 offset:38912\n"
"s_waitcnt lgkmcnt(4)\n"
"v_mfma_f32_16x16x32_bf16 v[16:19], a[0:3], v[152:155], 0\n"
"ds_read_b128 v[184:187], v223 offset:40960\n"
"buffer_load_dwordx4 v14, s[12:15], 0 offen offset:2048 lds\n"
"v_mfma_f32_16x16x32_bf16 v[16:19], a[8:11], v[156:159], v[16:19]\n"
"ds_read_b128 v[188:191], v224 offset:40960\n"
"v_cvt_pk_bf16_f32 v55, v62, v63\n"
"v_mfma_f32_16x16x32_bf16 v[16:19], a[16:19], v[160:163], v[16:19]\n"
"ds_read_b128 v[192:195], v223 offset:43008\n"
"v_pk_max_i16 v52, v52, 0\n"
"v_mfma_f32_16x16x32_bf16 v[16:19], a[24:27], v[164:167], v[16:19]\n"
"ds_read_b128 v[196:199], v224 offset:43008\n"
"v_pk_max_i16 v53, v53, 0\n"
"v_mfma_f32_16x16x32_bf16 v[20:23], a[4:7], v[152:155], 0\n"
"buffer_load_dwordx4 v15, s[12:15], 0 offen offset:3072 lds\n"
"s_add_u32 s65, s65, 384\n"
"v_mfma_f32_16x16x32_bf16 v[20:23], a[12:15], v[156:159], v[20:23]\n"
"ds_read_b128 v[152:155], v223 offset:45056\n"
"s_and_b32 s65, s65, 2047\n"
"v_mfma_f32_16x16x32_bf16 v[20:23], a[20:23], v[160:163], v[20:23]\n"
"s_add_u32 s12, s3, s65\n"
"s_add_u32 m0, s63, 65536\n"
"v_mfma_f32_16x16x32_bf16 v[20:23], a[28:31], v[164:167], v[20:23]\n"
"ds_read_b128 v[156:159], v224 offset:45056\n"
"s_waitcnt lgkmcnt(6)\n"
"v_mfma_f32_16x16x32_bf16 v[24:27], a[0:3], v[168:171], 0\n"
"v_pk_max_i16 v54, v54, 0\n"
"v_pk_max_i16 v55, v55, 0\n"
"v_mfma_f32_16x16x32_bf16 v[24:27], a[8:11], v[172:175], v[24:27]\n"
"ds_read_b128 v[160:163], v223 offset:47104\n"
"v_mul_f32_e64 v64, v64, abs(v64)\n"
"v_mfma_f32_16x16x32_bf16 v[24:27], a[16:19], v[176:179], v[24:27]\n"
"v_mul_f32_e64 v65, v65, abs(v65)\n"
"v_mul_f32_e64 v66, v66, abs(v66)\n"
"v_mfma_f32_16x16x32_bf16 v[24:27], a[24:27], v[180:183], v[24:27]\n"
"ds_read_b128 v[164:167], v224 offset:47104\n"
"v_mul_f32_e64 v67, v67, abs(v67)\n"
"v_mfma_f32_16x16x32_bf16 v[28:31], a[4:7], v[168:171], 0\n"
"s_waitcnt vmcnt(18)\n"
"s_barrier\n"
"v_mfma_f32_16x16x32_bf16 v[28:31], a[12:15], v[172:175], v[28:31]\n"
"ds_read_b128 v[168:171], v221 offset:0\n"
"buffer_load_dwordx4 v14, s[12:15], 0 offen offset:0 lds\n"
"v_mfma_f32_16x16x32_bf16 v[28:31], a[20:23], v[176:179], v[28:31]\n"
"v_mul_f32_e64 v72, v72, abs(v72)\n"
"v_mul_f32_e64 v73, v73, abs(v73)\n"
"v_mfma_f32_16x16x32_bf16 v[28:31], a[28:31], v[180:183], v[28:31]\n"
"ds_read_b128 v[172:175], v222 offset:0\n"
"s_waitcnt lgkmcnt(6)\n"
"v_mfma_f32_16x16x32_bf16 v[16:19], a[32:35], v[184:187], v[16:19]\n"
"v_mul_f32_e64 v74, v74, abs(v74)\n"
"v_mul_f32_e64 v75, v75, abs(v75)\n"
"v_mfma_f32_16x16x32_bf16 v[16:19], a[40:43], v[188:191], v[16:19]\n"
"ds_read_b128 v[176:179], v221 offset:2048\n"
"buffer_load_dwordx4 v15, s[12:15], 0 offen offset:1024 lds\n"
"v_mfma_f32_16x16x32_bf16 v[16:19], a[48:51], v[192:195], v[16:19]\n"
"s_add_u32 s65, s65, 128\n"
"s_and_b32 s65, s65, 2047\n"
"v_mfma_f32_16x16x32_bf16 v[16:19], a[56:59], v[196:199], v[16:19]\n"
"ds_read_b128 v[180:183], v222 offset:2048\n"
"s_add_u32 s12, s66, s65\n"
"v_mfma_f32_16x16x32_bf16 v[20:23], a[36:39], v[184:187], v[20:23]\n"
"v_cvt_pk_bf16_f32 v64, v64, v65\n"
"v_cvt_pk_bf16_f32 v65, v66, v67\n"
"v_mfma_f32_16x16x32_bf16 v[20:23], a[44:47], v[188:191], v[20:23]\n"
"ds_read_b128 v[184:187], v221 offset:4096\n"
"buffer_load_dwordx4 v14, s[12:15], 0 offen offset:2048 lds\n"
"v_mfma_f32_16x16x32_bf16 v[20:23], a[52:55], v[192:195], v[20:23]\n"
"v_cvt_pk_bf16_f32 v66, v72, v73\n"
"v_cvt_pk_bf16_f32 v67, v74, v75\n"
"v_mfma_f32_16x16x32_bf16 v[20:23], a[60:63], v[196:199], v[20:23]\n"
"ds_read_b128 v[188:191], v222 offset:4096\n"
"s_waitcnt lgkmcnt(6)\n"
"v_mfma_f32_16x16x32_bf16 v[24:27], a[32:35], v[152:155], v[24:27]\n"
"v_pk_max_i16 v64, v64, 0\n"
"v_pk_max_i16 v65, v65, 0\n"
"v_mfma_f32_16x16x32_bf16 v[24:27], a[40:43], v[156:159], v[24:27]\n"
"ds_read_b128 v[192:195], v221 offset:6144\n"
"v_pk_max_i16 v66, v66, 0\n"
"v_mfma_f32_16x16x32_bf16 v[24:27], a[48:51], v[160:163], v[24:27]\n"
"buffer_load_dwordx4 v15, s[12:15], 0 offen offset:3072 lds\n"
"s_add_u32 s65, s65, 384\n"
"v_mfma_f32_16x16x32_bf16 v[24:27], a[56:59], v[164:167], v[24:27]\n"
"ds_read_b128 v[196:199], v222 offset:6144\n"
"s_and_b32 s65, s65, 2047\n"
"v_mfma_f32_16x16x32_bf16 v[28:31], a[36:39], v[152:155], v[28:31]\n"
"s_add_u32 s12, s3, s65\n"
"s_add_u32 m0, s63, 81920\n"
"v_mfma_f32_16x16x32_bf16 v[28:31], a[44:47], v[156:159], v[28:31]\n"
"ds_read_b128 v[152:155], v221 offset:8192\n"
"v_pk_max_i16 v67, v67, 0\n"
"v_mfma_f32_16x16x32_bf16 v[28:31], a[52:55], v[160:163], v[28:31]\n"
"v_mul_f32_e64 v68, v68, abs(v68)\n"
"v_mul_f32_e64 v69, v69, abs(v69)\n"
"v_mfma_f32_16x16x32_bf16 v[28:31], a[60:63], v[164:167], v[28:31]\n"
"ds_read_b128 v[156:159], v222 offset:8192\n"
"s_waitcnt lgkmcnt(6)\n"
"v_mfma_f32_16x16x32_bf16 v[16:19], a[64:67], v[168:171], v[16:19]\n"
"buffer_load_dwordx4 v14, s[12:15], 0 offen offset:0 lds\n"
"v_mul_f32_e64 v70, v70, abs(v70)\n"
"v_mfma_f32_16x16x32_bf16 v[16:19], a[72:75], v[172:175], v[16:19]\n"
"ds_read_b128 v[160:163], v221 offset:10240\n"
"v_mul_f32_e64 v71, v71, abs(v71)\n"
"v_mfma_f32_16x16x32_bf16 v[16:19], a[80:83], v[176:179], v[16:19]\n"
"v_mul_f32_e64 v76, v76, abs(v76)\n"
"v_mul_f32_e64 v77, v77, abs(v77)\n"
"v_mfma_f32_16x16x32_bf16 v[16:19], a[88:91], v[180:183], v[16:19]\n"
"ds_read_b128 v[164:167], v222 offset:10240\n"
"v_mul_f32_e64 v78, v78, abs(v78)\n"
"v_mfma_f32_16x16x32_bf16 v[20:23], a[68:71], v[168:171], v[20:23]\n"
"v_mul_f32_e64 v79, v79, abs(v79)\n"
"v_cvt_pk_bf16_f32 v68, v68, v69\n"
"v_mfma_f32_16x16x32_bf16 v[20:23], a[76:79], v[172:175], v[20:23]\n"
"ds_read_b128 v[168:171], v221 offset:12288\n"
"v_cvt_pk_bf16_f32 v69, v70, v71\n"
"v_mfma_f32_16x16x32_bf16 v[20:23], a[84:87], v[176:179], v[20:23]\n"
"buffer_load_dwordx4 v15, s[12:15], 0 offen offset:1024 lds\n"
"s_add_u32 s65, s65, 128\n"
"v_mfma_f32_16x16x32_bf16 v[20:23], a[92:95], v[180:183], v[20:23]\n"
"ds_read_b128 v[172:175], v222 offset:12288\n"
"s_waitcnt lgkmcnt(6)\n"
"v_mfma_f32_16x16x32_bf16 v[24:27], a[64:67], v[184:187], v[24:27]\n"
"s_and_b32 s65, s65, 2047\n"
"s_add_u32 s12, s66, s65\n"
"v_mfma_f32_16x16x32_bf16 v[24:27], a[72:75], v[188:191], v[24:27]\n"
"ds_read_b128 v[176:179], v221 offset:14336\n"
"v_cvt_pk_bf16_f32 v70, v76, v77\n"
"v_mfma_f32_16x16x32_bf16 v[24:27], a[80:83], v[192:195], v[24:27]\n"
"v_cvt_pk_bf16_f32 v71, v78, v79\n"
"v_pk_max_i16 v68, v68, 0\n"
"v_mfma_f32_16x16x32_bf16 v[24:27], a[88:91], v[196:199], v[24:27]\n"
"ds_read_b128 v[180:183], v222 offset:14336\n"
"v_pk_max_i16 v69, v69, 0\n"
"v_mfma_f32_16x16x32_bf16 v[28:31], a[68:71], v[184:187], v[28:31]\n"
"s_waitcnt vmcnt(20)\n"
"s_barrier\n"
"v_mfma_f32_16x16x32_bf16 v[28:31], a[76:79], v[188:191], v[28:31]\n"
"ds_read_b128 v[184:187], v221 offset:16384\n"
"buffer_load_dwordx4 v14, s[12:15], 0 offen offset:2048 lds\n"
"v_mfma_f32_16x16x32_bf16 v[28:31], a[84:87], v[192:195], v[28:31]\n"
"v_pk_max_i16 v70, v70, 0\n"
"v_pk_max_i16 v71, v71, 0\n"
"v_mfma_f32_16x16x32_bf16 v[28:31], a[92:95], v[196:199], v[28:31]\n"
"ds_read_b128 v[188:191], v222 offset:16384\n"
"s_waitcnt lgkmcnt(6)\n"
"v_mfma_f32_16x16x32_bf16 v[16:19], a[96:99], v[152:155], v[16:19]\n"
"v_mul_f32_e64 v80, v80, abs(v80)\n"
"v_mul_f32_e64 v81, v81, abs(v81)\n"
"v_mfma_f32_16x16x32_bf16 v[16:19], a[104:107], v[156:159], v[16:19]\n"
"ds_read_b128 v[192:195], v221 offset:18432\n"
"v_mul_f32_e64 v82, v82, abs(v82)\n"
"v_mfma_f32_16x16x32_bf16 v[16:19], a[112:115], v[160:163], v[16:19]\n"
"buffer_store_dwordx4 v[48:51], v200, s[36:39], s91 offen offset:0 sc1\n"
"v_mfma_f32_16x16x32_bf16 v[16:19], a[120:123], v[164:167], v[16:19]\n"
"ds_read_b128 v[196:199], v222 offset:18432\n"
"v_mul_f32_e64 v83, v83, abs(v83)\n"
"v_mfma_f32_16x16x32_bf16 v[20:23], a[100:103], v[152:155], v[20:23]\n"
"v_mul_f32_e64 v88, v88, abs(v88)\n"
"v_mul_f32_e64 v89, v89, abs(v89)\n"
"v_mfma_f32_16x16x32_bf16 v[20:23], a[108:111], v[156:159], v[20:23]\n"
"ds_read_b128 v[152:155], v221 offset:20480\n"
"v_mul_f32_e64 v90, v90, abs(v90)\n"
"v_mfma_f32_16x16x32_bf16 v[20:23], a[116:119], v[160:163], v[20:23]\n"
"buffer_store_dwordx4 v[52:55], v200, s[36:39], s91 offen offset:1024 sc1\n"
"v_mfma_f32_16x16x32_bf16 v[20:23], a[124:127], v[164:167], v[20:23]\n"
"ds_read_b128 v[156:159], v222 offset:20480\n"
"s_waitcnt lgkmcnt(6)\n"
"v_mfma_f32_16x16x32_bf16 v[24:27], a[96:99], v[168:171], v[24:27]\n"
"v_mul_f32_e64 v91, v91, abs(v91)\n"
"v_cvt_pk_bf16_f32 v80, v80, v81\n"
"v_mfma_f32_16x16x32_bf16 v[24:27], a[104:107], v[172:175], v[24:27]\n"
"ds_read_b128 v[160:163], v221 offset:22528\n"
"v_cvt_pk_bf16_f32 v81, v82, v83\n"
"v_mfma_f32_16x16x32_bf16 v[24:27], a[112:115], v[176:179], v[24:27]\n"
"buffer_store_dwordx4 v[64:67], v206, s[36:39], s91 offen offset:0 sc1\n"
"v_mfma_f32_16x16x32_bf16 v[24:27], a[120:123], v[180:183], v[24:27]\n"
"ds_read_b128 v[164:167], v222 offset:22528\n"
"v_cvt_pk_bf16_f32 v82, v88, v89\n"
"v_mfma_f32_16x16x32_bf16 v[28:31], a[100:103], v[168:171], v[28:31]\n"
"v_cvt_pk_bf16_f32 v83, v90, v91\n"
"v_pk_max_i16 v80, v80, 0\n"
"v_mfma_f32_16x16x32_bf16 v[28:31], a[108:111], v[172:175], v[28:31]\n"
"ds_read_b128 v[168:171], v221 offset:24576\n"
"v_pk_max_i16 v81, v81, 0\n"
"v_mfma_f32_16x16x32_bf16 v[28:31], a[116:119], v[176:179], v[28:31]\n"
"buffer_store_dwordx4 v[68:71], v206, s[36:39], s91 offen offset:1024 sc1\n"
"v_mfma_f32_16x16x32_bf16 v[28:31], a[124:127], v[180:183], v[28:31]\n"
"ds_read_b128 v[172:175], v222 offset:24576\n"
"s_waitcnt lgkmcnt(6)\n"
"v_mfma_f32_16x16x32_bf16 v[16:19], a[128:131], v[184:187], v[16:19]\n"
"v_readlane_b32 s91, v207, 16\n"
"v_pk_max_i16 v82, v82, 0\n"
"v_mfma_f32_16x16x32_bf16 v[16:19], a[136:139], v[188:191], v[16:19]\n"
"ds_read_b128 v[176:179], v221 offset:26624\n"
"s_lshl_b32 s91, s91, 5\n"
"v_mfma_f32_16x16x32_bf16 v[16:19], a[144:147], v[192:195], v[16:19]\n"
"buffer_load_dwordx4 v15, s[12:15], 0 offen offset:3072 lds\n"
"s_add_u32 s65, s65, 384\n"
"v_mfma_f32_16x16x32_bf16 v[16:19], a[152:155], v[196:199], v[16:19]\n"
"ds_read_b128 v[180:183], v222 offset:26624\n"
"s_and_b32 s65, s65, 2047\n"
"v_mfma_f32_16x16x32_bf16 v[20:23], a[132:135], v[184:187], v[20:23]\n"
"s_add_u32 s12, s3, s65\n"
"s_add_u32 m0, s63, 98304\n"
"v_mfma_f32_16x16x32_bf16 v[20:23], a[140:143], v[188:191], v[20:23]\n"
"ds_read_b128 v[184:187], v221 offset:28672\n"
"s_add_u32 s91, s91, s1\n"
"v_mfma_f32_16x16x32_bf16 v[20:23], a[148:151], v[192:195], v[20:23]\n"
"buffer_load_dwordx4 v14, s[12:15], 0 offen offset:0 lds\n"
"v_pk_max_i16 v83, v83, 0\n"
"v_mfma_f32_16x16x32_bf16 v[20:23], a[156:159], v[196:199], v[20:23]\n"
"ds_read_b128 v[188:191], v222 offset:28672\n"
"s_waitcnt lgkmcnt(6)\n"
"v_mfma_f32_16x16x32_bf16 v[24:27], a[128:131], v[152:155], v[24:27]\n"
"v_mul_f32_e64 v84, v84, abs(v84)\n"
"v_mul_f32_e64 v85, v85, abs(v85)\n"
"v_mfma_f32_16x16x32_bf16 v[24:27], a[136:139], v[156:159], v[24:27]\n"
"ds_read_b128 v[192:195], v221 offset:30720\n"
"v_mul_f32_e64 v86, v86, abs(v86)\n"
"v_mfma_f32_16x16x32_bf16 v[24:27], a[144:147], v[160:163], v[24:27]\n"
"buffer_load_dwordx4 v15, s[12:15], 0 offen offset:1024 lds\n"
"s_add_u32 s65, s65, 128\n"
"v_mfma_f32_16x16x32_bf16 v[24:27], a[152:155], v[164:167], v[24:27]\n"
"ds_read_b128 v[196:199], v222 offset:30720\n"
"s_and_b32 s65, s65, 2047\n"
"v_mfma_f32_16x16x32_bf16 v[28:31], a[132:135], v[152:155], v[28:31]\n"
"s_waitcnt vmcnt(18)\n"
"s_barrier\n"
"v_mfma_f32_16x16x32_bf16 v[28:31], a[140:143], v[156:159], v[28:31]\n"
"ds_read_b128 v[152:155], v221 offset:32768\n"
"s_add_u32 s12, s66, s65\n"
"v_mfma_f32_16x16x32_bf16 v[28:31], a[148:151], v[160:163], v[28:31]\n"
"buffer_load_dwordx4 v14, s[12:15], 0 offen offset:2048 lds\n"
"v_mul_f32_e64 v87, v87, abs(v87)\n"
"v_mfma_f32_16x16x32_bf16 v[28:31], a[156:159], v[164:167], v[28:31]\n"
"ds_read_b128 v[156:159], v222 offset:32768\n"
"s_waitcnt lgkmcnt(6)\n"
"v_mfma_f32_16x16x32_bf16 v[16:19], a[160:163], v[168:171], v[16:19]\n"
"v_mul_f32_e64 v92, v92, abs(v92)\n"
"v_mul_f32_e64 v93, v93, abs(v93)\n"
"v_mfma_f32_16x16x32_bf16 v[16:19], a[168:171], v[172:175], v[16:19]\n"
"ds_read_b128 v[160:163], v221 offset:34816\n"
"v_mul_f32_e64 v94, v94, abs(v94)\n"
"v_mfma_f32_16x16x32_bf16 v[16:19], a[176:179], v[176:179], v[16:19]\n"
"buffer_load_dwordx4 v15, s[12:15], 0 offen offset:3072 lds\n"
"s_add_u32 s3, s3, 98304\n"
"v_mfma_f32_16x16x32_bf16 v[16:19], a[184:187], v[180:183], v[16:19]\n"
"ds_read_b128 v[164:167], v222 offset:34816\n"
"s_add_u32 s66, s66, 98304\n"
"v_mfma_f32_16x16x32_bf16 v[20:23], a[164:167], v[168:171], v[20:23]\n"
"s_add_u32 s65, s65, 384\n"
"s_and_b32 s65, s65, 2047\n"
"v_mfma_f32_16x16x32_bf16 v[20:23], a[172:175], v[172:175], v[20:23]\n"
"ds_read_b128 v[168:171], v221 offset:36864\n"
"s_add_u32 s12, s3, s65\n"
"v_mfma_f32_16x16x32_bf16 v[20:23], a[180:183], v[176:179], v[20:23]\n"
"s_add_u32 m0, s63, 0\n"
"v_mul_f32_e64 v95, v95, abs(v95)\n"
"v_mfma_f32_16x16x32_bf16 v[20:23], a[188:191], v[180:183], v[20:23]\n"
"ds_read_b128 v[172:175], v222 offset:36864\n"
"s_waitcnt lgkmcnt(6)\n"
"v_mfma_f32_16x16x32_bf16 v[24:27], a[160:163], v[184:187], v[24:27]\n"
"buffer_load_dwordx4 v14, s[12:15], 0 offen offset:0 lds\n"
"v_cvt_pk_bf16_f32 v84, v84, v85\n"
"v_mfma_f32_16x16x32_bf16 v[24:27], a[168:171], v[188:191], v[24:27]\n"
"ds_read_b128 v[176:179], v221 offset:38912\n"
"v_cvt_pk_bf16_f32 v85, v86, v87\n"
"v_mfma_f32_16x16x32_bf16 v[24:27], a[176:179], v[192:195], v[24:27]\n"
"v_cvt_pk_bf16_f32 v86, v92, v93\n"
"v_cvt_pk_bf16_f32 v87, v94, v95\n"
"v_mfma_f32_16x16x32_bf16 v[24:27], a[184:187], v[196:199], v[24:27]\n"
"ds_read_b128 v[180:183], v222 offset:38912\n"
"v_pk_max_i16 v84, v84, 0\n"
"v_mfma_f32_16x16x32_bf16 v[28:31], a[164:167], v[184:187], v[28:31]\n"
"v_pk_max_i16 v85, v85, 0\n"
"v_pk_max_i16 v86, v86, 0\n"
"v_mfma_f32_16x16x32_bf16 v[28:31], a[172:175], v[188:191], v[28:31]\n"
"ds_read_b128 v[184:187], v221 offset:40960\n"
"buffer_load_dwordx4 v15, s[12:15], 0 offen offset:1024 lds\n"
"v_mfma_f32_16x16x32_bf16 v[28:31], a[180:183], v[192:195], v[28:31]\n"
"s_add_u32 s65, s65, 128\n"
"s_and_b32 s65, s65, 2047\n"
"v_mfma_f32_16x16x32_bf16 v[28:31], a[188:191], v[196:199], v[28:31]\n"
"ds_read_b128 v[188:191], v222 offset:40960\n"
"s_waitcnt lgkmcnt(6)\n"
"v_mfma_f32_16x16x32_bf16 v[16:19], a[192:195], v[152:155], v[16:19]\n"
"s_add_u32 s12, s66, s65\n"
"v_pk_max_i16 v87, v87, 0\n"
"v_mfma_f32_16x16x32_bf16 v[16:19], a[200:203], v[156:159], v[16:19]\n"
"ds_read_b128 v[192:195], v221 offset:43008\n"
"v_mul_f32_e64 v96, v96, abs(v96)\n"
"v_mfma_f32_16x16x32_bf16 v[16:19], a[208:211], v[160:163], v[16:19]\n"
"v_mul_f32_e64 v97, v97, abs(v97)\n"
"v_mul_f32_e64 v98, v98, abs(v98)\n"
"v_mfma_f32_16x16x32_bf16 v[16:19], a[216:219], v[164:167], v[16:19]\n"
"ds_read_b128 v[196:199], v222 offset:43008\n"
"v_mul_f32_e64 v99, v99, abs(v99)\n"
"v_mfma_f32_16x16x32_bf16 v[20:23], a[196:199], v[152:155], v[20:23]\n"
"buffer_load_dwordx4 v14, s[12:15], 0 offen offset:2048 lds\n"
"v_mul_f32_e64 v104, v104, abs(v104)\n"
"v_mfma_f32_16x16x32_bf16 v[20:23], a[204:207], v[156:159], v[20:23]\n"
"ds_read_b128 v[152:155], v221 offset:45056\n"
"v_mul_f32_e64 v105, v105, abs(v105)\n"
"v_mfma_f32_16x16x32_bf16 v[20:23], a[212:215], v[160:163], v[20:23]\n"
"v_mul_f32_e64 v106, v106, abs(v106)\n"
"v_mul_f32_e64 v107, v107, abs(v107)\n"
"v_mfma_f32_16x16x32_bf16 v[20:23], a[220:223], v[164:167], v[20:23]\n"
"ds_read_b128 v[156:159], v222 offset:45056\n"
"s_waitcnt lgkmcnt(6)\n"
"v_mfma_f32_16x16x32_bf16 v[24:27], a[192:195], v[168:171], v[24:27]\n"
"v_cvt_pk_bf16_f32 v96, v96, v97\n"
"v_cvt_pk_bf16_f32 v97, v98, v99\n"
"v_mfma_f32_16x16x32_bf16 v[24:27], a[200:203], v[172:175], v[24:27]\n"
"ds_read_b128 v[160:163], v221 offset:47104\n"
"v_cvt_pk_bf16_f32 v98, v104, v105\n"
"v_mfma_f32_16x16x32_bf16 v[24:27], a[208:211], v[176:179], v[24:27]\n"
"v_cvt_pk_bf16_f32 v99, v106, v107\n"
"v_pk_max_i16 v96, v96, 0\n"
"v_mfma_f32_16x16x32_bf16 v[24:27], a[216:219], v[180:183], v[24:27]\n"
"ds_read_b128 v[164:167], v222 offset:47104\n"
"buffer_load_dwordx4 v15, s[12:15], 0 offen offset:3072 lds\n"
"v_mfma_f32_16x16x32_bf16 v[28:31], a[196:199], v[168:171], v[28:31]\n"
"s_waitcnt vmcnt(20)\n"
"s_barrier\n"
"v_mfma_f32_16x16x32_bf16 v[28:31], a[204:207], v[172:175], v[28:31]\n"
"ds_read_b128 v[168:171], v221 offset:49152\n"
"s_add_u32 s65, s65, 384\n"
"v_mfma_f32_16x16x32_bf16 v[28:31], a[212:215], v[176:179], v[28:31]\n"
"s_and_b32 s65, s65, 2047\n"
"s_add_u32 s12, s3, s65\n"
"v_mfma_f32_16x16x32_bf16 v[28:31], a[220:223], v[180:183], v[28:31]\n"
"ds_read_b128 v[172:175], v222 offset:49152\n"
"s_waitcnt lgkmcnt(6)\n"
"v_mfma_f32_16x16x32_bf16 v[16:19], a[224:227], v[184:187], v[16:19]\n"
"s_add_u32 m0, s63, 16384\n"
"v_pk_max_i16 v97, v97, 0\n"
"v_mfma_f32_16x16x32_bf16 v[16:19], a[232:235], v[188:191], v[16:19]\n"
"ds_read_b128 v[176:179], v221 offset:51200\n"
"v_pk_max_i16 v98, v98, 0\n"
"v_mfma_f32_16x16x32_bf16 v[16:19], a[240:243], v[192:195], v[16:19]\n"
"buffer_load_dwordx4 v14, s[12:15], 0 offen offset:0 lds\n"
"v_pk_max_i16 v99, v99, 0\n"
"v_mfma_f32_16x16x32_bf16 v[16:19], a[248:251], v[196:199], v[16:19]\n"
"ds_read_b128 v[180:183], v222 offset:51200\n"
"v_mul_f32_e64 v100, v100, abs(v100)\n"
"v_mfma_f32_16x16x32_bf16 v[20:23], a[228:231], v[184:187], v[20:23]\n"
".Lk_h4:\n"
"s_cbranch_execz .Lk_h8\n"
"v_mul_f32_e64 v101, v101, abs(v101)\n"
"v_mul_f32_e64 v102, v102, abs(v102)\n"
"v_mfma_f32_16x16x32_bf16 v[20:23], a[236:239], v[188:191], v[20:23]\n"
"ds_read_b128 v[184:187], v221 offset:53248\n"
"v_mul_f32_e64 v103, v103, abs(v103)\n"
"v_mfma_f32_16x16x32_bf16 v[20:23], a[244:247], v[192:195], v[20:23]\n"
"v_fma_f32 v2, v250, v16, v17\n"
"v_mul_f32_e64 v108, v108, abs(v108)\n"
"v_mfma_f32_16x16x32_bf16 v[20:23], a[252:255], v[196:199], v[20:23]\n"
"ds_read_b128 v[188:191], v222 offset:53248\n"
"s_waitcnt lgkmcnt(6)\n"
"v_mfma_f32_16x16x32_bf16 v[24:27], a[224:227], v[152:155], v[24:27]\n"
"v_mul_f32_e64 v109, v109, abs(v109)\n"
"v_mul_f32_e64 v110, v110, abs(v110)\n"
"v_mfma_f32_16x16x32_bf16 v[24:27], a[232:235], v[156:159], v[24:27]\n"
"ds_read_b128 v[192:195], v221 offset:55296\n"
"buffer_load_dwordx4 v15, s[12:15], 0 offen offset:1024 lds\n"
"v_mfma_f32_16x16x32_bf16 v[24:27], a[240:243], v[160:163], v[24:27]\n"
"s_add_u32 s65, s65, 128\n"
"s_and_b32 s65, s65, 2047\n"
"v_mfma_f32_16x16x32_bf16 v[24:27], a[248:251], v[164:167], v[24:27]\n"
"ds_read_b128 v[196:199], v222 offset:55296\n"
"s_add_u32 s12, s66, s65\n"
"v_mfma_f32_16x16x32_bf16 v[28:31], a[228:231], v[152:155], v[28:31]\n"
"v_mul_f32_e64 v111, v111, abs(v111)\n"
"v_cvt_pk_bf16_f32 v100, v100, v101\n"
"v_mfma_f32_16x16x32_bf16 v[28:31], a[236:239], v[156:159], v[28:31]\n"
"ds_read_b128 v[152:155], v221 offset:57344\n"
"v_cvt_pk_bf16_f32 v101, v102, v103\n"
"v_mfma_f32_16x16x32_bf16 v[28:31], a[244:247], v[160:163], v[28:31]\n"
"v_fma_f32 v3, v250, v24, v25\n"
"v_fma_f32 v2, -v251, v24, v2\n"
"v_mfma_f32_16x16x32_bf16 v[28:31], a[252:255], v[164:167], v[28:31]\n"
"ds_read_b128 v[156:159], v222 offset:57344\n"
"s_waitcnt lgkmcnt(6)\n"
"v_mfma_f32_16x16x32_bf16 v[32:35], a[0:3], v[168:171], 0\n"
"buffer_load_dwordx4 v14, s[12:15], 0 offen offset:2048 lds\n"
"v_fmac_f32 v3, v251, v16\n"
"v_mfma_f32_16x16x32_bf16 v[32:35], a[8:11], v[172:175], v[32:35]\n"
"ds_read_b128 v[160:163], v221 offset:59392\n"
"v_fma_f32 v216, v250, v2, v18\n"
"v_mfma_f32_16x16x32_bf16 v[32:35], a[16:19], v[176:179], v[32:35]\n"
"v_fma_f32 v217, v250, v3, v26\n"
"v_fma_f32 v216, -v251, v3, v216\n"
"v_mfma_f32_16x16x32_bf16 v[32:35], a[24:27], v[180:183], v[32:35]\n"
"ds_read_b128 v[164:167], v222 offset:59392\n"
"v_fmac_f32 v217, v251, v2\n"
"v_mfma_f32_16x16x32_bf16 v[36:39], a[4:7], v[168:171], 0\n"
"v_fma_f32 v2, v250, v216, v19\n"
"v_fma_f32 v3, v250, v217, v27\n"
"v_mfma_f32_16x16x32_bf16 v[36:39], a[12:15], v[172:175], v[36:39]\n"
"ds_read_b128 v[168:171], v221 offset:61440\n"
"v_fma_f32 v2, -v251, v217, v2\n"
"v_mfma_f32_16x16x32_bf16 v[36:39], a[20:23], v[176:179], v[36:39]\n"
"v_fmac_f32 v3, v251, v216\n"
"v_fma_f32 v216, v250, v2, v20\n"
"v_mfma_f32_16x16x32_bf16 v[36:39], a[28:31], v[180:183], v[36:39]\n"
"ds_read_b128 v[172:175], v222 offset:61440\n"
"s_waitcnt lgkmcnt(6)\n"
"v_mfma_f32_16x16x32_bf16 v[40:43], a[0:3], v[184:187], 0\n"
"buffer_load_dwordx4 v15, s[12:15], 0 offen offset:3072 lds\n"
"s_add_u32 s65, s65, 384\n"
"v_mfma_f32_16x16x32_bf16 v[40:43], a[8:11], v[188:191], v[40:43]\n"
"ds_read_b128 v[176:179], v221 offset:63488\n"
"s_and_b32 s65, s65, 2047\n"
"v_mfma_f32_16x16x32_bf16 v[40:43], a[16:19], v[192:195], v[40:43]\n"
"s_add_u32 s12, s3, s65\n"
"s_add_u32 m0, s63, 32768\n"
"v_mfma_f32_16x16x32_bf16 v[40:43], a[24:27], v[196:199], v[40:43]\n"
"ds_read_b128 v[180:183], v222 offset:63488\n"
"v_fma_f32 v217, v250, v3, v28\n"
"v_mfma_f32_16x16x32_bf16 v[44:47], a[4:7], v[184:187], 0\n"
"s_waitcnt vmcnt(20)\n"
"s_barrier\n"
"v_mfma_f32_16x16x32_bf16 v[44:47], a[12:15], v[188:191], v[44:47]\n"
"ds_read_b128 v[184:187], v223 offset:0\n"
"v_fma_f32 v216, -v251, v3, v216\n"
"v_mfma_f32_16x16x32_bf16 v[44:47], a[20:23], v[192:195], v[44:47]\n"
"buffer_load_dwordx4 v14, s[12:15], 0 offen offset:0 lds\n"
"v_fmac_f32 v217, v251, v2\n"
"v_mfma_f32_16x16x32_bf16 v[44:47], a[28:31], v[196:199], v[44:47]\n"
"ds_read_b128 v[188:191], v224 offset:0\n"
"s_waitcnt lgkmcnt(6)\n"
"v_mfma_f32_16x16x32_bf16 v[32:35], a[32:35], v[152:155], v[32:35]\n"
"v_fma_f32 v2, v250, v216, v21\n"
"v_fma_f32 v3, v250, v217, v29\n"
"v_mfma_f32_16x16x32_bf16 v[32:35], a[40:43], v[156:159], v[32:35]\n"
"ds_read_b128 v[192:195], v223 offset:2048\n"
"v_fma_f32 v2, -v251, v217, v2\n"
"v_mfma_f32_16x16x32_bf16 v[32:35], a[48:51], v[160:163], v[32:35]\n"
"v_fmac_f32 v3, v251, v216\n"
"v_fma_f32 v216, v250, v2, v22\n"
"v_mfma_f32_16x16x32_bf16 v[32:35], a[56:59], v[164:167], v[32:35]\n"
"ds_read_b128 v[196:199], v224 offset:2048\n"
"v_fma_f32 v217, v250, v3, v30\n"
"v_mfma_f32_16x16x32_bf16 v[36:39], a[36:39], v[152:155], v[36:39]\n"
"v_fma_f32 v216, -v251, v3, v216\n"
"v_fmac_f32 v217, v251, v2\n"
"v_mfma_f32_16x16x32_bf16 v[36:39], a[44:47], v[156:159], v[36:39]\n"
"ds_read_b128 v[152:155], v223 offset:4096\n"
"buffer_load_dwordx4 v15, s[12:15], 0 offen offset:1024 lds\n"
"v_mfma_f32_16x16x32_bf16 v[36:39], a[52:55], v[160:163], v[36:39]\n"
"s_add_u32 s65, s65, 128\n"
"s_and_b32 s65, s65, 2047\n"
"v_mfma_f32_16x16x32_bf16 v[36:39], a[60:63], v[164:167], v[36:39]\n"
"ds_read_b128 v[156:159], v224 offset:4096\n"
"s_waitcnt lgkmcnt(6)\n"
"v_mfma_f32_16x16x32_bf16 v[40:43], a[32:35], v[168:171], v[40:43]\n"
"s_add_u32 s12, s66, s65\n"
"v_fma_f32 v2, v250, v216, v23\n"
"v_mfma_f32_16x16x32_bf16 v[40:43], a[40:43], v[172:175], v[40:43]\n"
"ds_read_b128 v[160:163], v223 offset:6144\n"
"v_fma_f32 v3, v250, v217, v31\n"
"v_mfma_f32_16x16x32_bf16 v[40:43], a[48:51], v[176:179], v[40:43]\n"
"v_fma_f32 v2, -v251, v217, v2\n"
"v_fmac_f32 v3, v251, v216\n"
"v_mfma_f32_16x16x32_bf16 v[40:43], a[56:59], v[180:183], v[40:43]\n"
"ds_read_b128 v[164:167], v224 offset:6144\n"
"v_cvt_pk_bf16_f32 v102, v108, v109\n"
"v_mfma_f32_16x16x32_bf16 v[44:47], a[36:39], v[168:171], v[44:47]\n"
"ds_write_b64 v234, v[2:3] offset:0\n"
"v_mfma_f32_16x16x32_bf16 v[44:47], a[44:47], v[172:175], v[44:47]\n"
"ds_read_b128 v[168:171], v223 offset:8192\n"
"buffer_load_dwordx4 v14, s[12:15], 0 offen offset:2048 lds\n"
"v_mfma_f32_16x16x32_bf16 v[44:47], a[52:55], v[176:179], v[44:47]\n"
"v_cvt_pk_bf16_f32 v103, v110, v111\n"
"v_pk_max_i16 v100, v100, 0\n"
"v_mfma_f32_16x16x32_bf16 v[44:47], a[60:63], v[180:183], v[44:47]\n"
"ds_read_b128 v[172:175], v224 offset:8192\n"
"s_waitcnt lgkmcnt(7)\n"
"v_mfma_f32_16x16x32_bf16 v[32:35], a[64:67], v[184:187], v[32:35]\n"
"v_pk_max_i16 v101, v101, 0\n"
"v_pk_max_i16 v102, v102, 0\n"
"v_mfma_f32_16x16x32_bf16 v[32:35], a[72:75], v[188:191], v[32:35]\n"
"ds_read_b128 v[176:179], v223 offset:10240\n"
"v_pk_max_i16 v103, v103, 0\n"
"v_mfma_f32_16x16x32_bf16 v[32:35], a[80:83], v[192:195], v[32:35]\n"
"buffer_store_dwordx4 v[80:83], v200, s[36:39], s91 offen offset:0 sc1\n"
"v_mfma_f32_16x16x32_bf16 v[32:35], a[88:91], v[196:199], v[32:35]\n"
"ds_read_b128 v[180:183], v224 offset:10240\n"
"v_mul_f32_e64 v112, v112, abs(v112)\n"
"v_mfma_f32_16x16x32_bf16 v[36:39], a[68:71], v[184:187], v[36:39]\n"
"v_mul_f32_e64 v113, v113, abs(v113)\n"
"v_mul_f32_e64 v114, v114, abs(v114)\n"
"v_mfma_f32_16x16x32_bf16 v[36:39], a[76:79], v[188:191], v[36:39]\n"
"ds_read_b128 v[184:187], v223 offset:12288\n"
"v_mul_f32_e64 v115, v115, abs(v115)\n"
"v_mfma_f32_16x16x32_bf16 v[36:39], a[84:87], v[192:195], v[36:39]\n"
"buffer_store_dwordx4 v[84:87], v200, s[36:39], s91 offen offset:1024 sc1\n"
"v_mfma_f32_16x16x32_bf16 v[36:39], a[92:95], v[196:199], v[36:39]\n"
"ds_read_b128 v[188:191], v224 offset:12288\n"
"s_waitcnt lgkmcnt(7)\n"
"v_mfma_f32_16x16x32_bf16 v[40:43], a[64:67], v[152:155], v[40:43]\n"
"v_mul_f32_e64 v120, v120, abs(v120)\n"
"v_mul_f32_e64 v121, v121, abs(v121)\n"
"v_mfma_f32_16x16x32_bf16 v[40:43], a[72:75], v[156:159], v[40:43]\n"
"ds_read_b128 v[192:195], v223 offset:14336\n"
"v_mul_f32_e64 v122, v122, abs(v122)\n"
"v_mfma_f32_16x16x32_bf16 v[40:43], a[80:83], v[160:163], v[40:43]\n"
"buffer_store_dwordx4 v[96:99], v206, s[36:39], s91 offen offset:0 sc1\n"
"v_mfma_f32_16x16x32_bf16 v[40:43], a[88:91], v[164:167], v[40:43]\n"
"ds_read_b128 v[196:199], v224 offset:14336\n"
"v_mul_f32_e64 v123, v123, abs(v123)\n"
"v_mfma_f32_16x16x32_bf16 v[44:47], a[68:71], v[152:155], v[44:47]\n"
"s_waitcnt vmcnt(18)\n"
"s_barrier\n"
"v_mfma_f32_16x16x32_bf16 v[44:47], a[76:79], v[156:159], v[44:47]\n"
"ds_read_b128 v[152:155], v223 offset:16384\n"
"v_cvt_pk_bf16_f32 v112, v112, v113\n"
"v_mfma_f32_16x16x32_bf16 v[44:47], a[84:87], v[160:163], v[44:47]\n"
"buffer_store_dwordx4 v[100:103], v206, s[36:39], s91 offen offset:1024 sc1\n"
"v_mfma_f32_16x16x32_bf16 v[44:47], a[92:95], v[164:167], v[44:47]\n"
"ds_read_b128 v[156:159], v224 offset:16384\n"
"s_waitcnt lgkmcnt(6)\n"
"v_mfma_f32_16x16x32_bf16 v[32:35], a[96:99], v[168:171], v[32:35]\n"
"v_readlane_b32 s91, v207, 32\n"
"v_cvt_pk_bf16_f32 v113, v114, v115\n"
"v_mfma_f32_16x16x32_bf16 v[32:35], a[104:107], v[172:175], v[32:35]\n"
"ds_read_b128 v[160:163], v223 offset:18432\n"
"s_lshl_b32 s91, s91, 5\n"
"v_mfma_f32_16x16x32_bf16 v[32:35], a[112:115], v[176:179], v[32:35]\n"
"buffer_load_dwordx4 v15, s[12:15], 0 offen offset:3072 lds\n"
"s_add_u32 s65, s65, 384\n"
"v_mfma_f32_16x16x32_bf16 v[32:35], a[120:123], v[180:183], v[32:35]\n"
"ds_read_b128 v[164:167], v224 offset:18432\n"
"s_and_b32 s65, s65, 2047\n"
"v_mfma_f32_16x16x32_bf16 v[36:39], a[100:103], v[168:171], v[36:39]\n"
"s_add_u32 s12, s3, s65\n"
"s_add_u32 m0, s63, 49152\n"
"v_mfma_f32_16x16x32_bf16 v[36:39], a[108:111], v[172:175], v[36:39]\n"
"ds_read_b128 v[168:171], v223 offset:20480\n"
"s_add_u32 s91, s91, s1\n"
"v_mfma_f32_16x16x32_bf16 v[36:39], a[116:119], v[176:179], v[36:39]\n"
"buffer_load_dwordx4 v14, s[12:15], 0 offen offset:0 lds\n"
"v_cvt_pk_bf16_f32 v114, v120, v121\n"
"v_mfma_f32_16x16x32_bf16 v[36:39], a[124:127], v[180:183], v[36:39]\n"
"ds_read_b128 v[172:175], v224 offset:20480\n"
"s_waitcnt lgkmcnt(6)\n"
"v_mfma_f32_16x16x32_bf16 v[40:43], a[96:99], v[184:187], v[40:43]\n"
"v_cvt_pk_bf16_f32 v115, v122, v123\n"
"v_pk_max_i16 v112, v112, 0\n"
"v_mfma_f32_16x16x32_bf16 v[40:43], a[104:107], v[188:191], v[40:43]\n"
"ds_read_b128 v[176:179], v223 offset:22528\n"
"v_pk_max_i16 v113, v113, 0\n"
"v_mfma_f32_16x16x32_bf16 v[40:43], a[112:115], v[192:195], v[40:43]\n"
"buffer_load_dwordx4 v15, s[12:15], 0 offen offset:1024 lds\n"
"s_add_u32 s65, s65, 128\n"
"v_mfma_f32_16x16x32_bf16 v[40:43], a[120:123], v[196:199], v[40:43]\n"
"ds_read_b128 v[180:183], v224 offset:22528\n"
"s_and_b32 s65, s65, 2047\n"
"v_mfma_f32_16x16x32_bf16 v[44:47], a[100:103], v[184:187], v[44:47]\n"
"s_add_u32 s12, s66, s65\n"
"v_pk_max_i16 v114, v114, 0\n"
"v_mfma_f32_16x16x32_bf16 v[44:47], a[108:111], v[188:191], v[44:47]\n"
"ds_read_b128 v[184:187], v223 offset:24576\n"
"v_pk_max_i16 v115, v115, 0\n"
"v_mfma_f32_16x16x32_bf16 v[44:47], a[116:119], v[192:195], v[44:47]\n"
"buffer_load_dwordx4 v14, s[12:15], 0 offen offset:2048 lds\n"
"v_mul_f32_e64 v116, v116, abs(v116)\n"
"v_mfma_f32_16x16x32_bf16 v[44:47], a[124:127], v[196:199], v[44:47]\n"
"ds_read_b128 v[188:191], v224 offset:24576\n"
"s_waitcnt lgkmcnt(6)\n"
"v_mfma_f32_16x16x32_bf16 v[32:35], a[128:131], v[152:155], v[32:35]\n"
"v_mul_f32_e64 v117, v117, abs(v117)\n"
"v_mul_f32_e64 v118, v118, abs(v118)\n"
"v_mfma_f32_16x16x32_bf16 v[32:35], a[136:139], v[156:159], v[32:35]\n"
"ds_read_b128 v[192:195], v223 offset:26624\n"
"v_mul_f32_e64 v119, v119, abs(v119)\n"
"v_mfma_f32_16x16x32_bf16 v[32:35], a[144:147], v[160:163], v[32:35]\n"
"v_mul_f32_e64 v124, v124, abs(v124)\n"
"v_mul_f32_e64 v125, v125, abs(v125)\n"
"v_mfma_f32_16x16x32_bf16 v[32:35], a[152:155], v[164:167], v[32:35]\n"
"ds_read_b128 v[196:199], v224 offset:26624\n"
"buffer_load_dwordx4 v15, s[12:15], 0 offen offset:3072 lds\n"
"v_mfma_f32_16x16x32_bf16 v[36:39], a[132:135], v[152:155], v[36:39]\n"
"s_add_u32 s3, s3, 32768\n"
"s_add_u32 s66, s66, 32768\n"
"v_mfma_f32_16x16x32_bf16 v[36:39], a[140:143], v[156:159], v[36:39]\n"
"ds_read_b128 v[152:155], v223 offset:28672\n"
"v_mul_f32_e64 v126, v126, abs(v126)\n"
"v_mfma_f32_16x16x32_bf16 v[36:39], a[148:151], v[160:163], v[36:39]\n"
"v_mul_f32_e64 v127, v127, abs(v127)\n"
"v_cvt_pk_bf16_f32 v116, v116, v117\n"
"v_mfma_f32_16x16x32_bf16 v[36:39], a[156:159], v[164:167], v[36:39]\n"
"ds_read_b128 v[156:159], v224 offset:28672\n"
"s_waitcnt lgkmcnt(6)\n"
"v_mfma_f32_16x16x32_bf16 v[40:43], a[128:131], v[168:171], v[40:43]\n"
"s_add_u32 s65, s65, 384\n"
"s_and_b32 s65, s65, 2047\n"
"v_mfma_f32_16x16x32_bf16 v[40:43], a[136:139], v[172:175], v[40:43]\n"
"ds_read_b128 v[160:163], v223 offset:30720\n"
"s_add_u32 s12, s3, s65\n"
"v_mfma_f32_16x16x32_bf16 v[40:43], a[144:147], v[176:179], v[40:43]\n"
"s_add_u32 m0, s63, 65536\n"
"v_cvt_pk_bf16_f32 v117, v118, v119\n"
"v_mfma_f32_16x16x32_bf16 v[40:43], a[152:155], v[180:183], v[40:43]\n"
"ds_read_b128 v[164:167], v224 offset:30720\n"
"v_cvt_pk_bf16_f32 v118, v124, v125\n"
"v_mfma_f32_16x16x32_bf16 v[44:47], a[132:135], v[168:171], v[44:47]\n"
"s_waitcnt vmcnt(20)\n"
"s_barrier\n"
"v_mfma_f32_16x16x32_bf16 v[44:47], a[140:143], v[172:175], v[44:47]\n"
"ds_read_b128 v[168:171], v223 offset:32768\n"
"v_cvt_pk_bf16_f32 v119, v126, v127\n"
"v_mfma_f32_16x16x32_bf16 v[44:47], a[148:151], v[176:179], v[44:47]\n"
"buffer_load_dwordx4 v14, s[12:15], 0 offen offset:0 lds\n"
"v_pk_max_i16 v116, v116, 0\n"
"v_mfma_f32_16x16x32_bf16 v[44:47], a[156:159], v[180:183], v[44:47]\n"
"ds_read_b128 v[172:175], v224 offset:32768\n"
"s_waitcnt lgkmcnt(6)\n"
"v_mfma_f32_16x16x32_bf16 v[32:35], a[160:163], v[184:187], v[32:35]\n"
"v_pk_max_i16 v117, v117, 0\n"
"v_pk_max_i16 v118, v118, 0\n"
"v_mfma_f32_16x16x32_bf16 v[32:35], a[168:171], v[188:191], v[32:35]\n"
"ds_read_b128 v[176:179], v223 offset:34816\n"
"v_pk_max_i16 v119, v119, 0\n"
"v_mfma_f32_16x16x32_bf16 v[32:35], a[176:179], v[192:195], v[32:35]\n"
"v_mul_f32_e64 v128, v128, abs(v128)\n"
"v_mul_f32_e64 v129, v129, abs(v129)\n"
"v_mfma_f32_16x16x32_bf16 v[32:35], a[184:187], v[196:199], v[32:35]\n"
"ds_read_b128 v[180:183], v224 offset:34816\n"
"v_mul_f32_e64 v130, v130, abs(v130)\n"
"v_mfma_f32_16x16x32_bf16 v[36:39], a[164:167], v[184:187], v[36:39]\n"
"v_mul_f32_e64 v131, v131, abs(v131)\n"
"v_mul_f32_e64 v136, v136, abs(v136)\n"
"v_mfma_f32_16x16x32_bf16 v[36:39], a[172:175], v[188:191], v[36:39]\n"
"ds_read_b128 v[184:187], v223 offset:36864\n"
"buffer_load_dwordx4 v15, s[12:15], 0 offen offset:1024 lds\n"
"v_mfma_f32_16x16x32_bf16 v[36:39], a[180:183], v[192:195], v[36:39]\n"
"s_add_u32 s65, s65, 128\n"
"s_and_b32 s65, s65, 2047\n"
"v_mfma_f32_16x16x32_bf16 v[36:39], a[188:191], v[196:199], v[36:39]\n"
"ds_read_b128 v[188:191], v224 offset:36864\n"
"s_waitcnt lgkmcnt(6)\n"
"v_mfma_f32_16x16x32_bf16 v[40:43], a[160:163], v[152:155], v[40:43]\n"
"s_add_u32 s12, s66, s65\n"
"v_mul_f32_e64 v137, v137, abs(v137)\n"
"v_mfma_f32_16x16x32_bf16 v[40:43], a[168:171], v[156:159], v[40:43]\n"
"ds_read_b128 v[192:195], v223 offset:38912\n"
"v_mul_f32_e64 v138, v138, abs(v138)\n"
"v_mfma_f32_16x16x32_bf16 v[40:43], a[176:179], v[160:163], v[40:43]\n"
"v_mul_f32_e64 v139, v139, abs(v139)\n"
"v_cvt_pk_bf16_f32 v128, v128, v129\n"
"v_mfma_f32_16x16x32_bf16 v[40:43], a[184:187], v[164:167], v[40:43]\n"
"ds_read_b128 v[196:199], v224 offset:38912\n"
"v_cvt_pk_bf16_f32 v129, v130, v131\n"
"v_mfma_f32_16x16x32_bf16 v[44:47], a[164:167], v[152:155], v[44:47]\n"
"buffer_load_dwordx4 v14, s[12:15], 0 offen offset:2048 lds\n"
"v_cvt_pk_bf16_f32 v130, v136, v137\n"
"v_mfma_f32_16x16x32_bf16 v[44:47], a[172:175], v[156:159], v[44:47]\n"
"ds_read_b128 v[152:155], v223 offset:40960\n"
"v_cvt_pk_bf16_f32 v131, v138, v139\n"
"v_mfma_f32_16x16x32_bf16 v[44:47], a[180:183], v[160:163], v[44:47]\n"
"v_pk_max_i16 v128, v128, 0\n"
"v_pk_max_i16 v129, v129, 0\n"
"v_mfma_f32_16x16x32_bf16 v[44:47], a[188:191], v[164:167], v[44:47]\n"
"ds_read_b128 v[156:159], v224 offset:40960\n"
"s_waitcnt lgkmcnt(6)\n"
"v_mfma_f32_16x16x32_bf16 v[32:35], a[192:195], v[168:171], v[32:35]\n"
"v_pk_max_i16 v130, v130, 0\n"
"v_pk_max_i16 v131, v131, 0\n"
"v_mfma_f32_16x16x32_bf16 v[32:35], a[200:203], v[172:175], v[32:35]\n"
"ds_read_b128 v[160:163], v223 offset:43008\n"
"v_mul_f32_e64 v132, v132, abs(v132)\n"
"v_mfma_f32_16x16x32_bf16 v[32:35], a[208:211], v[176:179], v[32:35]\n"
"v_mul_f32_e64 v133, v133, abs(v133)\n"
"v_mul_f32_e64 v134, v134, abs(v134)\n"
"v_mfma_f32_16x16x32_bf16 v[32:35], a[216:219], v[180:183], v[32:35]\n"
"ds_read_b128 v[164:167], v224 offset:43008\n"
"buffer_load_dwordx4 v15, s[12:15], 0 offen offset:3072 lds\n"
"v_mfma_f32_16x16x32_bf16 v[36:39], a[196:199], v[168:171], v[36:39]\n"
"v_mul_f32_e64 v135, v135, abs(v135)\n"
"v_mul_f32_e64 v140, v140, abs(v140)\n"
"v_mfma_f32_16x16x32_bf16 v[36:39], a[204:207], v[172:175], v[36:39]\n"
"ds_read_b128 v[168:171], v223 offset:45056\n"
"v_mul_f32_e64 v141, v141, abs(v141)\n"
"v_mfma_f32_16x16x32_bf16 v[36:39], a[212:215], v[176:179], v[36:39]\n"
"v_mul_f32_e64 v142, v142, abs(v142)\n"
"v_mul_f32_e64 v143, v143, abs(v143)\n"
"v_mfma_f32_16x16x32_bf16 v[36:39], a[220:223], v[180:183], v[36:39]\n"
"ds_read_b128 v[172:175], v224 offset:45056\n"
"s_waitcnt lgkmcnt(6)\n"
"v_mfma_f32_16x16x32_bf16 v[40:43], a[192:195], v[184:187], v[40:43]\n"
"s_add_u32 s65, s65, 384\n"
"s_and_b32 s65, s65, 2047\n"
"v_mfma_f32_16x16x32_bf16 v[40:43], a[200:203], v[188:191], v[40:43]\n"
"ds_read_b128 v[176:179], v223 offset:47104\n"
"s_add_u32 s12, s3, s65\n"
"v_mfma_f32_16x16x32_bf16 v[40:43], a[208:211], v[192:195], v[40:43]\n"
"s_add_u32 m0, s63, 81920\n"
"v_cvt_pk_bf16_f32 v132, v132, v133\n"
"v_mfma_f32_16x16x32_bf16 v[40:43], a[216:219], v[196:199], v[40:43]\n"
"ds_read_b128 v[180:183], v224 offset:47104\n"
"v_cvt_pk_bf16_f32 v133, v134, v135\n"
"v_mfma_f32_16x16x32_bf16 v[44:47], a[196:199], v[184:187], v[44:47]\n"
"s_waitcnt vmcnt(20)\n"
"s_barrier\n"
"v_mfma_f32_16x16x32_bf16 v[44:47], a[204:207], v[188:191], v[44:47]\n"
"ds_read_b128 v[184:187], v221 offset:0\n"
"v_cvt_pk_bf16_f32 v134, v140, v141\n"
"v_mfma_f32_16x16x32_bf16 v[44:47], a[212:215], v[192:195], v[44:47]\n"
"buffer_load_dwordx4 v14, s[12:15], 0 offen offset:0 lds\n"
"v_cvt_pk_bf16_f32 v135, v142, v143\n"
"v_mfma_f32_16x16x32_bf16 v[44:47], a[220:223], v[196:199], v[44:47]\n"
"ds_read_b128 v[188:191], v222 offset:0\n"
"s_waitcnt lgkmcnt(6)\n"
"v_mfma_f32_16x16x32_bf16 v[32:35], a[224:227], v[152:155], v[32:35]\n"
"v_pk_max_i16 v132, v132, 0\n"
"v_pk_max_i16 v133, v133, 0\n"
"v_mfma_f32_16x16x32_bf16 v[32:35], a[232:235], v[156:159], v[32:35]\n"
"ds_read_b128 v[192:195], v221 offset:2048\n"
"v_pk_max_i16 v134, v134, 0\n"
"v_mfma_f32_16x16x32_bf16 v[32:35], a[240:243], v[160:163], v[32:35]\n"
"v_pk_max_i16 v135, v135, 0\n"
"v_mul_f32 v10, v237, v16\n"
"v_mfma_f32_16x16x32_bf16 v[32:35], a[248:251], v[164:167], v[32:35]\n"
"ds_read_b128 v[196:199], v222 offset:2048\n"
"v_mul_f32 v16, v236, v16\n"
"v_mfma_f32_16x16x32_bf16 v[36:39], a[228:231], v[152:155], v[36:39]\n"
"buffer_store_dwordx4 v[112:115], v200, s[36:39], s91 offen offset:0 sc1\n"
"v_mfma_f32_16x16x32_bf16 v[36:39], a[236:239], v[156:159], v[36:39]\n"
"ds_read_b128 v[152:155], v221 offset:4096\n"
"v_fma_f32 v16, -v237, v24, v16\n"
"v_mfma_f32_16x16x32_bf16 v[36:39], a[244:247], v[160:163], v[36:39]\n"
"v_fma_f32 v2, v252, v32, v33\n"
"v_fma_f32 v24, v236, v24, v10\n"
"v_mfma_f32_16x16x32_bf16 v[36:39], a[252:255], v[164:167], v[36:39]\n"
"ds_read_b128 v[156:159], v222 offset:4096\n"
"s_waitcnt lgkmcnt(6)\n"
"v_mfma_f32_16x16x32_bf16 v[40:43], a[224:227], v[168:171], v[40:43]\n"
"buffer_store_dwordx4 v[116:119], v200, s[36:39], s91 offen offset:1024 sc1\n"
"v_mfma_f32_16x16x32_bf16 v[40:43], a[232:235], v[172:175], v[40:43]\n"
"ds_read_b128 v[160:163], v221 offset:6144\n"
"v_mul_f32 v11, v237, v17\n"
"v_mfma_f32_16x16x32_bf16 v[40:43], a[240:243], v[176:179], v[40:43]\n"
"v_mul_f32 v17, v236, v17\n"
"v_fma_f32 v17, -v237, v25, v17\n"
"v_mfma_f32_16x16x32_bf16 v[40:43], a[248:251], v[180:183], v[40:43]\n"
"ds_read_b128 v[164:167], v222 offset:6144\n"
"v_fma_f32 v25, v236, v25, v11\n"
"v_mfma_f32_16x16x32_bf16 v[44:47], a[228:231], v[168:171], v[44:47]\n"
"buffer_store_dwordx4 v[128:131], v206, s[36:39], s91 offen offset:0 sc1\n"
"v_mfma_f32_16x16x32_bf16 v[44:47], a[236:239], v[172:175], v[44:47]\n"
"ds_read_b128 v[168:171], v221 offset:8192\n"
"v_mul_f32 v10, v237, v18\n"
"v_mfma_f32_16x16x32_bf16 v[44:47], a[244:247], v[176:179], v[44:47]\n"
"v_fma_f32 v3, v252, v40, v41\n"
"v_fma_f32 v2, -v253, v40, v2\n"
"v_mfma_f32_16x16x32_bf16 v[44:47], a[252:255], v[180:183], v[44:47]\n"
"ds_read_b128 v[172:175], v222 offset:8192\n"
"s_waitcnt lgkmcnt(6)\n"
"v_mfma_f32_16x16x32_bf16 v[48:51], a[0:3], v[184:187], 0\n"
"buffer_store_dwordx4 v[132:135], v206, s[36:39], s91 offen offset:1024 sc1\n"
"v_mfma_f32_16x16x32_bf16 v[48:51], a[8:11], v[188:191], v[48:51]\n"
"ds_read_b128 v[176:179], v221 offset:10240\n"
"v_readlane_b32 s91, v207, 48\n"
"v_mfma_f32_16x16x32_bf16 v[48:51], a[16:19], v[192:195], v[48:51]\n"
"s_lshl_b32 s91, s91, 5\n"
"v_fmac_f32 v3, v253, v32\n"
"v_mfma_f32_16x16x32_bf16 v[48:51], a[24:27], v[196:199], v[48:51]\n"
"ds_read_b128 v[180:183], v222 offset:10240\n"
"v_fma_f32 v216, v252, v2, v34\n"
"v_mfma_f32_16x16x32_bf16 v[52:55], a[4:7], v[184:187], 0\n"
"buffer_load_dwordx4 v15, s[12:15], 0 offen offset:1024 lds\n"
"s_add_u32 s65, s65, 128\n"
"v_mfma_f32_16x16x32_bf16 v[52:55], a[12:15], v[188:191], v[52:55]\n"
"ds_read_b128 v[184:187], v221 offset:12288\n"
"s_and_b32 s65, s65, 2047\n"
"v_mfma_f32_16x16x32_bf16 v[52:55], a[20:23], v[192:195], v[52:55]\n"
"s_add_u32 s12, s66, s65\n"
"v_fma_f32 v217, v252, v3, v42\n"
"v_mfma_f32_16x16x32_bf16 v[52:55], a[28:31], v[196:199], v[52:55]\n"
"ds_read_b128 v[188:191], v222 offset:12288\n"
"s_waitcnt lgkmcnt(6)\n"
"v_mfma_f32_16x16x32_bf16 v[56:59], a[0:3], v[152:155], 0\n"
"buffer_load_dwordx4 v14, s[12:15], 0 offen offset:2048 lds\n"
"v_fma_f32 v216, -v253, v3, v216\n"
"v_mfma_f32_16x16x32_bf16 v[56:59], a[8:11], v[156:159], v[56:59]\n"
"ds_read_b128 v[192:195], v221 offset:14336\n"
"v_fmac_f32 v217, v253, v2\n"
"v_mfma_f32_16x16x32_bf16 v[56:59], a[16:19], v[160:163], v[56:59]\n"
"v_fma_f32 v2, v252, v216, v35\n"
"v_fma_f32 v3, v252, v217, v43\n"
"v_mfma_f32_16x16x32_bf16 v[56:59], a[24:27], v[164:167], v[56:59]\n"
"ds_read_b128 v[196:199], v222 offset:14336\n"
"v_fma_f32 v2, -v253, v217, v2\n"
"v_mfma_f32_16x16x32_bf16 v[60:63], a[4:7], v[152:155], 0\n"
"s_waitcnt vmcnt(23)\n"
"s_barrier\n"
"v_mfma_f32_16x16x32_bf16 v[60:63], a[12:15], v[156:159], v[60:63]\n"
"ds_read_b128 v[152:155], v221 offset:16384\n"
"buffer_load_dwordx4 v15, s[12:15], 0 offen offset:3072 lds\n"
"v_mfma_f32_16x16x32_bf16 v[60:63], a[20:23], v[160:163], v[60:63]\n"
"s_add_u32 s65, s65, 384\n"
"s_and_b32 s65, s65, 2047\n"
"v_mfma_f32_16x16x32_bf16 v[60:63], a[28:31], v[164:167], v[60:63]\n"
"ds_read_b128 v[156:159], v222 offset:16384\n"
"s_waitcnt lgkmcnt(6)\n"
"v_mfma_f32_16x16x32_bf16 v[48:51], a[32:35], v[168:171], v[48:51]\n"
"s_add_u32 s12, s3, s65\n"
"s_add_u32 m0, s63, 98304\n"
"v_mfma_f32_16x16x32_bf16 v[48:51], a[40:43], v[172:175], v[48:51]\n"
"ds_read_b128 v[160:163], v221 offset:18432\n"
"buffer_load_dwordx4 v14, s[12:15], 0 offen offset:0 lds\n"
"v_mfma_f32_16x16x32_bf16 v[48:51], a[48:51], v[176:179], v[48:51]\n"
"v_fmac_f32 v3, v253, v216\n"
"v_fma_f32 v216, v252, v2, v36\n"
"v_mfma_f32_16x16x32_bf16 v[48:51], a[56:59], v[180:183], v[48:51]\n"
"ds_read_b128 v[164:167], v222 offset:18432\n"
"v_fma_f32 v217, v252, v3, v44\n"
"v_mfma_f32_16x16x32_bf16 v[52:55], a[36:39], v[168:171], v[52:55]\n"
"v_fma_f32 v216, -v253, v3, v216\n"
"v_fmac_f32 v217, v253, v2\n"
"v_mfma_f32_16x16x32_bf16 v[52:55], a[44:47], v[172:175], v[52:55]\n"
"ds_read_b128 v[168:171], v221 offset:20480\n"
"buffer_load_dwordx4 v15, s[12:15], 0 offen offset:1024 lds\n"
"v_mfma_f32_16x16x32_bf16 v[52:55], a[52:55], v[176:179], v[52:55]\n"
"s_add_u32 s65, s65, 128\n"
"s_and_b32 s65, s65, 2047\n"
"v_mfma_f32_16x16x32_bf16 v[52:55], a[60:63], v[180:183], v[52:55]\n"
"ds_read_b128 v[172:175], v222 offset:20480\n"
"s_waitcnt lgkmcnt(6)\n"
"v_mfma_f32_16x16x32_bf16 v[56:59], a[32:35], v[184:187], v[56:59]\n"
"s_add_u32 s12, s66, s65\n"
"v_fma_f32 v2, v252, v216, v37\n"
"v_mfma_f32_16x16x32_bf16 v[56:59], a[40:43], v[188:191], v[56:59]\n"
"ds_read_b128 v[176:179], v221 offset:22528\n"
"v_fma_f32 v3, v252, v217, v45\n"
"v_mfma_f32_16x16x32_bf16 v[56:59], a[48:51], v[192:195], v[56:59]\n"
"v_fma_f32 v2, -v253, v217, v2\n"
"v_fmac_f32 v3, v253, v216\n"
"v_mfma_f32_16x16x32_bf16 v[56:59], a[56:59], v[196:199], v[56:59]\n"
"ds_read_b128 v[180:183], v222 offset:22528\n"
"v_fma_f32 v216, v252, v2, v38\n"
"v_mfma_f32_16x16x32_bf16 v[60:63], a[36:39], v[184:187], v[60:63]\n"
"buffer_load_dwordx4 v14, s[12:15], 0 offen offset:2048 lds\n"
"v_fma_f32 v217, v252, v3, v46\n"
"v_mfma_f32_16x16x32_bf16 v[60:63], a[44:47], v[188:191], v[60:63]\n"
"ds_read_b128 v[184:187], v221 offset:24576\n"
"v_fma_f32 v216, -v253, v3, v216\n"
"v_mfma_f32_16x16x32_bf16 v[60:63], a[52:55], v[192:195], v[60:63]\n"
"v_fmac_f32 v217, v253, v2\n"
"v_fma_f32 v2, v252, v216, v39\n"
"v_mfma_f32_16x16x32_bf16 v[60:63], a[60:63], v[196:199], v[60:63]\n"
"ds_read_b128 v[188:191], v222 offset:24576\n"
"s_waitcnt lgkmcnt(6)\n"
"v_mfma_f32_16x16x32_bf16 v[48:51], a[64:67], v[152:155], v[48:51]\n"
"v_fma_f32 v3, v252, v217, v47\n"
"v_fma_f32 v2, -v253, v217, v2\n"
"v_mfma_f32_16x16x32_bf16 v[48:51], a[72:75], v[156:159], v[48:51]\n"
"ds_read_b128 v[192:195], v221 offset:26624\n"
"v_fmac_f32 v3, v253, v216\n"
"v_mfma_f32_16x16x32_bf16 v[48:51], a[80:83], v[160:163], v[48:51]\n"
"ds_write_b64 v234, v[2:3] offset:128\n"
"v_mfma_f32_16x16x32_bf16 v[48:51], a[88:91], v[164:167], v[48:51]\n"
"ds_read_b128 v[196:199], v222 offset:26624\n"
"buffer_load_dwordx4 v15, s[12:15], 0 offen offset:3072 lds\n"
"v_mfma_f32_16x16x32_bf16 v[52:55], a[68:71], v[152:155], v[52:55]\n"
"v_mul_f32 v18, v236, v18\n"
"v_fma_f32 v18, -v237, v26, v18\n"
"v_mfma_f32_16x16x32_bf16 v[52:55], a[76:79], v[156:159], v[52:55]\n"
"ds_read_b128 v[152:155], v221 offset:28672\n"
"v_fma_f32 v26, v236, v26, v10\n"
"v_mfma_f32_16x16x32_bf16 v[52:55], a[84:87], v[160:163], v[52:55]\n"
"v_mul_f32 v11, v237, v19\n"
"v_mul_f32 v19, v236, v19\n"
"v_mfma_f32_16x16x32_bf16 v[52:55], a[92:95], v[164:167], v[52:55]\n"
"ds_read_b128 v[156:159], v222 offset:28672\n"
"s_waitcnt lgkmcnt(7)\n"
"v_mfma_f32_16x16x32_bf16 v[56:59], a[64:67], v[168:171], v[56:59]\n"
"s_add_u32 s65, s65, 384\n"
"s_and_b32 s65, s65, 2047\n"
"v_mfma_f32_16x16x32_bf16 v[56:59], a[72:75], v[172:175], v[56:59]\n"
"ds_read_b128 v[160:163], v221 offset:30720\n"
"s_add_u32 s12, s3, s65\n"
"v_mfma_f32_16x16x32_bf16 v[56:59], a[80:83], v[176:179], v[56:59]\n"
"s_add_u32 m0, s63, 0\n"
"v_fma_f32 v19, -v237, v27, v19\n"
"v_mfma_f32_16x16x32_bf16 v[56:59], a[88:91], v[180:183], v[56:59]\n"
"ds_read_b128 v[164:167], v222 offset:30720\n"
"v_fma_f32 v27, v236, v27, v11\n"
"v_mfma_f32_16x16x32_bf16 v[60:63], a[68:71], v[168:171], v[60:63]\n"
"s_waitcnt vmcnt(20)\n"
"s_barrier\n"
"v_mfma_f32_16x16x32_bf16 v[60:63], a[76:79], v[172:175], v[60:63]\n"
"ds_read_b128 v[168:171], v221 offset:32768\n"
"v_mul_f32 v10, v237, v20\n"
"v_mfma_f32_16x16x32_bf16 v[60:63], a[84:87], v[176:179], v[60:63]\n"
".Lk_h5:\n"
"s_cbranch_execz .Lk_h9\n"
"buffer_load_dwordx4 v14, s[12:15], 0 offen offset:0 lds\n"
"v_mul_f32 v20, v236, v20\n"
"v_mfma_f32_16x16x32_bf16 v[60:63], a[92:95], v[180:183], v[60:63]\n"
"ds_read_b128 v[172:175], v222 offset:32768\n"
"s_waitcnt lgkmcnt(6)\n"
"v_mfma_f32_16x16x32_bf16 v[48:51], a[96:99], v[184:187], v[48:51]\n"
"v_fma_f32 v20, -v237, v28, v20\n"
"v_fma_f32 v28, v236, v28, v10\n"
"v_mfma_f32_16x16x32_bf16 v[48:51], a[104:107], v[188:191], v[48:51]\n"
"ds_read_b128 v[176:179], v221 offset:34816\n"
"v_mul_f32 v11, v237, v21\n"
"v_mfma_f32_16x16x32_bf16 v[48:51], a[112:115], v[192:195], v[48:51]\n"
"v_mul_f32 v21, v236, v21\n"
"v_fma_f32 v21, -v237, v29, v21\n"
"v_mfma_f32_16x16x32_bf16 v[48:51], a[120:123], v[196:199], v[48:51]\n"
"ds_read_b128 v[180:183], v222 offset:34816\n"
"v_fma_f32 v29, v236, v29, v11\n"
"v_mfma_f32_16x16x32_bf16 v[52:55], a[100:103], v[184:187], v[52:55]\n"
"v_mul_f32 v10, v237, v22\n"
"v_mul_f32 v22, v236, v22\n"
"v_mfma_f32_16x16x32_bf16 v[52:55], a[108:111], v[188:191], v[52:55]\n"
"ds_read_b128 v[184:187], v221 offset:36864\n"
"buffer_load_dwordx4 v15, s[12:15], 0 offen offset:1024 lds\n"
"v_mfma_f32_16x16x32_bf16 v[52:55], a[116:119], v[192:195], v[52:55]\n"
"s_add_u32 s65, s65, 128\n"
"s_and_b32 s65, s65, 2047\n"
"v_mfma_f32_16x16x32_bf16 v[52:55], a[124:127], v[196:199], v[52:55]\n"
"ds_read_b128 v[188:191], v222 offset:36864\n"
"s_waitcnt lgkmcnt(6)\n"
"v_mfma_f32_16x16x32_bf16 v[56:59], a[96:99], v[152:155], v[56:59]\n"
"s_add_u32 s12, s66, s65\n"
"v_fma_f32 v22, -v237, v30, v22\n"
"v_mfma_f32_16x16x32_bf16 v[56:59], a[104:107], v[156:159], v[56:59]\n"
"ds_read_b128 v[192:195], v221 offset:38912\n"
"v_fma_f32 v30, v236, v30, v10\n"
"v_mfma_f32_16x16x32_bf16 v[56:59], a[112:115], v[160:163], v[56:59]\n"
"v_mul_f32 v11, v237, v23\n"
"v_mul_f32 v23, v236, v23\n"
"v_mfma_f32_16x16x32_bf16 v[56:59], a[120:123], v[164:167], v[56:59]\n"
"ds_read_b128 v[196:199], v222 offset:38912\n"
"v_fma_f32 v23, -v237, v31, v23\n"
"v_mfma_f32_16x16x32_bf16 v[60:63], a[100:103], v[152:155], v[60:63]\n"
"buffer_load_dwordx4 v14, s[12:15], 0 offen offset:2048 lds\n"
"v_fma_f32 v31, v236, v31, v11\n"
"v_mfma_f32_16x16x32_bf16 v[60:63], a[108:111], v[156:159], v[60:63]\n"
"ds_read_b128 v[152:155], v221 offset:40960\n"
"v_mfma_f32_16x16x32_bf16 v[60:63], a[116:119], v[160:163], v[60:63]\n"
"ds_read_b64 v[8:9], v235 offset:264\n"
"v_mfma_f32_16x16x32_bf16 v[60:63], a[124:127], v[164:167], v[60:63]\n"
"ds_read_b128 v[156:159], v222 offset:40960\n"
"s_waitcnt lgkmcnt(7)\n"
"v_mfma_f32_16x16x32_bf16 v[48:51], a[128:131], v[168:171], v[48:51]\n"
"v_mfma_f32_16x16x32_bf16 v[48:51], a[136:139], v[172:175], v[48:51]\n"
"ds_read_b128 v[160:163], v221 offset:43008\n"
"v_mfma_f32_16x16x32_bf16 v[48:51], a[144:147], v[176:179], v[48:51]\n"
"s_waitcnt vmcnt(10)\n"
"buffer_store_dword v219, v220, s[36:39], s43 offen sc1\n"
"v_mfma_f32_16x16x32_bf16 v[48:51], a[152:155], v[180:183], v[48:51]\n"
"ds_read_b128 v[164:167], v222 offset:43008\n"
"buffer_load_dwordx4 v15, s[12:15], 0 offen offset:3072 lds\n"
"v_mfma_f32_16x16x32_bf16 v[52:55], a[132:135], v[168:171], v[52:55]\n"
"v_readlane_b32 s17, v207, 48\n"
"s_and_b32 s101, s101, 0x80000000\n"
"v_mfma_f32_16x16x32_bf16 v[52:55], a[140:143], v[172:175], v[52:55]\n"
"ds_read_b128 v[168:171], v221 offset:45056\n"
"s_add_u32 s17, s17, s101\n"
"v_mfma_f32_16x16x32_bf16 v[52:55], a[148:151], v[176:179], v[52:55]\n"
"s_add_u32 s18, s17, 64\n"
"s_add_u32 s43, s43, 131072\n"
"v_mfma_f32_16x16x32_bf16 v[52:55], a[156:159], v[180:183], v[52:55]\n"
"ds_read_b128 v[172:175], v222 offset:45056\n"
"s_waitcnt lgkmcnt(7)\n"
"v_mfma_f32_16x16x32_bf16 v[56:59], a[128:131], v[184:187], v[56:59]\n"
"s_waitcnt lgkmcnt(5)\n"
"v_mul_f32 v10, v9, v32\n"
"v_mfma_f32_16x16x32_bf16 v[56:59], a[136:139], v[188:191], v[56:59]\n"
"ds_read_b128 v[176:179], v221 offset:47104\n"
"v_mul_f32 v32, v8, v32\n"
"v_mfma_f32_16x16x32_bf16 v[56:59], a[144:147], v[192:195], v[56:59]\n"
"s_add_u32 s8, s18, s84\n"
"s_add_u32 m0, s63, 16384\n"
"v_mfma_f32_16x16x32_bf16 v[56:59], a[152:155], v[196:199], v[56:59]\n"
"ds_read_b128 v[180:183], v222 offset:47104\n"
"v_fma_f32 v32, -v9, v40, v32\n"
"v_mfma_f32_16x16x32_bf16 v[60:63], a[132:135], v[184:187], v[60:63]\n"
"s_barrier\n"
"v_fma_f32 v40, v8, v40, v10\n"
"v_mfma_f32_16x16x32_bf16 v[60:63], a[140:143], v[188:191], v[60:63]\n"
"ds_read_b128 v[184:187], v221 offset:49152\n"
"v_mul_f32 v11, v9, v33\n"
"v_mfma_f32_16x16x32_bf16 v[60:63], a[148:151], v[192:195], v[60:63]\n"
"buffer_load_dwordx4 v201, s[8:11], 0 offen offset:0 lds\n"
"v_mul_f32 v33, v8, v33\n"
"v_mfma_f32_16x16x32_bf16 v[60:63], a[156:159], v[196:199], v[60:63]\n"
"ds_read_b128 v[188:191], v222 offset:49152\n"
"s_waitcnt lgkmcnt(6)\n"
"v_mfma_f32_16x16x32_bf16 v[48:51], a[160:163], v[152:155], v[48:51]\n"
"v_fma_f32 v33, -v9, v41, v33\n"
"v_fma_f32 v41, v8, v41, v11\n"
"v_mfma_f32_16x16x32_bf16 v[48:51], a[168:171], v[156:159], v[48:51]\n"
"ds_read_b128 v[192:195], v221 offset:51200\n"
"v_mul_f32 v10, v9, v34\n"
"v_mfma_f32_16x16x32_bf16 v[48:51], a[176:179], v[160:163], v[48:51]\n"
"v_mul_f32 v34, v8, v34\n"
"v_fma_f32 v34, -v9, v42, v34\n"
"v_mfma_f32_16x16x32_bf16 v[48:51], a[184:187], v[164:167], v[48:51]\n"
"ds_read_b128 v[196:199], v222 offset:51200\n"
"v_fma_f32 v42, v8, v42, v10\n"
"v_mfma_f32_16x16x32_bf16 v[52:55], a[164:167], v[152:155], v[52:55]\n"
"v_mul_f32 v11, v9, v35\n"
"v_mul_f32 v35, v8, v35\n"
"v_mfma_f32_16x16x32_bf16 v[52:55], a[172:175], v[156:159], v[52:55]\n"
"ds_read_b128 v[152:155], v221 offset:53248\n"
"buffer_load_dwordx4 v203, s[8:11], 0 offen offset:2048 lds\n"
"v_mfma_f32_16x16x32_bf16 v[52:55], a[180:183], v[160:163], v[52:55]\n"
"v_fma_f32 v35, -v9, v43, v35\n"
"v_fma_f32 v43, v8, v43, v11\n"
"v_mfma_f32_16x16x32_bf16 v[52:55], a[188:191], v[164:167], v[52:55]\n"
"ds_read_b128 v[156:159], v222 offset:53248\n"
"s_waitcnt lgkmcnt(6)\n"
"v_mfma_f32_16x16x32_bf16 v[56:59], a[160:163], v[168:171], v[56:59]\n"
"s_add_u32 s8, s17, s85\n"
"v_mul_f32 v10, v9, v36\n"
"v_mfma_f32_16x16x32_bf16 v[56:59], a[168:171], v[172:175], v[56:59]\n"
"ds_read_b128 v[160:163], v221 offset:55296\n"
"v_mul_f32 v36, v8, v36\n"
"v_mfma_f32_16x16x32_bf16 v[56:59], a[176:179], v[176:179], v[56:59]\n"
"v_fma_f32 v36, -v9, v44, v36\n"
"v_fma_f32 v44, v8, v44, v10\n"
"v_mfma_f32_16x16x32_bf16 v[56:59], a[184:187], v[180:183], v[56:59]\n"
"ds_read_b128 v[164:167], v222 offset:55296\n"
"v_mul_f32 v11, v9, v37\n"
"v_mfma_f32_16x16x32_bf16 v[60:63], a[164:167], v[168:171], v[60:63]\n"
"buffer_load_dwordx4 v202, s[8:11], 0 offen offset:1024 lds\n"
"v_mul_f32 v37, v8, v37\n"
"v_mfma_f32_16x16x32_bf16 v[60:63], a[172:175], v[172:175], v[60:63]\n"
"ds_read_b128 v[168:171], v221 offset:57344\n"
"v_fma_f32 v37, -v9, v45, v37\n"
"v_mfma_f32_16x16x32_bf16 v[60:63], a[180:183], v[176:179], v[60:63]\n"
"v_fma_f32 v45, v8, v45, v11\n"
"v_mul_f32 v10, v9, v38\n"
"v_mfma_f32_16x16x32_bf16 v[60:63], a[188:191], v[180:183], v[60:63]\n"
"ds_read_b128 v[172:175], v222 offset:57344\n"
"s_waitcnt lgkmcnt(6)\n"
"v_mfma_f32_16x16x32_bf16 v[48:51], a[192:195], v[184:187], v[48:51]\n"
"v_mul_f32 v38, v8, v38\n"
"v_fma_f32 v38, -v9, v46, v38\n"
"v_mfma_f32_16x16x32_bf16 v[48:51], a[200:203], v[188:191], v[48:51]\n"
"ds_read_b128 v[176:179], v221 offset:59392\n"
"v_fma_f32 v46, v8, v46, v10\n"
"v_mfma_f32_16x16x32_bf16 v[48:51], a[208:211], v[192:195], v[48:51]\n"
"v_mul_f32 v11, v9, v39\n"
"v_mul_f32 v39, v8, v39\n"
"v_mfma_f32_16x16x32_bf16 v[48:51], a[216:219], v[196:199], v[48:51]\n"
"ds_read_b128 v[180:183], v222 offset:59392\n"
"buffer_load_dwordx4 v204, s[8:11], 0 offen offset:3072 lds\n"
"v_mfma_f32_16x16x32_bf16 v[52:55], a[196:199], v[184:187], v[52:55]\n"
"v_fma_f32 v39, -v9, v47, v39\n"
"v_fma_f32 v47, v8, v47, v11\n"
"v_mfma_f32_16x16x32_bf16 v[52:55], a[204:207], v[188:191], v[52:55]\n"
"ds_read_b128 v[184:187], v221 offset:61440\n"
"v_mfma_f32_16x16x32_bf16 v[52:55], a[212:215], v[192:195], v[52:55]\n"
"ds_read_b64 v[236:237], v235 offset:520\n"
"v_mfma_f32_16x16x32_bf16 v[52:55], a[220:223], v[196:199], v[52:55]\n"
"ds_read_b128 v[188:191], v222 offset:61440\n"
"s_waitcnt lgkmcnt(7)\n"
"v_mfma_f32_16x16x32_bf16 v[56:59], a[192:195], v[152:155], v[56:59]\n"
"v_mfma_f32_16x16x32_bf16 v[56:59], a[200:203], v[156:159], v[56:59]\n"
"ds_read_b128 v[192:195], v221 offset:63488\n"
"v_mfma_f32_16x16x32_bf16 v[56:59], a[208:211], v[160:163], v[56:59]\n"
"s_add_u32 s8, s18, s86\n"
"s_add_u32 m0, s63, 32768\n"
"v_mfma_f32_16x16x32_bf16 v[56:59], a[216:219], v[164:167], v[56:59]\n"
"ds_read_b128 v[196:199], v222 offset:63488\n"
"v_mfma_f32_16x16x32_bf16 v[60:63], a[196:199], v[152:155], v[60:63]\n"
"s_barrier\n"
"v_mfma_f32_16x16x32_bf16 v[60:63], a[204:207], v[156:159], v[60:63]\n"
"ds_read_b128 v[152:155], v223 offset:0\n"
"v_mfma_f32_16x16x32_bf16 v[60:63], a[212:215], v[160:163], v[60:63]\n"
"buffer_load_dwordx4 v201, s[8:11], 0 offen offset:0 lds\n"
"v_mfma_f32_16x16x32_bf16 v[60:63], a[220:223], v[164:167], v[60:63]\n"
"ds_read_b128 v[156:159], v224 offset:0\n"
"s_waitcnt lgkmcnt(7)\n"
"v_mfma_f32_16x16x32_bf16 v[48:51], a[224:227], v[168:171], v[48:51]\n"
"v_mfma_f32_16x16x32_bf16 v[48:51], a[232:235], v[172:175], v[48:51]\n"
"ds_read_b128 v[160:163], v223 offset:2048\n"
"v_mfma_f32_16x16x32_bf16 v[48:51], a[240:243], v[176:179], v[48:51]\n"
"v_mfma_f32_16x16x32_bf16 v[48:51], a[248:251], v[180:183], v[48:51]\n"
"ds_read_b128 v[164:167], v224 offset:2048\n"
"v_mfma_f32_16x16x32_bf16 v[52:55], a[228:231], v[168:171], v[52:55]\n"
"v_mfma_f32_16x16x32_bf16 v[52:55], a[236:239], v[172:175], v[52:55]\n"
"ds_read_b128 v[168:171], v223 offset:4096\n"
"buffer_load_dwordx4 v203, s[8:11], 0 offen offset:2048 lds\n"
"v_mfma_f32_16x16x32_bf16 v[52:55], a[244:247], v[176:179], v[52:55]\n"
"v_mfma_f32_16x16x32_bf16 v[52:55], a[252:255], v[180:183], v[52:55]\n"
"ds_read_b128 v[172:175], v224 offset:4096\n"
"s_waitcnt lgkmcnt(6)\n"
"v_mfma_f32_16x16x32_bf16 v[56:59], a[224:227], v[184:187], v[56:59]\n"
"s_add_u32 s8, s17, s87\n"
"v_fma_f32 v2, v254, v48, v49\n"
"v_mfma_f32_16x16x32_bf16 v[56:59], a[232:235], v[188:191], v[56:59]\n"
"ds_read_b128 v[176:179], v223 offset:6144\n"
"v_mul_f32 v10, v237, v48\n"
"v_mfma_f32_16x16x32_bf16 v[56:59], a[240:243], v[192:195], v[56:59]\n"
"v_mfma_f32_16x16x32_bf16 v[56:59], a[248:251], v[196:199], v[56:59]\n"
"ds_read_b128 v[180:183], v224 offset:6144\n"
"v_mfma_f32_16x16x32_bf16 v[60:63], a[228:231], v[184:187], v[60:63]\n"
"buffer_load_dwordx4 v202, s[8:11], 0 offen offset:1024 lds\n"
"v_mfma_f32_16x16x32_bf16 v[60:63], a[236:239], v[188:191], v[60:63]\n"
"ds_read_b128 v[184:187], v223 offset:8192\n"
"v_mfma_f32_16x16x32_bf16 v[60:63], a[244:247], v[192:195], v[60:63]\n"
"v_mfma_f32_16x16x32_bf16 v[60:63], a[252:255], v[196:199], v[60:63]\n"
"ds_read_b128 v[188:191], v224 offset:8192\n"
"s_waitcnt lgkmcnt(6)\n"
"v_mfma_f32_16x16x32_bf16 v[64:67], a[0:3], v[152:155], 0\n"
"v_fma_f32 v3, v254, v56, v57\n"
"v_fma_f32 v2, -v255, v56, v2\n"
"v_mfma_f32_16x16x32_bf16 v[64:67], a[8:11], v[156:159], v[64:67]\n"
"ds_read_b128 v[192:195], v223 offset:10240\n"
"v_fmac_f32 v3, v255, v48\n"
"v_mfma_f32_16x16x32_bf16 v[64:67], a[16:19], v[160:163], v[64:67]\n"
"v_fma_f32 v216, v254, v2, v50\n"
"v_fma_f32 v217, v254, v3, v58\n"
"v_mfma_f32_16x16x32_bf16 v[64:67], a[24:27], v[164:167], v[64:67]\n"
"ds_read_b128 v[196:199], v224 offset:10240\n"
"buffer_load_dwordx4 v204, s[8:11], 0 offen offset:3072 lds\n"
"v_mfma_f32_16x16x32_bf16 v[68:71], a[4:7], v[152:155], 0\n"
"v_fma_f32 v216, -v255, v3, v216\n"
"v_fmac_f32 v217, v255, v2\n"
"v_mfma_f32_16x16x32_bf16 v[68:71], a[12:15], v[156:159], v[68:71]\n"
"ds_read_b128 v[152:155], v223 offset:12288\n"
"v_fma_f32 v2, v254, v216, v51\n"
"v_mfma_f32_16x16x32_bf16 v[68:71], a[20:23], v[160:163], v[68:71]\n"
"v_fma_f32 v3, v254, v217, v59\n"
"v_fma_f32 v2, -v255, v217, v2\n"
"v_mfma_f32_16x16x32_bf16 v[68:71], a[28:31], v[164:167], v[68:71]\n"
"ds_read_b128 v[156:159], v224 offset:12288\n"
"s_waitcnt lgkmcnt(6)\n"
"v_mfma_f32_16x16x32_bf16 v[72:75], a[0:3], v[168:171], 0\n"
"v_fmac_f32 v3, v255, v216\n"
"v_fma_f32 v216, v254, v2, v52\n"
"v_mfma_f32_16x16x32_bf16 v[72:75], a[8:11], v[172:175], v[72:75]\n"
"ds_read_b128 v[160:163], v223 offset:14336\n"
"v_fma_f32 v217, v254, v3, v60\n"
"v_mfma_f32_16x16x32_bf16 v[72:75], a[16:19], v[176:179], v[72:75]\n"
"s_add_u32 s8, s18, s93\n"
"s_add_u32 m0, s63, 49152\n"
"v_mfma_f32_16x16x32_bf16 v[72:75], a[24:27], v[180:183], v[72:75]\n"
"ds_read_b128 v[164:167], v224 offset:14336\n"
"v_fma_f32 v216, -v255, v3, v216\n"
"v_mfma_f32_16x16x32_bf16 v[76:79], a[4:7], v[168:171], 0\n"
"s_waitcnt vmcnt(17)\n"
"s_barrier\n"
"v_mfma_f32_16x16x32_bf16 v[76:79], a[12:15], v[172:175], v[76:79]\n"
"ds_read_b128 v[168:171], v223 offset:16384\n"
"v_fmac_f32 v217, v255, v2\n"
"v_mfma_f32_16x16x32_bf16 v[76:79], a[20:23], v[176:179], v[76:79]\n"
"buffer_load_dwordx4 v201, s[8:11], 0 offen offset:0 lds\n"
"v_fma_f32 v2, v254, v216, v53\n"
"v_mfma_f32_16x16x32_bf16 v[76:79], a[28:31], v[180:183], v[76:79]\n"
"ds_read_b128 v[172:175], v224 offset:16384\n"
"s_waitcnt lgkmcnt(6)\n"
"v_mfma_f32_16x16x32_bf16 v[64:67], a[32:35], v[184:187], v[64:67]\n"
"v_fma_f32 v3, v254, v217, v61\n"
"v_fma_f32 v2, -v255, v217, v2\n"
"v_mfma_f32_16x16x32_bf16 v[64:67], a[40:43], v[188:191], v[64:67]\n"
"ds_read_b128 v[176:179], v223 offset:18432\n"
"v_fmac_f32 v3, v255, v216\n"
"v_mfma_f32_16x16x32_bf16 v[64:67], a[48:51], v[192:195], v[64:67]\n"
"v_fma_f32 v216, v254, v2, v54\n"
"v_fma_f32 v217, v254, v3, v62\n"
"v_mfma_f32_16x16x32_bf16 v[64:67], a[56:59], v[196:199], v[64:67]\n"
"ds_read_b128 v[180:183], v224 offset:18432\n"
"v_fma_f32 v216, -v255, v3, v216\n"
"v_mfma_f32_16x16x32_bf16 v[68:71], a[36:39], v[184:187], v[68:71]\n"
"v_fmac_f32 v217, v255, v2\n"
"v_fma_f32 v2, v254, v216, v55\n"
"v_mfma_f32_16x16x32_bf16 v[68:71], a[44:47], v[188:191], v[68:71]\n"
"ds_read_b128 v[184:187], v223 offset:20480\n"
"buffer_load_dwordx4 v203, s[8:11], 0 offen offset:2048 lds\n"
"v_mfma_f32_16x16x32_bf16 v[68:71], a[52:55], v[192:195], v[68:71]\n"
"v_fma_f32 v3, v254, v217, v63\n"
"v_fma_f32 v2, -v255, v217, v2\n"
"v_mfma_f32_16x16x32_bf16 v[68:71], a[60:63], v[196:199], v[68:71]\n"
"ds_read_b128 v[188:191], v224 offset:20480\n"
"s_waitcnt lgkmcnt(6)\n"
"v_mfma_f32_16x16x32_bf16 v[72:75], a[32:35], v[152:155], v[72:75]\n"
"s_add_u32 s8, s17, s68\n"
"v_fmac_f32 v3, v255, v216\n"
"v_mfma_f32_16x16x32_bf16 v[72:75], a[40:43], v[156:159], v[72:75]\n"
"ds_read_b128 v[192:195], v223 offset:22528\n"
"v_mul_f32 v48, v236, v48\n"
"v_mfma_f32_16x16x32_bf16 v[72:75], a[48:51], v[160:163], v[72:75]\n"
"ds_write_b64 v234, v[2:3] offset:256\n"
"v_mfma_f32_16x16x32_bf16 v[72:75], a[56:59], v[164:167], v[72:75]\n"
"ds_read_b128 v[196:199], v224 offset:22528\n"
"v_fma_f32 v48, -v237, v56, v48\n"
"v_mfma_f32_16x16x32_bf16 v[76:79], a[36:39], v[152:155], v[76:79]\n"
"buffer_load_dwordx4 v202, s[8:11], 0 offen offset:1024 lds\n"
"v_fma_f32 v56, v236, v56, v10\n"
"v_mfma_f32_16x16x32_bf16 v[76:79], a[44:47], v[156:159], v[76:79]\n"
"ds_read_b128 v[152:155], v223 offset:24576\n"
"v_mul_f32 v11, v237, v49\n"
"v_mfma_f32_16x16x32_bf16 v[76:79], a[52:55], v[160:163], v[76:79]\n"
"v_mul_f32 v49, v236, v49\n"
"v_fma_f32 v49, -v237, v57, v49\n"
"v_mfma_f32_16x16x32_bf16 v[76:79], a[60:63], v[164:167], v[76:79]\n"
"ds_read_b128 v[156:159], v224 offset:24576\n"
"s_waitcnt lgkmcnt(7)\n"
"v_mfma_f32_16x16x32_bf16 v[64:67], a[64:67], v[168:171], v[64:67]\n"
"v_fma_f32 v57, v236, v57, v11\n"
"v_mul_f32 v10, v237, v50\n"
"v_mfma_f32_16x16x32_bf16 v[64:67], a[72:75], v[172:175], v[64:67]\n"
"ds_read_b128 v[160:163], v223 offset:26624\n"
"v_mul_f32 v50, v236, v50\n"
"v_mfma_f32_16x16x32_bf16 v[64:67], a[80:83], v[176:179], v[64:67]\n"
"v_fma_f32 v50, -v237, v58, v50\n"
"v_fma_f32 v58, v236, v58, v10\n"
"v_mfma_f32_16x16x32_bf16 v[64:67], a[88:91], v[180:183], v[64:67]\n"
"ds_read_b128 v[164:167], v224 offset:26624\n"
"buffer_load_dwordx4 v204, s[8:11], 0 offen offset:3072 lds\n"
"v_mfma_f32_16x16x32_bf16 v[68:71], a[68:71], v[168:171], v[68:71]\n"
"v_mul_f32 v11, v237, v51\n"
"v_mul_f32 v51, v236, v51\n"
"v_mfma_f32_16x16x32_bf16 v[68:71], a[76:79], v[172:175], v[68:71]\n"
"ds_read_b128 v[168:171], v223 offset:28672\n"
"v_fma_f32 v51, -v237, v59, v51\n"
"v_mfma_f32_16x16x32_bf16 v[68:71], a[84:87], v[176:179], v[68:71]\n"
"v_fma_f32 v59, v236, v59, v11\n"
"v_mul_f32 v10, v237, v52\n"
"v_mfma_f32_16x16x32_bf16 v[68:71], a[92:95], v[180:183], v[68:71]\n"
"ds_read_b128 v[172:175], v224 offset:28672\n"
"s_waitcnt lgkmcnt(6)\n"
"v_mfma_f32_16x16x32_bf16 v[72:75], a[64:67], v[184:187], v[72:75]\n"
"v_mul_f32 v52, v236, v52\n"
"v_fma_f32 v52, -v237, v60, v52\n"
"v_mfma_f32_16x16x32_bf16 v[72:75], a[72:75], v[188:191], v[72:75]\n"
"ds_read_b128 v[176:179], v223 offset:30720\n"
"v_fma_f32 v60, v236, v60, v10\n"
"v_mfma_f32_16x16x32_bf16 v[72:75], a[80:83], v[192:195], v[72:75]\n"
"s_add_u32 s8, s18, s69\n"
"s_add_u32 m0, s63, 65536\n"
"v_mfma_f32_16x16x32_bf16 v[72:75], a[88:91], v[196:199], v[72:75]\n"
"ds_read_b128 v[180:183], v224 offset:30720\n"
"v_mul_f32 v11, v237, v53\n"
"v_mfma_f32_16x16x32_bf16 v[76:79], a[68:71], v[184:187], v[76:79]\n"
"s_waitcnt vmcnt(17)\n"
"s_barrier\n"
"v_mfma_f32_16x16x32_bf16 v[76:79], a[76:79], v[188:191], v[76:79]\n"
"ds_read_b128 v[184:187], v223 offset:32768\n"
"v_mul_f32 v53, v236, v53\n"
"v_mfma_f32_16x16x32_bf16 v[76:79], a[84:87], v[192:195], v[76:79]\n"
"buffer_load_dwordx4 v201, s[8:11], 0 offen offset:0 lds\n"
"v_fma_f32 v53, -v237, v61, v53\n"
"v_mfma_f32_16x16x32_bf16 v[76:79], a[92:95], v[196:199], v[76:79]\n"
"ds_read_b128 v[188:191], v224 offset:32768\n"
"s_waitcnt lgkmcnt(6)\n"
"v_mfma_f32_16x16x32_bf16 v[64:67], a[96:99], v[152:155], v[64:67]\n"
"v_fma_f32 v61, v236, v61, v11\n"
"v_mul_f32 v10, v237, v54\n"
"v_mfma_f32_16x16x32_bf16 v[64:67], a[104:107], v[156:159], v[64:67]\n"
"ds_read_b128 v[192:195], v223 offset:34816\n"
"v_mul_f32 v54, v236, v54\n"
"v_mfma_f32_16x16x32_bf16 v[64:67], a[112:115], v[160:163], v[64:67]\n"
"v_fma_f32 v54, -v237, v62, v54\n"
"v_fma_f32 v62, v236, v62, v10\n"
"v_mfma_f32_16x16x32_bf16 v[64:67], a[120:123], v[164:167], v[64:67]\n"
"ds_read_b128 v[196:199], v224 offset:34816\n"
"v_mul_f32 v11, v237, v55\n"
"v_mfma_f32_16x16x32_bf16 v[68:71], a[100:103], v[152:155], v[68:71]\n"
"v_mul_f32 v55, v236, v55\n"
"v_fma_f32 v55, -v237, v63, v55\n"
"v_mfma_f32_16x16x32_bf16 v[68:71], a[108:111], v[156:159], v[68:71]\n"
"ds_read_b128 v[152:155], v223 offset:36864\n"
"buffer_load_dwordx4 v203, s[8:11], 0 offen offset:2048 lds\n"
"v_mfma_f32_16x16x32_bf16 v[68:71], a[116:119], v[160:163], v[68:71]\n"
"v_fma_f32 v63, v236, v63, v11\n"
"s_add_u32 s69, s57, 1\n"
"v_mfma_f32_16x16x32_bf16 v[68:71], a[124:127], v[164:167], v[68:71]\n"
"ds_read_b128 v[156:159], v224 offset:36864\n"
"s_waitcnt lgkmcnt(6)\n"
"v_mfma_f32_16x16x32_bf16 v[72:75], a[96:99], v[168:171], v[72:75]\n"
"ds_read_b64 v[8:9], v235 offset:776\n"
"s_add_u32 s8, s17, s0\n"
"v_mfma_f32_16x16x32_bf16 v[72:75], a[104:107], v[172:175], v[72:75]\n"
"ds_read_b128 v[160:163], v223 offset:38912\n"
"s_and_b32 s69, s69, 3\n"
"v_mfma_f32_16x16x32_bf16 v[72:75], a[112:115], v[176:179], v[72:75]\n"
"s_lshl_b32 s69, s69, 16\n"
"s_add_u32 s69, s69, s41\n"
"v_mfma_f32_16x16x32_bf16 v[72:75], a[120:123], v[180:183], v[72:75]\n"
"ds_read_b128 v[164:167], v224 offset:38912\n"
"s_lshl_b32 s84, s59, 18\n"
"v_mfma_f32_16x16x32_bf16 v[76:79], a[100:103], v[168:171], v[76:79]\n"
"buffer_load_dwordx4 v202, s[8:11], 0 offen offset:1024 lds\n"
"s_add_u32 s84, s84, s69\n"
"v_mfma_f32_16x16x32_bf16 v[76:79], a[108:111], v[172:175], v[76:79]\n"
"ds_read_b128 v[168:171], v223 offset:40960\n"
"s_add_u32 s69, s57, 2\n"
"v_mfma_f32_16x16x32_bf16 v[76:79], a[116:119], v[176:179], v[76:79]\n"
"buffer_load_dword v232, v233, s[36:39], s67 offen sc1\n"
"v_mfma_f32_16x16x32_bf16 v[76:79], a[124:127], v[180:183], v[76:79]\n"
"ds_read_b128 v[172:175], v224 offset:40960\n"
"s_waitcnt lgkmcnt(7)\n"
"v_mfma_f32_16x16x32_bf16 v[64:67], a[128:131], v[184:187], v[64:67]\n"
"s_and_b32 s69, s69, 3\n"
"s_lshl_b32 s69, s69, 16\n"
"v_mfma_f32_16x16x32_bf16 v[64:67], a[136:139], v[188:191], v[64:67]\n"
"ds_read_b128 v[176:179], v223 offset:43008\n"
"s_add_u32 s69, s69, s41\n"
"v_mfma_f32_16x16x32_bf16 v[64:67], a[144:147], v[192:195], v[64:67]\n"
"s_lshl_b32 s85, s59, 18\n"
"s_add_u32 s85, s85, s69\n"
"v_mfma_f32_16x16x32_bf16 v[64:67], a[152:155], v[196:199], v[64:67]\n"
"ds_read_b128 v[180:183], v224 offset:43008\n"
"buffer_load_dwordx4 v204, s[8:11], 0 offen offset:3072 lds\n"
"v_mfma_f32_16x16x32_bf16 v[68:71], a[132:135], v[184:187], v[68:71]\n"
"s_add_u32 s69, s57, 3\n"
"s_and_b32 s69, s69, 3\n"
"v_mfma_f32_16x16x32_bf16 v[68:71], a[140:143], v[188:191], v[68:71]\n"
"ds_read_b128 v[184:187], v223 offset:45056\n"
"s_lshl_b32 s69, s69, 16\n"
"v_mfma_f32_16x16x32_bf16 v[68:71], a[148:151], v[192:195], v[68:71]\n"
"s_add_u32 s69, s69, s41\n"
"s_lshl_b32 s86, s59, 18\n"
"v_mfma_f32_16x16x32_bf16 v[68:71], a[156:159], v[196:199], v[68:71]\n"
"ds_read_b128 v[188:191], v224 offset:45056\n"
"s_waitcnt lgkmcnt(6)\n"
"v_mfma_f32_16x16x32_bf16 v[72:75], a[128:131], v[152:155], v[72:75]\n"
"s_add_u32 s86, s86, s69\n"
"s_add_u32 s69, s57, 4\n"
"v_mfma_f32_16x16x32_bf16 v[72:75], a[136:139], v[156:159], v[72:75]\n"
"ds_read_b128 v[192:195], v223 offset:47104\n"
"s_and_b32 s69, s69, 3\n"
"v_mfma_f32_16x16x32_bf16 v[72:75], a[144:147], v[160:163], v[72:75]\n"
"s_lshl_b32 s92, s70, 5\n"
"s_add_u32 s20, s40, s92\n"
"v_mfma_f32_16x16x32_bf16 v[72:75], a[152:155], v[164:167], v[72:75]\n"
"ds_read_b128 v[196:199], v224 offset:47104\n"
"s_add_u32 m0, s63, 81920\n"
"v_mfma_f32_16x16x32_bf16 v[76:79], a[132:135], v[152:155], v[76:79]\n"
"s_waitcnt vmcnt(17)\n"
"s_barrier\n"
"v_mfma_f32_16x16x32_bf16 v[76:79], a[140:143], v[156:159], v[76:79]\n"
"ds_read_b128 v[152:155], v221 offset:0\n"
"s_lshl_b32 s69, s69, 16\n"
"v_mfma_f32_16x16x32_bf16 v[76:79], a[148:151], v[160:163], v[76:79]\n"
"buffer_load_dwordx4 v238, s[20:23], 0 offen offset:0 lds\n"
"s_add_u32 s69, s69, s41\n"
"v_mfma_f32_16x16x32_bf16 v[76:79], a[156:159], v[164:167], v[76:79]\n"
"ds_read_b128 v[156:159], v222 offset:0\n"
"s_waitcnt lgkmcnt(6)\n"
"v_mfma_f32_16x16x32_bf16 v[64:67], a[160:163], v[168:171], v[64:67]\n"
"s_lshl_b32 s87, s59, 18\n"
"s_add_u32 s87, s87, s69\n"
"v_mfma_f32_16x16x32_bf16 v[64:67], a[168:171], v[172:175], v[64:67]\n"
"ds_read_b128 v[160:163], v221 offset:2048\n"
"s_lshr_b32 s69, s54, 1\n"
"v_mfma_f32_16x16x32_bf16 v[64:67], a[176:179], v[176:179], v[64:67]\n"
"s_lshl_b32 s65, s57, 1\n"
"s_add_u32 s65, s65, s69\n"
"v_mfma_f32_16x16x32_bf16 v[64:67], a[184:187], v[180:183], v[64:67]\n"
"ds_read_b128 v[164:167], v222 offset:2048\n"
"s_lshl_b32 s65, s65, 8\n"
"v_mfma_f32_16x16x32_bf16 v[68:71], a[164:167], v[168:171], v[68:71]\n"
"s_add_u32 s66, s65, 128\n"
"v_lshrrev_b32 v225, 3, v1\n"
"v_mfma_f32_16x16x32_bf16 v[68:71], a[172:175], v[172:175], v[68:71]\n"
"ds_read_b128 v[168:171], v221 offset:4096\n"
"buffer_load_dwordx4 v239, s[20:23], 0 offen offset:1024 lds\n"
"v_mfma_f32_16x16x32_bf16 v[68:71], a[180:183], v[176:179], v[68:71]\n"
"v_mfma_f32_16x16x32_bf16 v[68:71], a[188:191], v[180:183], v[68:71]\n"
"ds_read_b128 v[172:175], v222 offset:4096\n"
"s_waitcnt lgkmcnt(6)\n"
"v_mfma_f32_16x16x32_bf16 v[72:75], a[160:163], v[184:187], v[72:75]\n"
"v_mfma_f32_16x16x32_bf16 v[72:75], a[168:171], v[188:191], v[72:75]\n"
"ds_read_b128 v[176:179], v221 offset:6144\n"
"v_mfma_f32_16x16x32_bf16 v[72:75], a[176:179], v[192:195], v[72:75]\n"
"v_mfma_f32_16x16x32_bf16 v[72:75], a[184:187], v[196:199], v[72:75]\n"
"ds_read_b128 v[180:183], v222 offset:6144\n"
"v_mfma_f32_16x16x32_bf16 v[76:79], a[164:167], v[184:187], v[76:79]\n"
"buffer_load_dwordx4 v240, s[20:23], 0 offen offset:2048 lds\n"
"v_mfma_f32_16x16x32_bf16 v[76:79], a[172:175], v[188:191], v[76:79]\n"
"ds_read_b128 v[184:187], v221 offset:8192\n"
"v_mfma_f32_16x16x32_bf16 v[76:79], a[180:183], v[192:195], v[76:79]\n"
"v_mfma_f32_16x16x32_bf16 v[76:79], a[188:191], v[196:199], v[76:79]\n"
"ds_read_b128 v[188:191], v222 offset:8192\n"
"s_waitcnt lgkmcnt(6)\n"
"v_mfma_f32_16x16x32_bf16 v[64:67], a[192:195], v[152:155], v[64:67]\n"
"v_mfma_f32_16x16x32_bf16 v[64:67], a[200:203], v[156:159], v[64:67]\n"
"ds_read_b128 v[192:195], v221 offset:10240\n"
"v_mfma_f32_16x16x32_bf16 v[64:67], a[208:211], v[160:163], v[64:67]\n"
"v_mfma_f32_16x16x32_bf16 v[64:67], a[216:219], v[164:167], v[64:67]\n"
"ds_read_b128 v[196:199], v222 offset:10240\n"
"buffer_load_dwordx4 v241, s[20:23], 0 offen offset:3072 lds\n"
"v_mfma_f32_16x16x32_bf16 v[68:71], a[196:199], v[152:155], v[68:71]\n"
"v_mfma_f32_16x16x32_bf16 v[68:71], a[204:207], v[156:159], v[68:71]\n"
"ds_read_b128 v[152:155], v221 offset:12288\n"
"v_mfma_f32_16x16x32_bf16 v[68:71], a[212:215], v[160:163], v[68:71]\n"
"v_mfma_f32_16x16x32_bf16 v[68:71], a[220:223], v[164:167], v[68:71]\n"
"ds_read_b128 v[156:159], v222 offset:12288\n"
"s_waitcnt lgkmcnt(6)\n"
"v_mfma_f32_16x16x32_bf16 v[72:75], a[192:195], v[168:171], v[72:75]\n"
"v_mfma_f32_16x16x32_bf16 v[72:75], a[200:203], v[172:175], v[72:75]\n"
"ds_read_b128 v[160:163], v221 offset:14336\n"
"v_mfma_f32_16x16x32_bf16 v[72:75], a[208:211], v[176:179], v[72:75]\n"
"s_lshl_b32 s92, s71, 5\n"
"s_add_u32 s20, s40, s92\n"
"v_mfma_f32_16x16x32_bf16 v[72:75], a[216:219], v[180:183], v[72:75]\n"
"ds_read_b128 v[164:167], v222 offset:14336\n"
"ds_read_b128 v[84:87], v231 offset:2816\n"
"v_mfma_f32_16x16x32_bf16 v[76:79], a[196:199], v[168:171], v[76:79]\n"
"ds_read_b128 v[92:95], v231 offset:2880\n"
"s_add_u32 m0, s63, 98304\n"
"v_mfma_f32_16x16x32_bf16 v[76:79], a[204:207], v[172:175], v[76:79]\n"
"s_waitcnt vmcnt(17)\n"
"s_barrier\n"
"v_mfma_f32_16x16x32_bf16 v[76:79], a[212:215], v[176:179], v[76:79]\n"
"ds_read_b64_tr_b16 v[168:169], v227 offset:16384\n"
"buffer_load_dwordx4 v238, s[20:23], 0 offen offset:0 lds\n"
"v_mfma_f32_16x16x32_bf16 v[76:79], a[220:223], v[180:183], v[76:79]\n"
"ds_read_b64_tr_b16 v[170:171], v227 offset:16640\n"
"s_waitcnt lgkmcnt(8)\n"
"v_mfma_f32_16x16x32_bf16 v[64:67], a[224:227], v[184:187], v[64:67]\n"
"ds_read_b64_tr_b16 v[172:173], v227 offset:20480\n"
"v_mfma_f32_16x16x32_bf16 v[64:67], a[232:235], v[188:191], v[64:67]\n"
"ds_read_b64_tr_b16 v[174:175], v227 offset:20736\n"
"v_mfma_f32_16x16x32_bf16 v[64:67], a[240:243], v[192:195], v[64:67]\n"
"ds_read_b64_tr_b16 v[176:177], v227 offset:24576\n"
"v_mfma_f32_16x16x32_bf16 v[64:67], a[248:251], v[196:199], v[64:67]\n"
"ds_read_b64_tr_b16 v[178:179], v227 offset:24832\n"
"v_mfma_f32_16x16x32_bf16 v[68:71], a[228:231], v[184:187], v[68:71]\n"
"ds_read_b64_tr_b16 v[180:181], v227 offset:28672\n"
"v_mfma_f32_16x16x32_bf16 v[68:71], a[236:239], v[188:191], v[68:71]\n"
"ds_read_b64_tr_b16 v[182:183], v227 offset:28928\n"
"buffer_load_dwordx4 v239, s[20:23], 0 offen offset:1024 lds\n"
"v_mfma_f32_16x16x32_bf16 v[68:71], a[244:247], v[192:195], v[68:71]\n"
"ds_read_b64_tr_b16 v[184:185], v228 offset:16384\n"
"v_fma_f32 v2, v4, v64, v65\n"
"v_mfma_f32_16x16x32_bf16 v[68:71], a[252:255], v[196:199], v[68:71]\n"
"ds_read_b64_tr_b16 v[186:187], v228 offset:16640\n"
"s_waitcnt lgkmcnt(12)\n"
"v_mfma_f32_16x16x32_bf16 v[72:75], a[224:227], v[152:155], v[72:75]\n"
"ds_read_b64_tr_b16 v[188:189], v228 offset:20480\n"
"v_mul_f32 v10, v9, v64\n"
"v_mfma_f32_16x16x32_bf16 v[72:75], a[232:235], v[156:159], v[72:75]\n"
"ds_read_b64_tr_b16 v[190:191], v228 offset:20736\n"
"v_mfma_f32_16x16x32_bf16 v[72:75], a[240:243], v[160:163], v[72:75]\n"
"ds_read_b64_tr_b16 v[192:193], v228 offset:24576\n"
"v_mfma_f32_16x16x32_bf16 v[72:75], a[248:251], v[164:167], v[72:75]\n"
"ds_read_b64_tr_b16 v[194:195], v228 offset:24832\n"
"v_mfma_f32_16x16x32_bf16 v[76:79], a[228:231], v[152:155], v[76:79]\n"
"ds_read_b64_tr_b16 v[196:197], v228 offset:28672\n"
"buffer_load_dwordx4 v240, s[20:23], 0 offen offset:2048 lds\n"
"v_mfma_f32_16x16x32_bf16 v[76:79], a[236:239], v[156:159], v[76:79]\n"
"ds_read_b64_tr_b16 v[198:199], v228 offset:28928\n"
"v_mfma_f32_16x16x32_bf16 v[76:79], a[244:247], v[160:163], v[76:79]\n"
"ds_read_b128 v[152:155], v231 offset:0\n"
"v_fma_f32 v3, v4, v72, v73\n"
"v_mfma_f32_16x16x32_bf16 v[76:79], a[252:255], v[164:167], v[76:79]\n"
"ds_read_b128 v[156:159], v231 offset:64\n"
"s_waitcnt lgkmcnt(10)\n"
"v_mfma_f32_16x16x32_bf16 v[80:83], v[168:171], a[0:3], v[84:87]\n"
"s_waitcnt vmcnt(8)\n"
"v_cmp_ne_u32 vcc, s61, v232\n"
"s_cbranch_vccnz .Lk_slow0\n"
".Lk_back0:\n"
"v_mfma_f32_16x16x32_bf16 v[80:83], v[172:175], a[8:11], v[80:83]\n"
"ds_read_b128 v[160:163], v231 offset:128\n"
"s_mov_b32 s28, s70\n"
"v_mfma_f32_16x16x32_bf16 v[80:83], v[176:179], a[16:19], v[80:83]\n"
"v_fma_f32 v2, -v5, v72, v2\n"
"v_fmac_f32 v3, v5, v64\n"
"v_mfma_f32_16x16x32_bf16 v[80:83], v[180:183], a[24:27], v[80:83]\n"
"ds_read_b128 v[164:167], v231 offset:192\n"
"v_fma_f32 v216, v4, v2, v66\n"
"v_mfma_f32_16x16x32_bf16 v[84:87], v[168:171], a[4:7], v[84:87]\n"
"buffer_load_dwordx4 v[96:99], v246, s[28:31], 0 offen offset:0 sc1\n"
"v_fma_f32 v217, v4, v3, v74\n"
"v_mfma_f32_16x16x32_bf16 v[84:87], v[172:175], a[12:15], v[84:87]\n"
".Lk_h6:\n"
"s_cbranch_execz .Lk_h10\n"
"ds_read_b64_tr_b16 v[168:169], v227 offset:17408\n"
"v_fma_f32 v216, -v5, v3, v216\n"
"v_mfma_f32_16x16x32_bf16 v[84:87], v[176:179], a[20:23], v[84:87]\n"
"ds_read_b64_tr_b16 v[170:171], v227 offset:17664\n"
"v_fmac_f32 v217, v5, v2\n"
"v_mfma_f32_16x16x32_bf16 v[84:87], v[180:183], a[28:31], v[84:87]\n"
"ds_read_b64_tr_b16 v[172:173], v227 offset:21504\n"
"s_waitcnt lgkmcnt(7)\n"
"v_mfma_f32_16x16x32_bf16 v[88:91], v[184:187], a[0:3], v[92:95]\n"
"ds_read_b64_tr_b16 v[174:175], v227 offset:21760\n"
"buffer_load_dwordx4 v241, s[20:23], 0 offen offset:3072 lds\n"
"v_mfma_f32_16x16x32_bf16 v[88:91], v[188:191], a[8:11], v[88:91]\n"
"ds_read_b64_tr_b16 v[176:177], v227 offset:25600\n"
"v_fma_f32 v2, v4, v216, v67\n"
"v_mfma_f32_16x16x32_bf16 v[88:91], v[192:195], a[16:19], v[88:91]\n"
"ds_read_b64_tr_b16 v[178:179], v227 offset:25856\n"
"v_fma_f32 v3, v4, v217, v75\n"
"v_mfma_f32_16x16x32_bf16 v[88:91], v[196:199], a[24:27], v[88:91]\n"
"ds_read_b64_tr_b16 v[180:181], v227 offset:29696\n"
"v_fma_f32 v2, -v5, v217, v2\n"
"v_mfma_f32_16x16x32_bf16 v[92:95], v[184:187], a[4:7], v[92:95]\n"
"ds_read_b64_tr_b16 v[182:183], v227 offset:29952\n"
"buffer_load_dwordx4 v[104:107], v246, s[28:31], 0 offen offset:1024 sc1\n"
"v_mfma_f32_16x16x32_bf16 v[92:95], v[188:191], a[12:15], v[92:95]\n"
"ds_read_b64_tr_b16 v[184:185], v228 offset:17408\n"
"v_fmac_f32 v3, v5, v216\n"
"v_mfma_f32_16x16x32_bf16 v[92:95], v[192:195], a[20:23], v[92:95]\n"
"ds_read_b64_tr_b16 v[186:187], v228 offset:17664\n"
"v_fma_f32 v216, v4, v2, v68\n"
"v_mfma_f32_16x16x32_bf16 v[92:95], v[196:199], a[28:31], v[92:95]\n"
"ds_read_b64_tr_b16 v[188:189], v228 offset:21504\n"
"s_waitcnt lgkmcnt(11)\n"
"v_mfma_f32_16x16x32_bf16 v[144:147], a[0:3], v[152:155], 0\n"
"ds_read_b64_tr_b16 v[190:191], v228 offset:21760\n"
"v_fma_f32 v217, v4, v3, v76\n"
"v_mfma_f32_16x16x32_bf16 v[144:147], a[8:11], v[156:159], v[144:147]\n"
"ds_read_b64_tr_b16 v[192:193], v228 offset:25600\n"
"v_fma_f32 v216, -v5, v3, v216\n"
"v_mfma_f32_16x16x32_bf16 v[144:147], a[16:19], v[160:163], v[144:147]\n"
"ds_read_b64_tr_b16 v[194:195], v228 offset:25856\n"
"v_fmac_f32 v217, v5, v2\n"
"v_mfma_f32_16x16x32_bf16 v[144:147], a[24:27], v[164:167], v[144:147]\n"
"ds_read_b64_tr_b16 v[196:197], v228 offset:29696\n"
"v_fma_f32 v2, v4, v216, v69\n"
"v_mfma_f32_16x16x32_bf16 v[148:151], a[4:7], v[152:155], 0\n"
"ds_read_b64_tr_b16 v[198:199], v228 offset:29952\n"
"buffer_load_dwordx4 v[100:103], v246, s[28:31], 0 offen offset:2048 sc1\n"
"v_mfma_f32_16x16x32_bf16 v[148:151], a[12:15], v[156:159], v[148:151]\n"
"ds_read_b128 v[152:155], v231 offset:256\n"
"v_fma_f32 v3, v4, v217, v77\n"
"v_mfma_f32_16x16x32_bf16 v[148:151], a[20:23], v[160:163], v[148:151]\n"
"v_fma_f32 v2, -v5, v217, v2\n"
"v_fmac_f32 v3, v5, v216\n"
"v_mfma_f32_16x16x32_bf16 v[148:151], a[28:31], v[164:167], v[148:151]\n"
"ds_read_b128 v[156:159], v231 offset:320\n"
"s_waitcnt lgkmcnt(10)\n"
"v_mfma_f32_16x16x32_bf16 v[80:83], v[168:171], a[32:35], v[80:83]\n"
"v_fma_f32 v216, v4, v2, v70\n"
"v_fma_f32 v217, v4, v3, v78\n"
"v_mfma_f32_16x16x32_bf16 v[80:83], v[172:175], a[40:43], v[80:83]\n"
"ds_read_b128 v[160:163], v231 offset:384\n"
"v_fma_f32 v216, -v5, v3, v216\n"
"v_mfma_f32_16x16x32_bf16 v[80:83], v[176:179], a[48:51], v[80:83]\n"
"s_lshl_b32 s92, s72, 5\n"
"s_add_u32 s20, s40, s92\n"
"v_mfma_f32_16x16x32_bf16 v[80:83], v[180:183], a[56:59], v[80:83]\n"
"ds_read_b128 v[164:167], v231 offset:448\n"
"s_add_u32 m0, s63, 0\n"
"v_mfma_f32_16x16x32_bf16 v[84:87], v[168:171], a[36:39], v[84:87]\n"
"s_barrier\n"
"buffer_load_dwordx4 v[108:111], v246, s[28:31], 0 offen offset:3072 sc1\n"
"v_mfma_f32_16x16x32_bf16 v[84:87], v[172:175], a[44:47], v[84:87]\n"
"ds_read_b64_tr_b16 v[168:169], v227 offset:32768\n"
"v_fmac_f32 v217, v5, v2\n"
"v_mfma_f32_16x16x32_bf16 v[84:87], v[176:179], a[52:55], v[84:87]\n"
"ds_read_b64_tr_b16 v[170:171], v227 offset:33024\n"
"v_fma_f32 v2, v4, v216, v71\n"
"v_mfma_f32_16x16x32_bf16 v[84:87], v[180:183], a[60:63], v[84:87]\n"
"ds_read_b64_tr_b16 v[172:173], v227 offset:36864\n"
"s_waitcnt lgkmcnt(7)\n"
"v_mfma_f32_16x16x32_bf16 v[88:91], v[184:187], a[32:35], v[88:91]\n"
"ds_read_b64_tr_b16 v[174:175], v227 offset:37120\n"
"buffer_load_dwordx4 v238, s[20:23], 0 offen offset:0 lds\n"
"v_mfma_f32_16x16x32_bf16 v[88:91], v[188:191], a[40:43], v[88:91]\n"
"ds_read_b64_tr_b16 v[176:177], v227 offset:40960\n"
"v_fma_f32 v3, v4, v217, v79\n"
"v_mfma_f32_16x16x32_bf16 v[88:91], v[192:195], a[48:51], v[88:91]\n"
"ds_read_b64_tr_b16 v[178:179], v227 offset:41216\n"
"v_fma_f32 v2, -v5, v217, v2\n"
"v_mfma_f32_16x16x32_bf16 v[88:91], v[196:199], a[56:59], v[88:91]\n"
"ds_read_b64_tr_b16 v[180:181], v227 offset:45056\n"
"v_fmac_f32 v3, v5, v216\n"
"v_mfma_f32_16x16x32_bf16 v[92:95], v[184:187], a[36:39], v[92:95]\n"
"ds_read_b64_tr_b16 v[182:183], v227 offset:45312\n"
"buffer_load_dwordx4 v[112:115], v247, s[28:31], 0 offen offset:0 sc1\n"
"v_mfma_f32_16x16x32_bf16 v[92:95], v[188:191], a[44:47], v[92:95]\n"
"ds_read_b64_tr_b16 v[184:185], v228 offset:32768\n"
"v_mul_f32 v64, v8, v64\n"
"v_mfma_f32_16x16x32_bf16 v[92:95], v[192:195], a[52:55], v[92:95]\n"
"ds_write_b64 v234, v[2:3] offset:384\n"
"v_mfma_f32_16x16x32_bf16 v[92:95], v[196:199], a[60:63], v[92:95]\n"
"ds_read_b64_tr_b16 v[186:187], v228 offset:33024\n"
"s_waitcnt lgkmcnt(11)\n"
"v_mfma_f32_16x16x32_bf16 v[144:147], a[32:35], v[152:155], v[144:147]\n"
"ds_read_b64_tr_b16 v[188:189], v228 offset:36864\n"
"buffer_load_dwordx4 v239, s[20:23], 0 offen offset:1024 lds\n"
"v_mfma_f32_16x16x32_bf16 v[144:147], a[40:43], v[156:159], v[144:147]\n"
"ds_read_b64 v[250:251], v248 offset:0\n"
"v_fma_f32 v64, -v9, v72, v64\n"
"v_mfma_f32_16x16x32_bf16 v[144:147], a[48:51], v[160:163], v[144:147]\n"
"ds_read_b64_tr_b16 v[190:191], v228 offset:37120\n"
"v_fma_f32 v72, v8, v72, v10\n"
"v_mfma_f32_16x16x32_bf16 v[144:147], a[56:59], v[164:167], v[144:147]\n"
"ds_read_b64_tr_b16 v[192:193], v228 offset:40960\n"
"v_mul_f32 v11, v9, v65\n"
"v_mfma_f32_16x16x32_bf16 v[148:151], a[36:39], v[152:155], v[148:151]\n"
"ds_read_b64 v[252:253], v248 offset:512\n"
"v_mul_f32 v65, v8, v65\n"
"v_mfma_f32_16x16x32_bf16 v[148:151], a[44:47], v[156:159], v[148:151]\n"
"ds_read_b64_tr_b16 v[194:195], v228 offset:41216\n"
"buffer_load_dwordx4 v[120:123], v247, s[28:31], 0 offen offset:1024 sc1\n"
"v_mfma_f32_16x16x32_bf16 v[148:151], a[52:55], v[160:163], v[148:151]\n"
"ds_read_b64_tr_b16 v[196:197], v228 offset:45056\n"
"v_fma_f32 v65, -v9, v73, v65\n"
"v_mfma_f32_16x16x32_bf16 v[148:151], a[60:63], v[164:167], v[148:151]\n"
"ds_read_b64_tr_b16 v[198:199], v228 offset:45312\n"
"s_waitcnt lgkmcnt(11)\n"
"v_mfma_f32_16x16x32_bf16 v[80:83], v[168:171], a[64:67], v[80:83]\n"
"ds_read_b64 v[254:255], v248 offset:1024\n"
"v_fma_f32 v73, v8, v73, v11\n"
"v_mfma_f32_16x16x32_bf16 v[80:83], v[172:175], a[72:75], v[80:83]\n"
"ds_read_b128 v[152:155], v231 offset:512\n"
"v_mul_f32 v10, v9, v66\n"
"v_mfma_f32_16x16x32_bf16 v[80:83], v[176:179], a[80:83], v[80:83]\n"
"ds_read_b128 v[156:159], v231 offset:576\n"
"buffer_load_dwordx4 v240, s[20:23], 0 offen offset:2048 lds\n"
"v_mfma_f32_16x16x32_bf16 v[80:83], v[180:183], a[88:91], v[80:83]\n"
"ds_read_b64 v[4:5], v248 offset:1536\n"
"v_mul_f32 v66, v8, v66\n"
"v_mfma_f32_16x16x32_bf16 v[84:87], v[168:171], a[68:71], v[84:87]\n"
"ds_read_b128 v[160:163], v231 offset:640\n"
"v_fma_f32 v66, -v9, v74, v66\n"
"v_mfma_f32_16x16x32_bf16 v[84:87], v[172:175], a[76:79], v[84:87]\n"
"ds_read_b128 v[164:167], v231 offset:704\n"
"v_fma_f32 v74, v8, v74, v10\n"
"v_mfma_f32_16x16x32_bf16 v[84:87], v[176:179], a[84:87], v[84:87]\n"
"s_waitcnt lgkmcnt(9)\n"
"v_fmac_f32 v252, v212, v250\n"
"v_mfma_f32_16x16x32_bf16 v[84:87], v[180:183], a[92:95], v[84:87]\n"
"ds_read_b64_tr_b16 v[168:169], v227 offset:33792\n"
"s_waitcnt lgkmcnt(7)\n"
"v_mfma_f32_16x16x32_bf16 v[88:91], v[184:187], a[64:67], v[88:91]\n"
"ds_read_b64_tr_b16 v[170:171], v227 offset:34048\n"
"buffer_load_dwordx4 v[116:119], v247, s[28:31], 0 offen offset:2048 sc1\n"
"v_mfma_f32_16x16x32_bf16 v[88:91], v[188:191], a[72:75], v[88:91]\n"
"ds_read_b64_tr_b16 v[172:173], v227 offset:37888\n"
"v_fmac_f32 v253, v212, v251\n"
"v_mfma_f32_16x16x32_bf16 v[88:91], v[192:195], a[80:83], v[88:91]\n"
"ds_read_b64_tr_b16 v[174:175], v227 offset:38144\n"
"v_fma_f32 v252, -v213, v251, v252\n"
"v_mfma_f32_16x16x32_bf16 v[88:91], v[196:199], a[88:91], v[88:91]\n"
"ds_read_b64_tr_b16 v[176:177], v227 offset:41984\n"
"v_fmac_f32 v253, v213, v250\n"
"v_mfma_f32_16x16x32_bf16 v[92:95], v[184:187], a[68:71], v[92:95]\n"
"ds_read_b64_tr_b16 v[178:179], v227 offset:42240\n"
"buffer_load_dwordx4 v[124:127], v247, s[28:31], 0 offen offset:3072 sc1\n"
"v_mfma_f32_16x16x32_bf16 v[92:95], v[188:191], a[76:79], v[92:95]\n"
"ds_read_b64_tr_b16 v[180:181], v227 offset:46080\n"
"v_mul_f32 v11, v9, v67\n"
"v_mfma_f32_16x16x32_bf16 v[92:95], v[192:195], a[84:87], v[92:95]\n"
"s_waitcnt lgkmcnt(12)\n"
"v_fmac_f32 v254, v212, v252\n"
"v_mfma_f32_16x16x32_bf16 v[92:95], v[196:199], a[92:95], v[92:95]\n"
"ds_read_b64_tr_b16 v[182:183], v227 offset:46336\n"
"s_waitcnt lgkmcnt(8)\n"
"v_mfma_f32_16x16x32_bf16 v[144:147], a[64:67], v[152:155], v[144:147]\n"
"ds_read_b64_tr_b16 v[184:185], v228 offset:33792\n"
"v_fmac_f32 v255, v212, v253\n"
"v_mfma_f32_16x16x32_bf16 v[144:147], a[72:75], v[156:159], v[144:147]\n"
"ds_read_b64_tr_b16 v[186:187], v228 offset:34048\n"
"buffer_load_dwordx4 v241, s[20:23], 0 offen offset:3072 lds\n"
"v_mfma_f32_16x16x32_bf16 v[144:147], a[80:83], v[160:163], v[144:147]\n"
"ds_read_b64_tr_b16 v[188:189], v228 offset:37888\n"
"v_fma_f32 v254, -v213, v253, v254\n"
"v_mfma_f32_16x16x32_bf16 v[144:147], a[88:91], v[164:167], v[144:147]\n"
"ds_read_b64_tr_b16 v[190:191], v228 offset:38144\n"
"v_fmac_f32 v255, v213, v252\n"
"v_mfma_f32_16x16x32_bf16 v[148:151], a[68:71], v[152:155], v[148:151]\n"
"ds_read_b64_tr_b16 v[192:193], v228 offset:41984\n"
"v_fmac_f32 v4, v212, v254\n"
"v_mfma_f32_16x16x32_bf16 v[148:151], a[76:79], v[156:159], v[148:151]\n"
"ds_read_b64_tr_b16 v[194:195], v228 offset:42240\n"
"v_fmac_f32 v5, v212, v255\n"
"v_mfma_f32_16x16x32_bf16 v[148:151], a[84:87], v[160:163], v[148:151]\n"
"ds_read_b64_tr_b16 v[196:197], v228 offset:46080\n"
"v_fma_f32 v4, -v213, v255, v4\n"
"v_mfma_f32_16x16x32_bf16 v[148:151], a[92:95], v[164:167], v[148:151]\n"
"ds_read_b64_tr_b16 v[198:199], v228 offset:46336\n"
"s_waitcnt lgkmcnt(8)\n"
"v_mfma_f32_16x16x32_bf16 v[80:83], v[168:171], a[96:99], v[80:83]\n"
"ds_read_b128 v[152:155], v231 offset:768\n"
"v_fmac_f32 v5, v213, v254\n"
"v_mfma_f32_16x16x32_bf16 v[80:83], v[172:175], a[104:107], v[80:83]\n"
"ds_read_b128 v[156:159], v231 offset:832\n"
"v_mul_f32 v67, v8, v67\n"
"v_mfma_f32_16x16x32_bf16 v[80:83], v[176:179], a[112:115], v[80:83]\n"
"ds_read_b128 v[160:163], v231 offset:896\n"
"s_lshl_b32 s92, s73, 5\n"
"v_mfma_f32_16x16x32_bf16 v[80:83], v[180:183], a[120:123], v[80:83]\n"
"ds_write_b64 v205, v[4:5] offset:10240\n"
"v_mfma_f32_16x16x32_bf16 v[84:87], v[168:171], a[100:103], v[84:87]\n"
"ds_read_b128 v[164:167], v231 offset:960\n"
"s_barrier\n"
"v_mfma_f32_16x16x32_bf16 v[84:87], v[172:175], a[108:111], v[84:87]\n"
"ds_read_b64_tr_b16 v[168:169], v227 offset:49152\n"
"s_add_u32 s20, s40, s92\n"
"v_mfma_f32_16x16x32_bf16 v[84:87], v[176:179], a[116:119], v[84:87]\n"
"ds_read_b64_tr_b16 v[170:171], v227 offset:49408\n"
"s_add_u32 m0, s63, 16384\n"
"v_mfma_f32_16x16x32_bf16 v[84:87], v[180:183], a[124:127], v[84:87]\n"
"ds_read_b64_tr_b16 v[172:173], v227 offset:53248\n"
"s_waitcnt lgkmcnt(8)\n"
"v_mfma_f32_16x16x32_bf16 v[88:91], v[184:187], a[96:99], v[88:91]\n"
"ds_read_b64_tr_b16 v[174:175], v227 offset:53504\n"
"s_mov_b32 s28, s71\n"
"v_mfma_f32_16x16x32_bf16 v[88:91], v[188:191], a[104:107], v[88:91]\n"
"ds_read_b64_tr_b16 v[176:177], v227 offset:57344\n"
"buffer_load_dwordx4 a[0:3], v246, s[28:31], 0 offen offset:0 sc1\n"
"v_mfma_f32_16x16x32_bf16 v[88:91], v[192:195], a[112:115], v[88:91]\n"
"ds_read_b64_tr_b16 v[178:179], v227 offset:57600\n"
"v_mov_b32 v129, s61\n"
"v_mfma_f32_16x16x32_bf16 v[88:91], v[196:199], a[120:123], v[88:91]\n"
"ds_read_b64_tr_b16 v[180:181], v227 offset:61440\n"
"v_mov_b32 v131, s61\n"
"v_mfma_f32_16x16x32_bf16 v[92:95], v[184:187], a[100:103], v[92:95]\n"
"ds_read_b64_tr_b16 v[182:183], v227 offset:61696\n"
"v_fma_f32 v67, -v9, v75, v67\n"
"v_mfma_f32_16x16x32_bf16 v[92:95], v[188:191], a[108:111], v[92:95]\n"
"ds_read_b64_tr_b16 v[184:185], v228 offset:49152\n"
"buffer_load_dwordx4 a[8:11], v246, s[28:31], 0 offen offset:1024 sc1\n"
"v_mfma_f32_16x16x32_bf16 v[92:95], v[192:195], a[116:119], v[92:95]\n"
"s_waitcnt lgkmcnt(10)\n"
"s_barrier\n"
"v_mfma_f32_16x16x32_bf16 v[92:95], v[196:199], a[124:127], v[92:95]\n"
"ds_read_b64_tr_b16 v[186:187], v228 offset:49408\n"
"s_waitcnt lgkmcnt(10)\n"
"v_mfma_f32_16x16x32_bf16 v[144:147], a[96:99], v[152:155], v[144:147]\n"
"ds_read_b64_tr_b16 v[188:189], v228 offset:53248\n"
"v_fma_f32 v75, v8, v75, v11\n"
"v_mfma_f32_16x16x32_bf16 v[144:147], a[104:107], v[156:159], v[144:147]\n"
"ds_read_b64 v[250:251], v249 offset:10240\n"
"buffer_load_dwordx4 a[4:7], v246, s[28:31], 0 offen offset:2048 sc1\n"
"v_mfma_f32_16x16x32_bf16 v[144:147], a[112:115], v[160:163], v[144:147]\n"
"ds_read_b64_tr_b16 v[190:191], v228 offset:53504\n"
"v_mul_f32 v10, v9, v68\n"
"v_mfma_f32_16x16x32_bf16 v[144:147], a[120:123], v[164:167], v[144:147]\n"
"ds_read_b64_tr_b16 v[192:193], v228 offset:57344\n"
"v_mul_f32 v68, v8, v68\n"
"v_mfma_f32_16x16x32_bf16 v[148:151], a[100:103], v[152:155], v[148:151]\n"
"ds_read_b64 v[252:253], v249 offset:10752\n"
"v_fma_f32 v68, -v9, v76, v68\n"
"v_mfma_f32_16x16x32_bf16 v[148:151], a[108:111], v[156:159], v[148:151]\n"
"ds_read_b64_tr_b16 v[194:195], v228 offset:57600\n"
"buffer_load_dwordx4 a[12:15], v246, s[28:31], 0 offen offset:3072 sc1\n"
"v_mfma_f32_16x16x32_bf16 v[148:151], a[116:119], v[160:163], v[148:151]\n"
"ds_read_b64_tr_b16 v[196:197], v228 offset:61440\n"
"v_fma_f32 v76, v8, v76, v10\n"
"v_mfma_f32_16x16x32_bf16 v[148:151], a[124:127], v[164:167], v[148:151]\n"
"ds_read_b64_tr_b16 v[198:199], v228 offset:61696\n"
"s_waitcnt lgkmcnt(10)\n"
"v_mfma_f32_16x16x32_bf16 v[80:83], v[168:171], a[128:131], v[80:83]\n"
"ds_read_b64 v[254:255], v249 offset:11264\n"
"v_mul_f32 v11, v9, v69\n"
"v_mfma_f32_16x16x32_bf16 v[80:83], v[172:175], a[136:139], v[80:83]\n"
"ds_read_b128 v[152:155], v231 offset:1024\n"
"v_mul_f32 v69, v8, v69\n"
"v_mfma_f32_16x16x32_bf16 v[80:83], v[176:179], a[144:147], v[80:83]\n"
"ds_read_b128 v[156:159], v231 offset:1088\n"
"buffer_load_dwordx4 a[16:19], v247, s[28:31], 0 offen offset:0 sc1\n"
"v_mfma_f32_16x16x32_bf16 v[80:83], v[180:183], a[152:155], v[80:83]\n"
"ds_read_b64 v[4:5], v249 offset:11776\n"
"v_fma_f32 v69, -v9, v77, v69\n"
"v_mfma_f32_16x16x32_bf16 v[84:87], v[168:171], a[132:135], v[84:87]\n"
"ds_read_b128 v[160:163], v231 offset:1152\n"
"v_fma_f32 v77, v8, v77, v11\n"
"v_mfma_f32_16x16x32_bf16 v[84:87], v[172:175], a[140:143], v[84:87]\n"
"ds_read_b128 v[164:167], v231 offset:1216\n"
"v_mul_f32 v10, v9, v70\n"
"v_mfma_f32_16x16x32_bf16 v[84:87], v[176:179], a[148:151], v[84:87]\n"
"s_waitcnt lgkmcnt(9)\n"
"v_fmac_f32 v252, v214, v250\n"
"v_mfma_f32_16x16x32_bf16 v[84:87], v[180:183], a[156:159], v[84:87]\n"
"ds_read_b64_tr_b16 v[168:169], v227 offset:50176\n"
"s_waitcnt lgkmcnt(7)\n"
"v_mfma_f32_16x16x32_bf16 v[88:91], v[184:187], a[128:131], v[88:91]\n"
"ds_read_b64_tr_b16 v[170:171], v227 offset:50432\n"
"buffer_load_dwordx4 a[24:27], v247, s[28:31], 0 offen offset:1024 sc1\n"
"v_mfma_f32_16x16x32_bf16 v[88:91], v[188:191], a[136:139], v[88:91]\n"
"ds_read_b64_tr_b16 v[172:173], v227 offset:54272\n"
"v_fmac_f32 v253, v214, v251\n"
"v_mfma_f32_16x16x32_bf16 v[88:91], v[192:195], a[144:147], v[88:91]\n"
"ds_read_b64_tr_b16 v[174:175], v227 offset:54528\n"
"v_fma_f32 v252, -v215, v251, v252\n"
"v_mfma_f32_16x16x32_bf16 v[88:91], v[196:199], a[152:155], v[88:91]\n"
"ds_read_b64_tr_b16 v[176:177], v227 offset:58368\n"
"v_fmac_f32 v253, v215, v250\n"
"v_mfma_f32_16x16x32_bf16 v[92:95], v[184:187], a[132:135], v[92:95]\n"
"ds_read_b64_tr_b16 v[178:179], v227 offset:58624\n"
"buffer_load_dwordx4 a[20:23], v247, s[28:31], 0 offen offset:2048 sc1\n"
"v_mfma_f32_16x16x32_bf16 v[92:95], v[188:191], a[140:143], v[92:95]\n"
"ds_read_b64_tr_b16 v[180:181], v227 offset:62464\n"
"v_mul_f32 v70, v8, v70\n"
"v_mfma_f32_16x16x32_bf16 v[92:95], v[192:195], a[148:151], v[92:95]\n"
"s_waitcnt lgkmcnt(12)\n"
"v_fmac_f32 v254, v214, v252\n"
"v_mfma_f32_16x16x32_bf16 v[92:95], v[196:199], a[156:159], v[92:95]\n"
"ds_read_b64_tr_b16 v[182:183], v227 offset:62720\n"
"s_waitcnt lgkmcnt(8)\n"
"v_mfma_f32_16x16x32_bf16 v[144:147], a[128:131], v[152:155], v[144:147]\n"
"ds_read_b64_tr_b16 v[184:185], v228 offset:50176\n"
"v_fmac_f32 v255, v214, v253\n"
"v_mfma_f32_16x16x32_bf16 v[144:147], a[136:139], v[156:159], v[144:147]\n"
"ds_read_b64_tr_b16 v[186:187], v228 offset:50432\n"
"buffer_load_dwordx4 a[28:31], v247, s[28:31], 0 offen offset:3072 sc1\n"
"v_mfma_f32_16x16x32_bf16 v[144:147], a[144:147], v[160:163], v[144:147]\n"
"ds_read_b64_tr_b16 v[188:189], v228 offset:54272\n"
"s_mov_b32 s28, s72\n"
"v_mfma_f32_16x16x32_bf16 v[144:147], a[152:155], v[164:167], v[144:147]\n"
"ds_read_b64_tr_b16 v[190:191], v228 offset:54528\n"
"v_fma_f32 v254, -v215, v253, v254\n"
"v_mfma_f32_16x16x32_bf16 v[148:151], a[132:135], v[152:155], v[148:151]\n"
"ds_read_b64_tr_b16 v[192:193], v228 offset:58368\n"
"v_fmac_f32 v255, v215, v252\n"
"v_mfma_f32_16x16x32_bf16 v[148:151], a[140:143], v[156:159], v[148:151]\n"
"ds_read_b64_tr_b16 v[194:195], v228 offset:58624\n"
"v_fmac_f32 v4, v214, v254\n"
"v_mfma_f32_16x16x32_bf16 v[148:151], a[148:151], v[160:163], v[148:151]\n"
"ds_read_b64_tr_b16 v[196:197], v228 offset:62464\n"
"buffer_load_dwordx4 a[32:35], v246, s[28:31], 0 offen offset:0 sc1\n"
"v_mfma_f32_16x16x32_bf16 v[148:151], a[156:159], v[164:167], v[148:151]\n"
"ds_read_b64_tr_b16 v[198:199], v228 offset:62720\n"
"s_waitcnt lgkmcnt(8)\n"
"v_mfma_f32_16x16x32_bf16 v[80:83], v[168:171], a[160:163], v[80:83]\n"
"ds_read_b128 v[152:155], v231 offset:1280\n"
"v_fmac_f32 v5, v214, v255\n"
"v_mfma_f32_16x16x32_bf16 v[80:83], v[172:175], a[168:171], v[80:83]\n"
"ds_read_b128 v[156:159], v231 offset:1344\n"
"v_fma_f32 v4, -v215, v255, v4\n"
"v_mfma_f32_16x16x32_bf16 v[80:83], v[176:179], a[176:179], v[80:83]\n"
"ds_read_b128 v[160:163], v231 offset:1408\n"
"v_fmac_f32 v5, v215, v254\n"
"v_mfma_f32_16x16x32_bf16 v[80:83], v[180:183], a[184:187], v[80:83]\n"
"ds_read_b128 v[164:167], v231 offset:1472\n"
"buffer_load_dwordx4 a[40:43], v246, s[28:31], 0 offen offset:1024 sc1\n"
"v_mfma_f32_16x16x32_bf16 v[84:87], v[168:171], a[164:167], v[84:87]\n"
"s_waitcnt vmcnt(30)\n"
"s_barrier\n"
"v_mfma_f32_16x16x32_bf16 v[84:87], v[172:175], a[172:175], v[84:87]\n"
"ds_read_b64_tr_b16 v[168:169], v229 offset:0\n"
"v_mov_b32 v128, v4\n"
"v_mfma_f32_16x16x32_bf16 v[84:87], v[176:179], a[180:183], v[84:87]\n"
"ds_read_b64_tr_b16 v[170:171], v229 offset:256\n"
"v_mov_b32 v130, v5\n"
"v_mfma_f32_16x16x32_bf16 v[84:87], v[180:183], a[188:191], v[84:87]\n"
"ds_read_b64_tr_b16 v[172:173], v229 offset:4096\n"
"s_waitcnt lgkmcnt(7)\n"
"v_mfma_f32_16x16x32_bf16 v[88:91], v[184:187], a[160:163], v[88:91]\n"
"buffer_store_dwordx4 v[128:131], v7, s[36:39], s90 offen sc1\n"
"v_mfma_f32_16x16x32_bf16 v[88:91], v[188:191], a[168:171], v[88:91]\n"
"ds_read_b64_tr_b16 v[174:175], v229 offset:4352\n"
"v_cndmask_b32 v14, 0, v250, s[94:95]\n"
"v_mfma_f32_16x16x32_bf16 v[88:91], v[192:195], a[176:179], v[88:91]\n"
"ds_read_b64_tr_b16 v[176:177], v229 offset:8192\n"
"v_cndmask_b32 v14, v14, v252, s[96:97]\n"
"v_mfma_f32_16x16x32_bf16 v[88:91], v[196:199], a[184:187], v[88:91]\n"
"ds_read_b64_tr_b16 v[178:179], v229 offset:8448\n"
"v_cndmask_b32 v14, v14, v254, s[98:99]\n"
"v_mfma_f32_16x16x32_bf16 v[92:95], v[184:187], a[164:167], v[92:95]\n"
"ds_read_b64_tr_b16 v[180:181], v229 offset:12288\n"
"buffer_load_dwordx4 a[36:39], v246, s[28:31], 0 offen offset:2048 sc1\n"
"v_mfma_f32_16x16x32_bf16 v[92:95], v[188:191], a[172:175], v[92:95]\n"
"ds_read_b64_tr_b16 v[182:183], v229 offset:12544\n"
"v_cndmask_b32 v15, 0, v251, s[94:95]\n"
"v_mfma_f32_16x16x32_bf16 v[92:95], v[192:195], a[180:183], v[92:95]\n"
"ds_read_b64_tr_b16 v[184:185], v230 offset:0\n"
"v_cndmask_b32 v15, v15, v253, s[96:97]\n"
"v_mfma_f32_16x16x32_bf16 v[92:95], v[196:199], a[188:191], v[92:95]\n"
"ds_read_b64_tr_b16 v[186:187], v230 offset:256\n"
"s_waitcnt lgkmcnt(10)\n"
"v_mfma_f32_16x16x32_bf16 v[144:147], a[160:163], v[152:155], v[144:147]\n"
"ds_read_b64_tr_b16 v[188:189], v230 offset:4096\n"
"buffer_load_dwordx4 a[44:47], v246, s[28:31], 0 offen offset:3072 sc1\n"
"v_mfma_f32_16x16x32_bf16 v[144:147], a[168:171], v[156:159], v[144:147]\n"
"ds_read_b64_tr_b16 v[190:191], v230 offset:4352\n"
"v_cndmask_b32 v15, v15, v255, s[98:99]\n"
"v_mfma_f32_16x16x32_bf16 v[144:147], a[176:179], v[160:163], v[144:147]\n"
"ds_read_b64_tr_b16 v[192:193], v230 offset:8192\n"
"s_lshl_b32 s88, s54, 2\n"
"v_mfma_f32_16x16x32_bf16 v[144:147], a[184:187], v[164:167], v[144:147]\n"
"ds_read_b64_tr_b16 v[194:195], v230 offset:8448\n"
"s_sub_u32 s88, s55, s88\n"
"v_mfma_f32_16x16x32_bf16 v[148:151], a[164:167], v[152:155], v[148:151]\n"
"ds_read_b64_tr_b16 v[196:197], v230 offset:12288\n"
"buffer_load_dwordx4 a[48:51], v247, s[28:31], 0 offen offset:0 sc1\n"
"v_mfma_f32_16x16x32_bf16 v[148:151], a[172:175], v[156:159], v[148:151]\n"
"ds_read_b64_tr_b16 v[198:199], v230 offset:12544\n"
"s_lshl_b32 s98, s88, 4\n"
"v_mfma_f32_16x16x32_bf16 v[148:151], a[180:183], v[160:163], v[148:151]\n"
"ds_read_b128 v[152:155], v231 offset:1536\n"
"s_add_u32 s98, s98, s56\n"
"v_mfma_f32_16x16x32_bf16 v[148:151], a[188:191], v[164:167], v[148:151]\n"
"ds_read_b128 v[156:159], v231 offset:1600\n"
"s_waitcnt lgkmcnt(10)\n"
"v_mfma_f32_16x16x32_bf16 v[80:83], v[168:171], a[192:195], v[80:83]\n"
"buffer_load_dwordx4 a[56:59], v247, s[28:31], 0 offen offset:1024 sc1\n"
"s_lshl_b32 s98, s98, 10\n"
"v_mfma_f32_16x16x32_bf16 v[80:83], v[172:175], a[200:203], v[80:83]\n"
"ds_read_b128 v[160:163], v231 offset:1664\n"
"s_add_u32 s99, s98, 38780928\n"
"v_mfma_f32_16x16x32_bf16 v[80:83], v[176:179], a[208:211], v[80:83]\n"
"s_cmp_ge_i32 s88, 1\n"
"s_cselect_b32 s100, s99, 1879048192\n"
"v_mfma_f32_16x16x32_bf16 v[80:83], v[180:183], a[216:219], v[80:83]\n"
"ds_read_b128 v[164:167], v231 offset:1728\n"
"v_fma_f32 v70, -v9, v78, v70\n"
"v_mfma_f32_16x16x32_bf16 v[84:87], v[168:171], a[196:199], v[84:87]\n"
"s_cmp_ge_i32 s88, 1\n"
"s_cselect_b32 s94, s61, 0\n"
"v_mfma_f32_16x16x32_bf16 v[84:87], v[172:175], a[204:207], v[84:87]\n"
"ds_read_b64_tr_b16 v[168:169], v229 offset:1024\n"
"buffer_load_dwordx4 a[52:55], v247, s[28:31], 0 offen offset:2048 sc1\n"
"v_mfma_f32_16x16x32_bf16 v[84:87], v[176:179], a[212:215], v[84:87]\n"
"ds_read_b64_tr_b16 v[170:171], v229 offset:1280\n"
"s_add_u32 s99, s98, 38764544\n"
"v_mfma_f32_16x16x32_bf16 v[84:87], v[180:183], a[220:223], v[84:87]\n"
"ds_read_b64_tr_b16 v[172:173], v229 offset:5120\n"
"s_waitcnt lgkmcnt(7)\n"
"v_mfma_f32_16x16x32_bf16 v[88:91], v[184:187], a[192:195], v[88:91]\n"
"ds_read_b64_tr_b16 v[174:175], v229 offset:5376\n"
"v_fma_f32 v78, v8, v78, v10\n"
"v_mfma_f32_16x16x32_bf16 v[88:91], v[188:191], a[200:203], v[88:91]\n"
"s_cmp_ge_i32 s88, 2\n"
"s_cselect_b32 s16, s99, 1879048192\n"
"v_mfma_f32_16x16x32_bf16 v[88:91], v[192:195], a[208:211], v[88:91]\n"
"ds_read_b64_tr_b16 v[176:177], v229 offset:9216\n"
"buffer_load_dwordx4 a[60:63], v247, s[28:31], 0 offen offset:3072 sc1\n"
"v_mfma_f32_16x16x32_bf16 v[88:91], v[196:199], a[216:219], v[88:91]\n"
"ds_read_b64_tr_b16 v[178:179], v229 offset:9472\n"
"s_mov_b32 s28, s73\n"
"v_mfma_f32_16x16x32_bf16 v[92:95], v[184:187], a[196:199], v[92:95]\n"
"s_cmp_ge_i32 s88, 2\n"
"s_cselect_b32 s95, s61, 0\n"
"v_mfma_f32_16x16x32_bf16 v[92:95], v[188:191], a[204:207], v[92:95]\n"
"ds_read_b64_tr_b16 v[180:181], v229 offset:13312\n"
"s_add_u32 s99, s98, 38748160\n"
"v_mfma_f32_16x16x32_bf16 v[92:95], v[192:195], a[212:215], v[92:95]\n"
"ds_read_b64_tr_b16 v[182:183], v229 offset:13568\n"
"v_mul_f32 v11, v9, v71\n"
"v_mfma_f32_16x16x32_bf16 v[92:95], v[196:199], a[220:223], v[92:95]\n"
"ds_read_b64_tr_b16 v[184:185], v230 offset:1024\n"
"s_waitcnt lgkmcnt(9)\n"
"v_mfma_f32_16x16x32_bf16 v[144:147], a[192:195], v[152:155], v[144:147]\n"
"s_cmp_ge_i32 s88, 3\n"
"s_cselect_b32 s89, s99, 1879048192\n"
"v_mfma_f32_16x16x32_bf16 v[144:147], a[200:203], v[156:159], v[144:147]\n"
"ds_read_b64_tr_b16 v[186:187], v230 offset:1280\n"
"buffer_load_dwordx4 v238, s[20:23], 0 offen offset:0 lds\n"
"v_mfma_f32_16x16x32_bf16 v[144:147], a[208:211], v[160:163], v[144:147]\n"
"ds_read_b64_tr_b16 v[188:189], v230 offset:5120\n"
"v_mul_f32 v71, v8, v71\n"
"v_mfma_f32_16x16x32_bf16 v[144:147], a[216:219], v[164:167], v[144:147]\n"
"s_cmp_ge_i32 s88, 3\n"
"s_cselect_b32 s96, s61, 0\n"
"v_mfma_f32_16x16x32_bf16 v[148:151], a[196:199], v[152:155], v[148:151]\n"
"ds_read_b64_tr_b16 v[190:191], v230 offset:5376\n"
"s_add_u32 s99, s98, 38731776\n"
"v_mfma_f32_16x16x32_bf16 v[148:151], a[204:207], v[156:159], v[148:151]\n"
"ds_read_b64_tr_b16 v[192:193], v230 offset:9216\n"
"buffer_load_dwordx4 v239, s[20:23], 0 offen offset:1024 lds\n"
"v_mfma_f32_16x16x32_bf16 v[148:151], a[212:215], v[160:163], v[148:151]\n"
"ds_read_b64_tr_b16 v[194:195], v230 offset:9472\n"
"v_fma_f32 v71, -v9, v79, v71\n"
"v_mfma_f32_16x16x32_bf16 v[148:151], a[220:223], v[164:167], v[148:151]\n"
"ds_read_b64_tr_b16 v[196:197], v230 offset:13312\n"
"s_waitcnt lgkmcnt(7)\n"
"v_mfma_f32_16x16x32_bf16 v[80:83], v[168:171], a[224:227], v[80:83]\n"
"s_cmp_ge_i32 s88, 4\n"
"s_cselect_b32 s90, s99, 1879048192\n"
"v_mfma_f32_16x16x32_bf16 v[80:83], v[172:175], a[232:235], v[80:83]\n"
"ds_read_b64_tr_b16 v[198:199], v230 offset:13568\n"
"ds_read_b128 v[152:155], v231 offset:1792\n"
"v_mfma_f32_16x16x32_bf16 v[80:83], v[176:179], a[240:243], v[80:83]\n"
"ds_read_b128 v[156:159], v231 offset:1856\n"
"buffer_load_dwordx4 v240, s[20:23], 0 offen offset:2048 lds\n"
"v_mfma_f32_16x16x32_bf16 v[80:83], v[180:183], a[248:251], v[80:83]\n"
"s_cmp_ge_i32 s88, 4\n"
"s_cselect_b32 s97, s61, 0\n"
"v_mfma_f32_16x16x32_bf16 v[84:87], v[168:171], a[228:231], v[84:87]\n"
"ds_read_b128 v[160:163], v231 offset:1920\n"
"v_fma_f32 v79, v8, v79, v11\n"
"v_mfma_f32_16x16x32_bf16 v[84:87], v[172:175], a[236:239], v[84:87]\n"
"ds_read_b128 v[164:167], v231 offset:1984\n"
"v_mfma_f32_16x16x32_bf16 v[84:87], v[176:179], a[244:247], v[84:87]\n"
"ds_read_b64 v[226:227], v211 offset:5120\n"
"buffer_load_dwordx4 v241, s[20:23], 0 offen offset:3072 lds\n"
"v_mfma_f32_16x16x32_bf16 v[84:87], v[180:183], a[252:255], v[84:87]\n"
"s_lshl_b32 s92, s74, 5\n"
"s_waitcnt lgkmcnt(5)\n"
"v_mfma_f32_16x16x32_bf16 v[88:91], v[184:187], a[224:227], v[88:91]\n"
"s_waitcnt vmcnt(37)\n"
"s_barrier\n"
"v_mfma_f32_16x16x32_bf16 v[88:91], v[188:191], a[232:235], v[88:91]\n"
"ds_read_b64_tr_b16 v[168:169], v244 offset:16384\n"
"s_add_u32 s20, s40, s92\n"
"v_mfma_f32_16x16x32_bf16 v[88:91], v[192:195], a[240:243], v[88:91]\n"
"ds_read_b64_tr_b16 v[170:171], v244 offset:20480\n"
"s_add_u32 m0, s63, 32768\n"
"v_mfma_f32_16x16x32_bf16 v[88:91], v[196:199], a[248:251], v[88:91]\n"
"ds_read_b64_tr_b16 v[172:173], v244 offset:24576\n"
"buffer_load_dwordx4 a[64:67], v246, s[28:31], 0 offen offset:0 sc1\n"
"v_mfma_f32_16x16x32_bf16 v[92:95], v[184:187], a[228:231], v[92:95]\n"
"ds_read_b64_tr_b16 v[174:175], v244 offset:28672\n"
"s_lshl_b32 s98, s54, 10\n"
"v_mfma_f32_16x16x32_bf16 v[92:95], v[188:191], a[236:239], v[92:95]\n"
"v_add_u32 v211, s98, v211\n"
"v_mul_f32_e64 v80, v80, abs(v80)\n"
"v_mfma_f32_16x16x32_bf16 v[92:95], v[192:195], a[244:247], v[92:95]\n"
"ds_read_b128 v[2:5], v211 offset:6144\n"
"v_mul_f32_e64 v81, v81, abs(v81)\n"
"v_mfma_f32_16x16x32_bf16 v[92:95], v[196:199], a[252:255], v[92:95]\n"
"v_mul_f32_e64 v82, v82, abs(v82)\n"
"s_waitcnt lgkmcnt(6)\n"
"v_mfma_f32_16x16x32_bf16 v[144:147], a[224:227], v[152:155], v[144:147]\n"
"buffer_load_dwordx4 v[184:187], v7, s[36:39], s100 offen sc1\n"
"v_mfma_f32_16x16x32_bf16 v[144:147], a[232:235], v[156:159], v[144:147]\n"
"ds_read_b64_tr_b16 v[176:177], v245 offset:16384\n"
"v_mul_f32_e64 v83, v83, abs(v83)\n"
"v_mfma_f32_16x16x32_bf16 v[144:147], a[240:243], v[160:163], v[144:147]\n"
"ds_read_b64_tr_b16 v[178:179], v245 offset:20480\n"
"v_mul_f32_e64 v88, v88, abs(v88)\n"
"v_mfma_f32_16x16x32_bf16 v[144:147], a[248:251], v[164:167], v[144:147]\n"
"ds_read_b64_tr_b16 v[180:181], v245 offset:24576\n"
"v_mul_f32_e64 v89, v89, abs(v89)\n"
"v_mfma_f32_16x16x32_bf16 v[148:151], a[228:231], v[152:155], v[148:151]\n"
"ds_read_b64_tr_b16 v[182:183], v245 offset:28672\n"
"v_mul_f32_e64 v90, v90, abs(v90)\n"
"v_mfma_f32_16x16x32_bf16 v[148:151], a[236:239], v[156:159], v[148:151]\n"
"buffer_load_dwordx4 v[188:191], v7, s[36:39], s16 offen sc1\n"
"v_mfma_f32_16x16x32_bf16 v[148:151], a[244:247], v[160:163], v[148:151]\n"
"v_mul_f32_e64 v91, v91, abs(v91)\n"
"v_cvt_pk_bf16_f32 v80, v80, v81\n"
"v_mfma_f32_16x16x32_bf16 v[148:151], a[252:255], v[164:167], v[148:151]\n"
"v_cvt_pk_bf16_f32 v81, v82, v83\n"
"s_waitcnt vmcnt(25) lgkmcnt(5)\n"
"v_mfma_f32_16x16x32_bf16 a[160:163], v[168:171], v[96:99], 0\n"
"v_cvt_pk_bf16_f32 v82, v88, v89\n"
"v_cvt_pk_bf16_f32 v83, v90, v91\n"
"v_mfma_f32_16x16x32_bf16 a[160:163], v[172:175], v[100:103], a[160:163]\n"
"ds_read_b64_tr_b16 v[152:153], v244 offset:17408\n"
"v_pk_max_i16 v80, v80, 0\n"
"v_mfma_f32_16x16x32_bf16 a[164:167], v[168:171], v[104:107], 0\n"
"ds_read_b64_tr_b16 v[154:155], v244 offset:21504\n"
"v_pk_max_i16 v81, v81, 0\n"
"v_mfma_f32_16x16x32_bf16 a[164:167], v[172:175], v[108:111], a[164:167]\n"
"buffer_load_dwordx4 v[192:195], v7, s[36:39], s89 offen sc1\n"
"v_mfma_f32_16x16x32_bf16 a[168:171], v[168:171], v[112:115], 0\n"
".Lk_h7:\n"
"s_cbranch_execz .Lk_h11\n"
"ds_read_b64_tr_b16 v[156:157], v244 offset:25600\n"
"v_pk_max_i16 v82, v82, 0\n"
"v_mfma_f32_16x16x32_bf16 a[168:171], v[172:175], v[116:119], a[168:171]\n"
"ds_read_b64_tr_b16 v[158:159], v244 offset:29696\n"
"v_pk_max_i16 v83, v83, 0\n"
"v_mfma_f32_16x16x32_bf16 a[172:175], v[168:171], v[120:123], 0\n"
"v_mul_f32_e64 v84, v84, abs(v84)\n"
"v_mul_f32_e64 v85, v85, abs(v85)\n"
"v_mfma_f32_16x16x32_bf16 a[172:175], v[172:175], v[124:127], a[172:175]\n"
"v_mul_f32_e64 v86, v86, abs(v86)\n"
"s_waitcnt lgkmcnt(4)\n"
"v_mfma_f32_16x16x32_bf16 a[176:179], v[176:179], v[96:99], 0\n"
"buffer_load_dwordx4 v[196:199], v7, s[36:39], s90 offen sc1\n"
"v_mfma_f32_16x16x32_bf16 a[176:179], v[180:183], v[100:103], a[176:179]\n"
"ds_read_b64_tr_b16 v[160:161], v245 offset:17408\n"
"v_mul_f32_e64 v87, v87, abs(v87)\n"
"v_mfma_f32_16x16x32_bf16 a[180:183], v[176:179], v[104:107], 0\n"
"ds_read_b64_tr_b16 v[162:163], v245 offset:21504\n"
"v_mul_f32_e64 v92, v92, abs(v92)\n"
"v_mfma_f32_16x16x32_bf16 a[180:183], v[180:183], v[108:111], a[180:183]\n"
"ds_read_b64_tr_b16 v[164:165], v245 offset:25600\n"
"v_mul_f32_e64 v93, v93, abs(v93)\n"
"v_mfma_f32_16x16x32_bf16 a[184:187], v[176:179], v[112:115], 0\n"
"ds_read_b64_tr_b16 v[166:167], v245 offset:29696\n"
"buffer_load_dwordx4 a[72:75], v246, s[28:31], 0 offen offset:1024 sc1\n"
"v_mfma_f32_16x16x32_bf16 a[184:187], v[180:183], v[116:119], a[184:187]\n"
"v_mul_f32_e64 v94, v94, abs(v94)\n"
"v_mul_f32_e64 v95, v95, abs(v95)\n"
"v_mfma_f32_16x16x32_bf16 a[188:191], v[176:179], v[120:123], 0\n"
"v_cvt_pk_bf16_f32 v84, v84, v85\n"
"v_cvt_pk_bf16_f32 v85, v86, v87\n"
"v_mfma_f32_16x16x32_bf16 a[188:191], v[180:183], v[124:127], a[188:191]\n"
"v_cvt_pk_bf16_f32 v86, v92, v93\n"
"s_waitcnt lgkmcnt(4)\n"
"v_mfma_f32_16x16x32_bf16 a[192:195], v[152:155], v[96:99], 0\n"
"buffer_load_dwordx4 a[68:71], v246, s[28:31], 0 offen offset:2048 sc1\n"
"v_cvt_pk_bf16_f32 v87, v94, v95\n"
"v_mfma_f32_16x16x32_bf16 a[192:195], v[156:159], v[100:103], a[192:195]\n"
"ds_read_b64_tr_b16 v[168:169], v244 offset:19456\n"
"v_pk_max_i16 v84, v84, 0\n"
"v_mfma_f32_16x16x32_bf16 a[196:199], v[152:155], v[104:107], 0\n"
"ds_read_b64_tr_b16 v[170:171], v244 offset:23552\n"
"v_pk_max_i16 v85, v85, 0\n"
"v_mfma_f32_16x16x32_bf16 a[196:199], v[156:159], v[108:111], a[196:199]\n"
"ds_read_b64_tr_b16 v[172:173], v244 offset:27648\n"
"v_pk_max_i16 v86, v86, 0\n"
"v_mfma_f32_16x16x32_bf16 a[200:203], v[152:155], v[112:115], 0\n"
"ds_read_b64_tr_b16 v[174:175], v244 offset:31744\n"
"v_pk_max_i16 v87, v87, 0\n"
"v_mfma_f32_16x16x32_bf16 a[200:203], v[156:159], v[116:119], a[200:203]\n"
"buffer_store_dwordx4 v[80:83], v200, s[36:39], s91 offen offset:0 sc1\n"
"v_mfma_f32_16x16x32_bf16 a[204:207], v[152:155], v[120:123], 0\n"
"v_min_f32 v144, 0x41f00000, v144\n"
"v_mul_f32 v144, 0x3fb8aa3b, v144\n"
"v_mfma_f32_16x16x32_bf16 a[204:207], v[156:159], v[124:127], a[204:207]\n"
"v_min_f32 v145, 0x41f00000, v145\n"
"s_waitcnt lgkmcnt(4)\n"
"v_mfma_f32_16x16x32_bf16 a[208:211], v[160:163], v[96:99], 0\n"
"v_mul_f32 v145, 0x3fb8aa3b, v145\n"
"v_min_f32 v146, 0x41f00000, v146\n"
"v_mfma_f32_16x16x32_bf16 a[208:211], v[164:167], v[100:103], a[208:211]\n"
"ds_read_b64_tr_b16 v[176:177], v245 offset:19456\n"
"v_mul_f32 v146, 0x3fb8aa3b, v146\n"
"v_mfma_f32_16x16x32_bf16 a[212:215], v[160:163], v[104:107], 0\n"
"buffer_store_dwordx4 v[84:87], v200, s[36:39], s91 offen offset:1024 sc1\n"
"v_mfma_f32_16x16x32_bf16 a[212:215], v[164:167], v[108:111], a[212:215]\n"
"ds_read_b64_tr_b16 v[178:179], v245 offset:23552\n"
"v_min_f32 v147, 0x41f00000, v147\n"
"v_mfma_f32_16x16x32_bf16 a[216:219], v[160:163], v[112:115], 0\n"
"ds_read_b64_tr_b16 v[180:181], v245 offset:27648\n"
"v_mul_f32 v147, 0x3fb8aa3b, v147\n"
"v_mfma_f32_16x16x32_bf16 a[216:219], v[164:167], v[116:119], a[216:219]\n"
"ds_read_b64_tr_b16 v[182:183], v245 offset:31744\n"
"v_min_f32 v148, 0x41f00000, v148\n"
"v_mfma_f32_16x16x32_bf16 a[220:223], v[160:163], v[120:123], 0\n"
"buffer_load_dwordx4 a[76:79], v246, s[28:31], 0 offen offset:3072 sc1\n"
"v_mul_f32 v148, 0x3fb8aa3b, v148\n"
"v_mfma_f32_16x16x32_bf16 a[220:223], v[164:167], v[124:127], a[220:223]\n"
"v_min_f32 v149, 0x41f00000, v149\n"
"s_waitcnt lgkmcnt(4)\n"
"v_mfma_f32_16x16x32_bf16 a[224:227], v[168:171], v[96:99], 0\n"
"v_mul_f32 v149, 0x3fb8aa3b, v149\n"
"v_min_f32 v150, 0x41f00000, v150\n"
"v_mfma_f32_16x16x32_bf16 a[224:227], v[172:175], v[100:103], a[224:227]\n"
"ds_read_b64_tr_b16 v[152:153], v244 offset:18432\n"
"v_mul_f32 v150, 0x3fb8aa3b, v150\n"
"v_mfma_f32_16x16x32_bf16 a[228:231], v[168:171], v[104:107], 0\n"
"ds_read_b64_tr_b16 v[154:155], v244 offset:22528\n"
"buffer_load_dwordx4 a[80:83], v247, s[28:31], 0 offen offset:0 sc1\n"
"v_mfma_f32_16x16x32_bf16 a[228:231], v[172:175], v[108:111], a[228:231]\n"
"ds_read_b64_tr_b16 v[156:157], v244 offset:26624\n"
"v_min_f32 v151, 0x41f00000, v151\n"
"v_mfma_f32_16x16x32_bf16 a[232:235], v[168:171], v[112:115], 0\n"
"ds_read_b64_tr_b16 v[158:159], v244 offset:30720\n"
"v_mul_f32 v151, 0x3fb8aa3b, v151\n"
"v_mfma_f32_16x16x32_bf16 a[232:235], v[172:175], v[116:119], a[232:235]\n"
"v_exp_f32 v144, v144\n"
"v_mfma_f32_16x16x32_bf16 a[236:239], v[168:171], v[120:123], 0\n"
"buffer_load_dwordx4 a[88:91], v247, s[28:31], 0 offen offset:1024 sc1\n"
"v_and_b32 v211, 7, v1\n"
"v_mfma_f32_16x16x32_bf16 a[236:239], v[172:175], v[124:127], a[236:239]\n"
"s_waitcnt lgkmcnt(4)\n"
"v_mfma_f32_16x16x32_bf16 a[240:243], v[176:179], v[96:99], 0\n"
"v_exp_f32 v145, v145\n"
"v_mfma_f32_16x16x32_bf16 a[240:243], v[180:183], v[100:103], a[240:243]\n"
"ds_read_b64_tr_b16 v[160:161], v245 offset:18432\n"
"v_mfma_f32_16x16x32_bf16 a[244:247], v[176:179], v[104:107], 0\n"
"ds_read_b64_tr_b16 v[162:163], v245 offset:22528\n"
"buffer_load_dwordx4 a[84:87], v247, s[28:31], 0 offen offset:2048 sc1\n"
"v_mfma_f32_16x16x32_bf16 a[244:247], v[180:183], v[108:111], a[244:247]\n"
"v_exp_f32 v146, v146\n"
"v_mfma_f32_16x16x32_bf16 a[248:251], v[176:179], v[112:115], 0\n"
"ds_read_b64_tr_b16 v[164:165], v245 offset:26624\n"
"v_mfma_f32_16x16x32_bf16 a[248:251], v[180:183], v[116:119], a[248:251]\n"
"ds_read_b64_tr_b16 v[166:167], v245 offset:30720\n"
"v_mfma_f32_16x16x32_bf16 a[252:255], v[176:179], v[120:123], 0\n"
"v_exp_f32 v147, v147\n"
"v_mfma_f32_16x16x32_bf16 a[252:255], v[180:183], v[124:127], a[252:255]\n"
"buffer_load_dwordx4 a[92:95], v247, s[28:31], 0 offen offset:3072 sc1\n"
"s_waitcnt lgkmcnt(4)\n"
"v_mfma_f32_16x16x32_bf16 v[80:83], v[152:155], v[96:99], 0\n"
"s_barrier\n"
"s_mov_b32 s28, s74\n"
"v_mfma_f32_16x16x32_bf16 v[80:83], v[156:159], v[100:103], v[80:83]\n"
"ds_read_b64_tr_b16 v[168:169], v244 offset:32768\n"
"v_mfma_f32_16x16x32_bf16 v[84:87], v[152:155], v[104:107], 0\n"
"v_exp_f32 v148, v148\n"
"v_mfma_f32_16x16x32_bf16 v[84:87], v[156:159], v[108:111], v[84:87]\n"
"ds_read_b64_tr_b16 v[170:171], v244 offset:36864\n"
"buffer_load_dwordx4 v238, s[20:23], 0 offen offset:0 lds\n"
"v_mfma_f32_16x16x32_bf16 v[88:91], v[152:155], v[112:115], 0\n"
"ds_read_b64_tr_b16 v[172:173], v244 offset:40960\n"
"v_mfma_f32_16x16x32_bf16 v[88:91], v[156:159], v[116:119], v[88:91]\n"
"v_exp_f32 v149, v149\n"
"v_mfma_f32_16x16x32_bf16 v[92:95], v[152:155], v[120:123], 0\n"
"ds_read_b64_tr_b16 v[174:175], v244 offset:45056\n"
"v_mfma_f32_16x16x32_bf16 v[92:95], v[156:159], v[124:127], v[92:95]\n"
"s_waitcnt lgkmcnt(4)\n"
"v_mfma_f32_16x16x32_bf16 v[128:131], v[160:163], v[96:99], 0\n"
"v_exp_f32 v150, v150\n"
"v_mfma_f32_16x16x32_bf16 v[128:131], v[164:167], v[100:103], v[128:131]\n"
"ds_read_b64_tr_b16 v[176:177], v245 offset:32768\n"
"buffer_load_dwordx4 v239, s[20:23], 0 offen offset:1024 lds\n"
"v_mfma_f32_16x16x32_bf16 v[132:135], v[160:163], v[104:107], 0\n"
"ds_read_b64_tr_b16 v[178:179], v245 offset:36864\n"
"v_mfma_f32_16x16x32_bf16 v[132:135], v[164:167], v[108:111], v[132:135]\n"
"s_waitcnt vmcnt(14)\n"
"v_xor_b32 v185, s94, v185\n"
"v_mfma_f32_16x16x32_bf16 v[136:139], v[160:163], v[112:115], 0\n"
"v_exp_f32 v151, v151\n"
"v_mfma_f32_16x16x32_bf16 v[136:139], v[164:167], v[116:119], v[136:139]\n"
"ds_read_b64_tr_b16 v[180:181], v245 offset:40960\n"
"ds_read_b64_tr_b16 v[182:183], v245 offset:45056\n"
"v_mfma_f32_16x16x32_bf16 v[140:143], v[160:163], v[120:123], 0\n"
"buffer_load_dwordx4 v240, s[20:23], 0 offen offset:2048 lds\n"
"v_xor_b32 v187, s94, v187\n"
"v_mfma_f32_16x16x32_bf16 v[140:143], v[164:167], v[124:127], v[140:143]\n"
"v_lshrrev_b32 v236, 1, v209\n"
"s_waitcnt lgkmcnt(4)\n"
"v_mfma_f32_16x16x32_bf16 a[160:163], v[168:171], a[0:3], a[160:163]\n"
"s_waitcnt vmcnt(14)\n"
"v_xor_b32 v189, s95, v189\n"
"v_mfma_f32_16x16x32_bf16 a[160:163], v[172:175], a[4:7], a[160:163]\n"
"ds_read_b64_tr_b16 v[152:153], v244 offset:33792\n"
"v_xor_b32 v191, s95, v191\n"
"v_mfma_f32_16x16x32_bf16 a[164:167], v[168:171], a[8:11], a[164:167]\n"
"ds_read_b64_tr_b16 v[154:155], v244 offset:37888\n"
"buffer_load_dwordx4 v241, s[20:23], 0 offen offset:3072 lds\n"
"v_mfma_f32_16x16x32_bf16 a[164:167], v[172:175], a[12:15], a[164:167]\n"
"ds_read_b64_tr_b16 v[156:157], v244 offset:41984\n"
"s_lshl_b32 s92, s75, 5\n"
"v_mfma_f32_16x16x32_bf16 a[168:171], v[168:171], a[16:19], a[168:171]\n"
"ds_read_b64_tr_b16 v[158:159], v244 offset:46080\n"
"s_add_u32 s20, s40, s92\n"
"v_mfma_f32_16x16x32_bf16 a[168:171], v[172:175], a[20:23], a[168:171]\n"
"s_add_u32 m0, s63, 49152\n"
"v_min_u32 v236, 1, v236\n"
"v_mfma_f32_16x16x32_bf16 a[172:175], v[168:171], a[24:27], a[172:175]\n"
"s_waitcnt vmcnt(14)\n"
"v_xor_b32 v193, s96, v193\n"
"v_mfma_f32_16x16x32_bf16 a[172:175], v[172:175], a[28:31], a[172:175]\n"
"buffer_load_dwordx4 a[96:99], v246, s[28:31], 0 offen offset:0 sc1\n"
"s_waitcnt lgkmcnt(4)\n"
"v_mfma_f32_16x16x32_bf16 a[176:179], v[176:179], a[0:3], a[176:179]\n"
"v_xor_b32 v195, s96, v195\n"
"v_lshlrev_b32 v236, 30, v236\n"
"v_mfma_f32_16x16x32_bf16 a[176:179], v[180:183], a[4:7], a[176:179]\n"
"ds_read_b64_tr_b16 v[160:161], v245 offset:33792\n"
"v_lshl_add_u32 v236, v210, 5, v236\n"
"v_mfma_f32_16x16x32_bf16 a[180:183], v[176:179], a[8:11], a[180:183]\n"
"ds_read_b64_tr_b16 v[162:163], v245 offset:37888\n"
"v_lshl_add_u32 v236, v209, 4, v236\n"
"v_mfma_f32_16x16x32_bf16 a[180:183], v[180:183], a[12:15], a[180:183]\n"
"s_waitcnt vmcnt(14)\n"
"v_xor_b32 v197, s97, v197\n"
"v_mfma_f32_16x16x32_bf16 a[184:187], v[176:179], a[16:19], a[184:187]\n"
"ds_read_b64_tr_b16 v[164:165], v245 offset:41984\n"
"buffer_load_dwordx4 a[104:107], v246, s[28:31], 0 offen offset:1024 sc1\n"
"v_mfma_f32_16x16x32_bf16 a[184:187], v[180:183], a[20:23], a[184:187]\n"
"ds_read_b64_tr_b16 v[166:167], v245 offset:46080\n"
"v_xor_b32 v199, s97, v199\n"
"v_mfma_f32_16x16x32_bf16 a[188:191], v[176:179], a[24:27], a[188:191]\n"
"v_or3_b32 v208, v185, v187, v189\n"
"v_or3_b32 v6, v191, v193, v195\n"
"v_mfma_f32_16x16x32_bf16 a[188:191], v[180:183], a[28:31], a[188:191]\n"
"v_or3_b32 v208, v208, v197, v199\n"
"s_waitcnt lgkmcnt(4)\n"
"v_mfma_f32_16x16x32_bf16 a[192:195], v[152:155], a[0:3], a[192:195]\n"
"buffer_load_dwordx4 a[100:103], v246, s[28:31], 0 offen offset:2048 sc1\n"
"v_or_b32 v208, v208, v6\n"
"v_mfma_f32_16x16x32_bf16 a[192:195], v[156:159], a[4:7], a[192:195]\n"
"ds_read_b64_tr_b16 v[168:169], v244 offset:35840\n"
"v_sub_u32 v237, 0, v209\n"
"v_mfma_f32_16x16x32_bf16 a[196:199], v[152:155], a[8:11], a[196:199]\n"
"ds_read_b64_tr_b16 v[170:171], v244 offset:39936\n"
"v_bfi_b32 v8, v237, v148, v144\n"
"v_mfma_f32_16x16x32_bf16 a[196:199], v[156:159], a[12:15], a[196:199]\n"
"v_cmp_ne_u32 vcc, 0, v208\n"
"s_cbranch_vccnz .Lk_slow1\n"
".Lk_back1:\n"
"v_mfma_f32_16x16x32_bf16 a[200:203], v[152:155], a[16:19], a[200:203]\n"
"ds_read_b64_tr_b16 v[172:173], v244 offset:44032\n"
"buffer_load_dwordx4 a[108:111], v246, s[28:31], 0 offen offset:3072 sc1\n"
"v_mfma_f32_16x16x32_bf16 a[200:203], v[156:159], a[20:23], a[200:203]\n"
"ds_read_b64_tr_b16 v[174:175], v244 offset:48128\n"
"v_fmac_f32 v192, v226, v196\n"
"v_mfma_f32_16x16x32_bf16 a[204:207], v[152:155], a[24:27], a[204:207]\n"
"v_fmac_f32 v194, v226, v198\n"
"v_fma_f32 v192, -v227, v198, v192\n"
"v_mfma_f32_16x16x32_bf16 a[204:207], v[156:159], a[28:31], a[204:207]\n"
"v_fmac_f32 v194, v227, v196\n"
"s_waitcnt lgkmcnt(4)\n"
"v_mfma_f32_16x16x32_bf16 a[208:211], v[160:163], a[0:3], a[208:211]\n"
"v_fmac_f32 v188, v226, v192\n"
"v_fmac_f32 v190, v226, v194\n"
"v_mfma_f32_16x16x32_bf16 a[208:211], v[164:167], a[4:7], a[208:211]\n"
"ds_read_b64_tr_b16 v[176:177], v245 offset:35840\n"
"buffer_load_dwordx4 a[112:115], v247, s[28:31], 0 offen offset:0 sc1\n"
"v_mfma_f32_16x16x32_bf16 a[212:215], v[160:163], a[8:11], a[212:215]\n"
"ds_read_b64_tr_b16 v[178:179], v245 offset:39936\n"
"v_fma_f32 v188, -v227, v194, v188\n"
"v_mfma_f32_16x16x32_bf16 a[212:215], v[164:167], a[12:15], a[212:215]\n"
"ds_read_b64_tr_b16 v[180:181], v245 offset:44032\n"
"v_fmac_f32 v190, v227, v192\n"
"v_mfma_f32_16x16x32_bf16 a[216:219], v[160:163], a[16:19], a[216:219]\n"
"s_waitcnt vmcnt(14)\n"
"buffer_store_dword v219, v220, s[36:39], s43 offen sc1\n"
"v_mfma_f32_16x16x32_bf16 a[216:219], v[164:167], a[20:23], a[216:219]\n"
"ds_read_b64_tr_b16 v[182:183], v245 offset:48128\n"
"buffer_load_dwordx4 a[120:123], v247, s[28:31], 0 offen offset:1024 sc1\n"
"v_mfma_f32_16x16x32_bf16 a[220:223], v[160:163], a[24:27], a[220:223]\n"
"s_add_u32 s43, s43, 131072\n"
"v_fmac_f32 v184, v226, v188\n"
"v_mfma_f32_16x16x32_bf16 a[220:223], v[164:167], a[28:31], a[220:223]\n"
"v_fmac_f32 v186, v226, v190\n"
"s_waitcnt lgkmcnt(4)\n"
"v_mfma_f32_16x16x32_bf16 a[224:227], v[168:171], a[0:3], a[224:227]\n"
"v_fma_f32 v184, -v227, v190, v184\n"
"v_fmac_f32 v186, v227, v188\n"
"v_mfma_f32_16x16x32_bf16 a[224:227], v[172:175], a[4:7], a[224:227]\n"
"ds_read_b64_tr_b16 v[152:153], v244 offset:34816\n"
"v_mul_f32 v226, v4, v184\n"
"v_mfma_f32_16x16x32_bf16 a[228:231], v[168:171], a[8:11], a[228:231]\n"
"ds_read_b64_tr_b16 v[154:155], v244 offset:38912\n"
"buffer_load_dwordx4 a[116:119], v247, s[28:31], 0 offen offset:2048 sc1\n"
"v_mfma_f32_16x16x32_bf16 a[228:231], v[172:175], a[12:15], a[228:231]\n"
"ds_read_b64_tr_b16 v[156:157], v244 offset:43008\n"
"v_mul_f32 v227, v4, v186\n"
"v_mfma_f32_16x16x32_bf16 a[232:235], v[168:171], a[16:19], a[232:235]\n"
"ds_read_b64_tr_b16 v[158:159], v244 offset:47104\n"
"v_fma_f32 v226, -v5, v186, v226\n"
"v_mfma_f32_16x16x32_bf16 a[232:235], v[172:175], a[20:23], a[232:235]\n"
"v_fmac_f32 v227, v5, v184\n"
"v_bfi_b32 v9, v237, v149, v145\n"
"v_mfma_f32_16x16x32_bf16 a[236:239], v[168:171], a[24:27], a[236:239]\n"
"ds_write_b64 v205, v[226:227] offset:12288\n"
"v_mfma_f32_16x16x32_bf16 a[236:239], v[172:175], a[28:31], a[236:239]\n"
"ds_read_b64 v[192:193], v248 offset:0\n"
"s_waitcnt lgkmcnt(6)\n"
"v_mfma_f32_16x16x32_bf16 a[240:243], v[176:179], a[0:3], a[240:243]\n"
"ds_read_b64 v[194:195], v248 offset:512\n"
"buffer_load_dwordx4 a[124:127], v247, s[28:31], 0 offen offset:3072 sc1\n"
"v_mfma_f32_16x16x32_bf16 a[240:243], v[180:183], a[4:7], a[240:243]\n"
"ds_read_b64_tr_b16 v[160:161], v245 offset:34816\n"
"s_mov_b32 s28, s75\n"
"v_mfma_f32_16x16x32_bf16 a[244:247], v[176:179], a[8:11], a[244:247]\n"
"ds_read_b64_tr_b16 v[162:163], v245 offset:38912\n"
"v_bfi_b32 v10, v237, v150, v146\n"
"v_mfma_f32_16x16x32_bf16 a[244:247], v[180:183], a[12:15], a[244:247]\n"
"ds_read_b64 v[196:197], v248 offset:1024\n"
"v_bfi_b32 v11, v237, v151, v147\n"
"v_mfma_f32_16x16x32_bf16 a[248:251], v[176:179], a[16:19], a[248:251]\n"
"ds_read_b64_tr_b16 v[164:165], v245 offset:43008\n"
"buffer_load_dwordx4 v238, s[20:23], 0 offen offset:0 lds\n"
"v_mfma_f32_16x16x32_bf16 a[248:251], v[180:183], a[20:23], a[248:251]\n"
"ds_read_b64_tr_b16 v[166:167], v245 offset:47104\n"
"s_lshl_b32 s1, s56, 4\n"
"v_mfma_f32_16x16x32_bf16 a[252:255], v[176:179], a[24:27], a[252:255]\n"
"s_waitcnt lgkmcnt(7)\n"
"s_barrier\n"
"v_mfma_f32_16x16x32_bf16 a[252:255], v[180:183], a[28:31], a[252:255]\n"
"ds_read_b64 v[184:185], v249 offset:12288\n"
"s_add_u32 s1, s1, s55\n"
"v_mfma_f32_16x16x32_bf16 v[80:83], v[152:155], a[0:3], v[80:83]\n"
"s_barrier\n"
"buffer_load_dwordx4 v239, s[20:23], 0 offen offset:1024 lds\n"
"v_mfma_f32_16x16x32_bf16 v[80:83], v[156:159], a[4:7], v[80:83]\n"
"ds_read_b64_tr_b16 v[168:169], v242 offset:0\n"
"s_lshl_b32 s1, s1, 2\n"
"v_mfma_f32_16x16x32_bf16 v[84:87], v[152:155], a[8:11], v[84:87]\n"
"ds_read_b64_tr_b16 v[170:171], v242 offset:4096\n"
"s_add_u32 s1, s1, s54\n"
"v_mfma_f32_16x16x32_bf16 v[84:87], v[156:159], a[12:15], v[84:87]\n"
"ds_read_b64 v[186:187], v249 offset:12800\n"
"s_lshl_b32 s1, s1, 7\n"
"v_mfma_f32_16x16x32_bf16 v[88:91], v[152:155], a[16:19], v[88:91]\n"
"ds_read_b64_tr_b16 v[172:173], v242 offset:8192\n"
"s_add_u32 s1, s1, 39059456\n"
"v_mfma_f32_16x16x32_bf16 v[88:91], v[156:159], a[20:23], v[88:91]\n"
"ds_read_b64_tr_b16 v[174:175], v242 offset:12288\n"
"buffer_load_dwordx4 v240, s[20:23], 0 offen offset:2048 lds\n"
"v_mfma_f32_16x16x32_bf16 v[92:95], v[152:155], a[24:27], v[92:95]\n"
"ds_read_b64 v[188:189], v249 offset:13312\n"
"v_mfma_f32_16x16x32_bf16 v[92:95], v[156:159], a[28:31], v[92:95]\n"
"ds_read_b64 v[190:191], v249 offset:13824\n"
"s_waitcnt lgkmcnt(8)\n"
"v_mfma_f32_16x16x32_bf16 v[128:131], v[160:163], a[0:3], v[128:131]\n"
"v_mfma_f32_16x16x32_bf16 v[128:131], v[164:167], a[4:7], v[128:131]\n"
"buffer_store_dwordx4 v[8:11], v236, s[36:39], s1 offen sc1\n"
"v_mfma_f32_16x16x32_bf16 v[132:135], v[160:163], a[8:11], v[132:135]\n"
"ds_read_b64_tr_b16 v[176:177], v243 offset:0\n"
"v_mfma_f32_16x16x32_bf16 v[132:135], v[164:167], a[12:15], v[132:135]\n"
"ds_read_b64_tr_b16 v[178:179], v243 offset:4096\n"
"v_mfma_f32_16x16x32_bf16 v[136:139], v[160:163], a[16:19], v[136:139]\n"
"s_waitcnt lgkmcnt(6)\n"
"v_add_f32 v184, v184, v186\n"
"v_mfma_f32_16x16x32_bf16 v[136:139], v[164:167], a[20:23], v[136:139]\n"
"ds_read_b64_tr_b16 v[180:181], v243 offset:8192\n"
"ds_read_b64_tr_b16 v[182:183], v243 offset:12288\n"
"v_mfma_f32_16x16x32_bf16 v[140:143], v[160:163], a[24:27], v[140:143]\n"
"buffer_load_dwordx4 v241, s[20:23], 0 offen offset:3072 lds\n"
"s_lshl_b32 s92, s76, 5\n"
"v_mfma_f32_16x16x32_bf16 v[140:143], v[164:167], a[28:31], v[140:143]\n"
"s_add_u32 s20, s40, s92\n"
"s_waitcnt lgkmcnt(6)\n"
"v_mfma_f32_16x16x32_bf16 a[160:163], v[168:171], a[32:35], a[160:163]\n"
"s_waitcnt lgkmcnt(4)\n"
"v_add_f32 v188, v188, v190\n"
"v_mfma_f32_16x16x32_bf16 a[160:163], v[172:175], a[36:39], a[160:163]\n"
"ds_read_b64_tr_b16 v[152:153], v242 offset:1024\n"
"s_add_u32 m0, s63, 65536\n"
"v_mfma_f32_16x16x32_bf16 a[164:167], v[168:171], a[40:43], a[164:167]\n"
"ds_read_b64_tr_b16 v[154:155], v242 offset:5120\n"
"buffer_load_dwordx4 a[128:131], v246, s[28:31], 0 offen offset:0 sc1\n"
"v_mfma_f32_16x16x32_bf16 a[164:167], v[172:175], a[44:47], a[164:167]\n"
"ds_read_b64_tr_b16 v[156:157], v242 offset:9216\n"
"v_add_f32 v185, v185, v187\n"
"v_mfma_f32_16x16x32_bf16 a[168:171], v[168:171], a[48:51], a[168:171]\n"
"ds_read_b64_tr_b16 v[158:159], v242 offset:13312\n"
"v_add_f32 v189, v189, v191\n"
"v_mfma_f32_16x16x32_bf16 a[168:171], v[172:175], a[52:55], a[168:171]\n"
"v_add_f32 v184, v184, v188\n"
"v_add_f32 v185, v185, v189\n"
"v_mfma_f32_16x16x32_bf16 a[172:175], v[168:171], a[56:59], a[172:175]\n"
"buffer_load_dwordx4 a[136:139], v246, s[28:31], 0 offen offset:1024 sc1\n"
"v_fmac_f32 v14, v2, v184\n"
"v_mfma_f32_16x16x32_bf16 a[172:175], v[172:175], a[60:63], a[172:175]\n"
"v_fmac_f32 v15, v2, v185\n"
"s_waitcnt lgkmcnt(4)\n"
"v_mfma_f32_16x16x32_bf16 a[176:179], v[176:179], a[32:35], a[176:179]\n"
"v_fma_f32 v14, -v3, v185, v14\n"
"v_fmac_f32 v15, v3, v184\n"
"v_mfma_f32_16x16x32_bf16 a[176:179], v[180:183], a[36:39], a[176:179]\n"
"ds_read_b64_tr_b16 v[160:161], v243 offset:1024\n"
"v_mfma_f32_16x16x32_bf16 a[180:183], v[176:179], a[40:43], a[180:183]\n"
"ds_read_b64_tr_b16 v[162:163], v243 offset:5120\n"
"buffer_load_dwordx4 a[132:135], v246, s[28:31], 0 offen offset:2048 sc1\n"
"v_mfma_f32_16x16x32_bf16 a[180:183], v[180:183], a[44:47], a[180:183]\n"
"ds_write_b64 v248, v[14:15]\n"
"v_mfma_f32_16x16x32_bf16 a[184:187], v[176:179], a[48:51], a[184:187]\n"
"ds_read_b64_tr_b16 v[164:165], v243 offset:9216\n"
"v_fmac_f32 v192, v212, v14\n"
"v_mfma_f32_16x16x32_bf16 a[184:187], v[180:183], a[52:55], a[184:187]\n"
"ds_read_b64_tr_b16 v[166:167], v243 offset:13312\n"
"v_fmac_f32 v193, v212, v15\n"
"v_mfma_f32_16x16x32_bf16 a[188:191], v[176:179], a[56:59], a[188:191]\n"
"buffer_load_dwordx4 a[140:143], v246, s[28:31], 0 offen offset:3072 sc1\n"
"v_fma_f32 v192, -v213, v15, v192\n"
"v_mfma_f32_16x16x32_bf16 a[188:191], v[180:183], a[60:63], a[188:191]\n"
"v_fmac_f32 v193, v213, v14\n"
"s_waitcnt lgkmcnt(5)\n"
"v_mfma_f32_16x16x32_bf16 a[192:195], v[152:155], a[32:35], a[192:195]\n"
"ds_write_b64 v248, v[192:193] offset:512\n"
"v_mfma_f32_16x16x32_bf16 a[192:195], v[156:159], a[36:39], a[192:195]\n"
"ds_read_b64_tr_b16 v[168:169], v242 offset:3072\n"
"v_fmac_f32 v194, v212, v192\n"
"v_mfma_f32_16x16x32_bf16 a[196:199], v[152:155], a[40:43], a[196:199]\n"
"ds_read_b64_tr_b16 v[170:171], v242 offset:7168\n"
"v_fmac_f32 v195, v212, v193\n"
"v_mfma_f32_16x16x32_bf16 a[196:199], v[156:159], a[44:47], a[196:199]\n"
"ds_read_b64_tr_b16 v[172:173], v242 offset:11264\n"
"buffer_load_dwordx4 a[144:147], v247, s[28:31], 0 offen offset:0 sc1\n"
"v_mfma_f32_16x16x32_bf16 a[200:203], v[152:155], a[48:51], a[200:203]\n"
"ds_read_b64_tr_b16 v[174:175], v242 offset:15360\n"
"v_fma_f32 v194, -v213, v193, v194\n"
"v_mfma_f32_16x16x32_bf16 a[200:203], v[156:159], a[52:55], a[200:203]\n"
"v_fmac_f32 v195, v213, v192\n"
"v_lshrrev_b32 v192, 2, v209\n"
"v_mfma_f32_16x16x32_bf16 a[204:207], v[152:155], a[56:59], a[204:207]\n"
"ds_write_b64 v248, v[194:195] offset:1024\n"
"v_mfma_f32_16x16x32_bf16 a[204:207], v[156:159], a[60:63], a[204:207]\n"
"v_fmac_f32 v196, v212, v194\n"
"s_waitcnt lgkmcnt(6)\n"
"v_mfma_f32_16x16x32_bf16 a[208:211], v[160:163], a[32:35], a[208:211]\n"
"buffer_load_dwordx4 a[152:155], v247, s[28:31], 0 offen offset:1024 sc1\n"
"v_fmac_f32 v197, v212, v195\n"
"v_mfma_f32_16x16x32_bf16 a[208:211], v[164:167], a[36:39], a[208:211]\n"
"ds_read_b64_tr_b16 v[176:177], v243 offset:3072\n"
"v_fma_f32 v196, -v213, v195, v196\n"
"v_mfma_f32_16x16x32_bf16 a[212:215], v[160:163], a[40:43], a[212:215]\n"
"ds_read_b64_tr_b16 v[178:179], v243 offset:7168\n"
"v_fmac_f32 v197, v213, v194\n"
"v_mfma_f32_16x16x32_bf16 a[212:215], v[164:167], a[44:47], a[212:215]\n"
"ds_read_b64_tr_b16 v[180:181], v243 offset:11264\n"
"v_cmp_eq_u32 s[94:95], v192, v210\n"
"v_mfma_f32_16x16x32_bf16 a[216:219], v[160:163], a[48:51], a[216:219]\n"
"ds_read_b64_tr_b16 v[182:183], v243 offset:15360\n"
"buffer_load_dwordx4 a[148:151], v247, s[28:31], 0 offen offset:2048 sc1\n"
"v_mfma_f32_16x16x32_bf16 a[216:219], v[164:167], a[52:55], a[216:219]\n"
"ds_write_b64 v248, v[196:197] offset:1536\n"
"v_mfma_f32_16x16x32_bf16 a[220:223], v[160:163], a[56:59], a[220:223]\n"
"ds_read_b64 v[184:185], v234 offset:0\n"
"v_and_b32 v193, 1, v209\n"
"v_mfma_f32_16x16x32_bf16 a[220:223], v[164:167], a[60:63], a[220:223]\n"
"ds_read_b64 v[186:187], v234 offset:128\n"
"s_waitcnt lgkmcnt(8)\n"
"v_mfma_f32_16x16x32_bf16 a[224:227], v[168:171], a[32:35], a[224:227]\n"
"ds_read_b64 v[188:189], v234 offset:256\n"
"v_lshlrev_b32 v193, 4, v193\n"
"v_mfma_f32_16x16x32_bf16 a[224:227], v[172:175], a[36:39], a[224:227]\n"
"ds_read_b64_tr_b16 v[152:153], v242 offset:2048\n"
"buffer_load_dwordx4 a[156:159], v247, s[28:31], 0 offen offset:3072 sc1\n"
"v_mfma_f32_16x16x32_bf16 a[228:231], v[168:171], a[40:43], a[228:231]\n"
"ds_read_b64_tr_b16 v[154:155], v242 offset:6144\n"
"s_mov_b32 s28, s76\n"
"v_mfma_f32_16x16x32_bf16 a[228:231], v[172:175], a[44:47], a[228:231]\n"
"ds_read_b64 v[190:191], v234 offset:384\n"
"v_bfe_u32 v208, v209, 1, 2\n"
"v_mfma_f32_16x16x32_bf16 a[232:235], v[168:171], a[48:51], a[232:235]\n"
"ds_read_b64_tr_b16 v[156:157], v242 offset:10240\n"
"v_xor_b32 v208, v208, v210\n"
"v_mfma_f32_16x16x32_bf16 a[232:235], v[172:175], a[52:55], a[232:235]\n"
"ds_read_b64_tr_b16 v[158:159], v242 offset:14336\n"
"v_lshlrev_b32 v208, 4, v208\n"
"v_mfma_f32_16x16x32_bf16 a[236:239], v[168:171], a[56:59], a[236:239]\n"
"buffer_load_dwordx4 v238, s[20:23], 0 offen offset:0 lds\n"
"v_lshl_add_u32 v208, v209, 6, v208\n"
"v_mfma_f32_16x16x32_bf16 a[236:239], v[172:175], a[60:63], a[236:239]\n"
"s_add_u32 s88, s63, 114688\n"
"s_waitcnt lgkmcnt(9)\n"
"v_mfma_f32_16x16x32_bf16 a[240:243], v[176:179], a[32:35], a[240:243]\n"
"v_add_u32 v248, s88, v208\n"
"v_bfe_u32 v208, v1, 2, 2\n"
"v_mfma_f32_16x16x32_bf16 a[240:243], v[180:183], a[36:39], a[240:243]\n"
"ds_read_b64_tr_b16 v[160:161], v243 offset:2048\n"
"v_lshrrev_b32 v6, 1, v208\n"
"v_mfma_f32_16x16x32_bf16 a[244:247], v[176:179], a[40:43], a[244:247]\n"
"ds_read_b64_tr_b16 v[162:163], v243 offset:6144\n"
"v_xor_b32 v6, v6, v1\n"
"v_mfma_f32_16x16x32_bf16 a[244:247], v[180:183], a[44:47], a[244:247]\n"
"ds_read_b128 v[212:215], v235 offset:0\n"
"buffer_load_dwordx4 v239, s[20:23], 0 offen offset:1024 lds\n"
"v_mfma_f32_16x16x32_bf16 a[248:251], v[176:179], a[48:51], a[248:251]\n"
"ds_read_b64_tr_b16 v[164:165], v243 offset:10240\n"
"v_and_b32 v6, 3, v6\n"
"v_mfma_f32_16x16x32_bf16 a[248:251], v[180:183], a[52:55], a[248:251]\n"
"ds_read_b64_tr_b16 v[166:167], v243 offset:14336\n"
"v_lshl_add_u32 v208, v210, 3, v208\n"
"v_mfma_f32_16x16x32_bf16 a[252:255], v[176:179], a[56:59], a[252:255]\n"
"ds_read_b128 v[2:5], v235 offset:256\n"
"v_lshlrev_b32 v208, 6, v208\n"
"v_mfma_f32_16x16x32_bf16 a[252:255], v[180:183], a[60:63], a[252:255]\n"
"buffer_load_dwordx4 v240, s[20:23], 0 offen offset:2048 lds\n"
"s_waitcnt lgkmcnt(6)\n"
"v_mfma_f32_16x16x32_bf16 v[80:83], v[152:155], a[32:35], v[80:83]\n"
"s_barrier\n"
"v_lshl_add_u32 v208, v6, 4, v208\n"
"v_mfma_f32_16x16x32_bf16 v[80:83], v[156:159], a[36:39], v[80:83]\n"
"ds_read_b64_tr_b16 v[168:169], v242 offset:16384\n"
"v_add_u32 v249, s88, v208\n"
"v_mfma_f32_16x16x32_bf16 v[84:87], v[152:155], a[40:43], v[84:87]\n"
"ds_read_b64_tr_b16 v[170:171], v242 offset:20480\n"
"s_add_u32 s88, s57, 1\n"
"v_mfma_f32_16x16x32_bf16 v[84:87], v[156:159], a[44:47], v[84:87]\n"
"ds_read_b64_tr_b16 v[172:173], v242 offset:24576\n"
"s_and_b32 s88, s88, 3\n"
"v_mfma_f32_16x16x32_bf16 v[88:91], v[152:155], a[48:51], v[88:91]\n"
"s_waitcnt lgkmcnt(6)\n"
"v_mul_f32 v14, v214, v184\n"
"v_mfma_f32_16x16x32_bf16 v[88:91], v[156:159], a[52:55], v[88:91]\n"
"ds_read_b64_tr_b16 v[174:175], v242 offset:28672\n"
"buffer_load_dwordx4 v241, s[20:23], 0 offen offset:3072 lds\n"
"v_mfma_f32_16x16x32_bf16 v[92:95], v[152:155], a[56:59], v[92:95]\n"
"s_lshl_b32 s92, s77, 5\n"
"s_add_u32 s20, s40, s92\n"
"v_mfma_f32_16x16x32_bf16 v[92:95], v[156:159], a[60:63], v[92:95]\n"
"s_add_u32 m0, s63, 81920\n"
"s_waitcnt lgkmcnt(5)\n"
"v_mfma_f32_16x16x32_bf16 v[128:131], v[160:163], a[32:35], v[128:131]\n"
"v_mul_f32 v15, v214, v185\n"
"v_fma_f32 v14, -v215, v185, v14\n"
"v_mfma_f32_16x16x32_bf16 v[128:131], v[164:167], a[36:39], v[128:131]\n"
"ds_read_b64_tr_b16 v[176:177], v243 offset:16384\n"
"buffer_load_dwordx4 v[96:99], v246, s[28:31], 0 offen offset:0 sc1\n"
"v_mfma_f32_16x16x32_bf16 v[132:135], v[160:163], a[40:43], v[132:135]\n"
"ds_read_b64_tr_b16 v[178:179], v243 offset:20480\n"
"v_fmac_f32 v15, v215, v184\n"
"v_mfma_f32_16x16x32_bf16 v[132:135], v[164:167], a[44:47], v[132:135]\n"
"ds_read_b64_tr_b16 v[180:181], v243 offset:24576\n"
"v_fmac_f32 v16, v212, v14\n"
"v_mfma_f32_16x16x32_bf16 v[136:139], v[160:163], a[48:51], v[136:139]\n"
"ds_read_b64_tr_b16 v[182:183], v243 offset:28672\n"
"v_fmac_f32 v24, v212, v15\n"
"v_mfma_f32_16x16x32_bf16 v[136:139], v[164:167], a[52:55], v[136:139]\n"
"buffer_load_dwordx4 v[104:107], v246, s[28:31], 0 offen offset:1024 sc1\n"
"v_fma_f32 v16, -v213, v15, v16\n"
"v_mfma_f32_16x16x32_bf16 v[140:143], v[160:163], a[56:59], v[140:143]\n"
"v_fmac_f32 v24, v213, v14\n"
"v_fmac_f32 v17, v212, v16\n"
"v_mfma_f32_16x16x32_bf16 v[140:143], v[164:167], a[60:63], v[140:143]\n"
"v_fmac_f32 v25, v212, v24\n"
"s_waitcnt vmcnt(32) lgkmcnt(4)\n"
"v_mfma_f32_16x16x32_bf16 a[160:163], v[168:171], a[64:67], a[160:163]\n"
"v_fma_f32 v17, -v213, v24, v17\n"
"v_fmac_f32 v25, v213, v16\n"
"v_mfma_f32_16x16x32_bf16 a[160:163], v[172:175], a[68:71], a[160:163]\n"
"ds_read_b64_tr_b16 v[152:153], v242 offset:17408\n"
"v_fmac_f32 v18, v212, v17\n"
"v_mfma_f32_16x16x32_bf16 a[164:167], v[168:171], a[72:75], a[164:167]\n"
"ds_read_b64_tr_b16 v[154:155], v242 offset:21504\n"
"buffer_load_dwordx4 v[100:103], v246, s[28:31], 0 offen offset:2048 sc1\n"
"v_mfma_f32_16x16x32_bf16 a[164:167], v[172:175], a[76:79], a[164:167]\n"
"ds_read_b64_tr_b16 v[156:157], v242 offset:25600\n"
"v_fmac_f32 v26, v212, v25\n"
"v_mfma_f32_16x16x32_bf16 a[168:171], v[168:171], a[80:83], a[168:171]\n"
"ds_read_b64_tr_b16 v[158:159], v242 offset:29696\n"
"v_fma_f32 v18, -v213, v25, v18\n"
"v_mfma_f32_16x16x32_bf16 a[168:171], v[172:175], a[84:87], a[168:171]\n"
"v_fmac_f32 v26, v213, v17\n"
"v_fmac_f32 v19, v212, v18\n"
"v_mfma_f32_16x16x32_bf16 a[172:175], v[168:171], a[88:91], a[172:175]\n"
"v_fmac_f32 v27, v212, v26\n"
"v_fma_f32 v19, -v213, v26, v19\n"
"v_mfma_f32_16x16x32_bf16 a[172:175], v[172:175], a[92:95], a[172:175]\n"
"buffer_load_dwordx4 v[108:111], v246, s[28:31], 0 offen offset:3072 sc1\n"
"s_waitcnt lgkmcnt(4)\n"
"v_mfma_f32_16x16x32_bf16 a[176:179], v[176:179], a[64:67], a[176:179]\n"
"v_fmac_f32 v27, v213, v18\n"
"v_fmac_f32 v20, v212, v19\n"
"v_mfma_f32_16x16x32_bf16 a[176:179], v[180:183], a[68:71], a[176:179]\n"
"ds_read_b64_tr_b16 v[160:161], v243 offset:17408\n"
"v_fmac_f32 v28, v212, v27\n"
"v_mfma_f32_16x16x32_bf16 a[180:183], v[176:179], a[72:75], a[180:183]\n"
"ds_read_b64_tr_b16 v[162:163], v243 offset:21504\n"
"v_fma_f32 v20, -v213, v27, v20\n"
"v_mfma_f32_16x16x32_bf16 a[180:183], v[180:183], a[76:79], a[180:183]\n"
"ds_read_b64_tr_b16 v[164:165], v243 offset:25600\n"
"buffer_load_dwordx4 v[112:115], v247, s[28:31], 0 offen offset:0 sc1\n"
"v_mfma_f32_16x16x32_bf16 a[184:187], v[176:179], a[80:83], a[184:187]\n"
"ds_read_b64_tr_b16 v[166:167], v243 offset:29696\n"
"v_fmac_f32 v28, v213, v19\n"
"v_mfma_f32_16x16x32_bf16 a[184:187], v[180:183], a[84:87], a[184:187]\n"
"v_fmac_f32 v21, v212, v20\n"
"v_fmac_f32 v29, v212, v28\n"
"v_mfma_f32_16x16x32_bf16 a[188:191], v[176:179], a[88:91], a[188:191]\n"
"v_fma_f32 v21, -v213, v28, v21\n"
"v_fmac_f32 v29, v213, v20\n"
"v_mfma_f32_16x16x32_bf16 a[188:191], v[180:183], a[92:95], a[188:191]\n"
"v_fmac_f32 v22, v212, v21\n"
"s_waitcnt lgkmcnt(4)\n"
"v_mfma_f32_16x16x32_bf16 a[192:195], v[152:155], a[64:67], a[192:195]\n"
"buffer_load_dwordx4 v[120:123], v247, s[28:31], 0 offen offset:1024 sc1\n"
"v_fmac_f32 v30, v212, v29\n"
".Lk_h8:\n"
"s_cbranch_execz .Lk_h12\n"
"v_mfma_f32_16x16x32_bf16 a[192:195], v[156:159], a[68:71], a[192:195]\n"
"ds_read_b64_tr_b16 v[168:169], v242 offset:19456\n"
"v_fma_f32 v22, -v213, v29, v22\n"
"v_mfma_f32_16x16x32_bf16 a[196:199], v[152:155], a[72:75], a[196:199]\n"
"ds_read_b64_tr_b16 v[170:171], v242 offset:23552\n"
"v_fmac_f32 v30, v213, v21\n"
"v_mfma_f32_16x16x32_bf16 a[196:199], v[156:159], a[76:79], a[196:199]\n"
"ds_read_b64_tr_b16 v[172:173], v242 offset:27648\n"
"v_fmac_f32 v23, v212, v22\n"
"v_mfma_f32_16x16x32_bf16 a[200:203], v[152:155], a[80:83], a[200:203]\n"
"ds_read_b64_tr_b16 v[174:175], v242 offset:31744\n"
"v_fmac_f32 v31, v212, v30\n"
"v_mfma_f32_16x16x32_bf16 a[200:203], v[156:159], a[84:87], a[200:203]\n"
"buffer_load_dwordx4 v[116:119], v247, s[28:31], 0 offen offset:2048 sc1\n"
"v_fma_f32 v23, -v213, v30, v23\n"
"v_mfma_f32_16x16x32_bf16 a[204:207], v[152:155], a[88:91], a[204:207]\n"
"v_fmac_f32 v31, v213, v22\n"
"v_xor_b32 v218, 32, v249\n"
"v_mfma_f32_16x16x32_bf16 a[204:207], v[156:159], a[92:95], a[204:207]\n"
"ds_read_b128 v[212:215], v235 offset:512\n"
"s_waitcnt lgkmcnt(5)\n"
"v_mfma_f32_16x16x32_bf16 a[208:211], v[160:163], a[64:67], a[208:211]\n"
"v_mul_f32 v14, v4, v186\n"
"v_mul_f32 v15, v4, v187\n"
"v_mfma_f32_16x16x32_bf16 a[208:211], v[164:167], a[68:71], a[208:211]\n"
"ds_read_b64_tr_b16 v[176:177], v243 offset:19456\n"
"v_fma_f32 v14, -v5, v187, v14\n"
"v_mfma_f32_16x16x32_bf16 a[212:215], v[160:163], a[72:75], a[212:215]\n"
"ds_read_b64_tr_b16 v[178:179], v243 offset:23552\n"
"buffer_load_dwordx4 v[124:127], v247, s[28:31], 0 offen offset:3072 sc1\n"
"v_mfma_f32_16x16x32_bf16 a[212:215], v[164:167], a[76:79], a[212:215]\n"
"ds_read_b64_tr_b16 v[180:181], v243 offset:27648\n"
"s_mov_b32 s28, s77\n"
"v_mfma_f32_16x16x32_bf16 a[216:219], v[160:163], a[80:83], a[216:219]\n"
"ds_read_b64_tr_b16 v[182:183], v243 offset:31744\n"
"v_fmac_f32 v15, v5, v186\n"
"v_mfma_f32_16x16x32_bf16 a[216:219], v[164:167], a[84:87], a[216:219]\n"
"v_fmac_f32 v32, v2, v14\n"
"v_fmac_f32 v40, v2, v15\n"
"v_mfma_f32_16x16x32_bf16 a[220:223], v[160:163], a[88:91], a[220:223]\n"
"buffer_load_dwordx4 v238, s[20:23], 0 offen offset:0 lds\n"
"v_fma_f32 v32, -v3, v15, v32\n"
"v_mfma_f32_16x16x32_bf16 a[220:223], v[164:167], a[92:95], a[220:223]\n"
"v_fmac_f32 v40, v3, v14\n"
"s_waitcnt lgkmcnt(5)\n"
"v_mfma_f32_16x16x32_bf16 a[224:227], v[168:171], a[64:67], a[224:227]\n"
"v_fmac_f32 v33, v2, v32\n"
"v_fmac_f32 v41, v2, v40\n"
"v_mfma_f32_16x16x32_bf16 a[224:227], v[172:175], a[68:71], a[224:227]\n"
"ds_read_b64_tr_b16 v[152:153], v242 offset:18432\n"
"v_fma_f32 v33, -v3, v40, v33\n"
"v_mfma_f32_16x16x32_bf16 a[228:231], v[168:171], a[72:75], a[228:231]\n"
"ds_read_b64_tr_b16 v[154:155], v242 offset:22528\n"
"v_fmac_f32 v41, v3, v32\n"
"v_mfma_f32_16x16x32_bf16 a[228:231], v[172:175], a[76:79], a[228:231]\n"
"ds_read_b64_tr_b16 v[156:157], v242 offset:26624\n"
"buffer_load_dwordx4 v239, s[20:23], 0 offen offset:1024 lds\n"
"v_mfma_f32_16x16x32_bf16 a[232:235], v[168:171], a[80:83], a[232:235]\n"
"ds_read_b64_tr_b16 v[158:159], v242 offset:30720\n"
"v_fmac_f32 v34, v2, v33\n"
"v_mfma_f32_16x16x32_bf16 a[232:235], v[172:175], a[84:87], a[232:235]\n"
"v_fmac_f32 v42, v2, v41\n"
"v_fma_f32 v34, -v3, v41, v34\n"
"v_mfma_f32_16x16x32_bf16 a[236:239], v[168:171], a[88:91], a[236:239]\n"
"v_fmac_f32 v42, v3, v33\n"
"v_fmac_f32 v35, v2, v34\n"
"v_mfma_f32_16x16x32_bf16 a[236:239], v[172:175], a[92:95], a[236:239]\n"
"v_fmac_f32 v43, v2, v42\n"
"s_waitcnt lgkmcnt(4)\n"
"v_mfma_f32_16x16x32_bf16 a[240:243], v[176:179], a[64:67], a[240:243]\n"
"buffer_load_dwordx4 v240, s[20:23], 0 offen offset:2048 lds\n"
"v_fma_f32 v35, -v3, v42, v35\n"
"v_mfma_f32_16x16x32_bf16 a[240:243], v[180:183], a[68:71], a[240:243]\n"
"ds_read_b64_tr_b16 v[160:161], v243 offset:18432\n"
"v_fmac_f32 v43, v3, v34\n"
"v_mfma_f32_16x16x32_bf16 a[244:247], v[176:179], a[72:75], a[244:247]\n"
"ds_read_b64_tr_b16 v[162:163], v243 offset:22528\n"
"v_fmac_f32 v36, v2, v35\n"
"v_mfma_f32_16x16x32_bf16 a[244:247], v[180:183], a[76:79], a[244:247]\n"
"ds_read_b64_tr_b16 v[164:165], v243 offset:26624\n"
"v_fmac_f32 v44, v2, v43\n"
"v_mfma_f32_16x16x32_bf16 a[248:251], v[176:179], a[80:83], a[248:251]\n"
"ds_read_b64_tr_b16 v[166:167], v243 offset:30720\n"
"buffer_load_dwordx4 v241, s[20:23], 0 offen offset:3072 lds\n"
"v_mfma_f32_16x16x32_bf16 a[248:251], v[180:183], a[84:87], a[248:251]\n"
"s_lshl_b32 s92, s78, 5\n"
"s_add_u32 s20, s40, s92\n"
"v_mfma_f32_16x16x32_bf16 a[252:255], v[176:179], a[88:91], a[252:255]\n"
"s_add_u32 m0, s63, 98304\n"
"v_fma_f32 v36, -v3, v43, v36\n"
"v_mfma_f32_16x16x32_bf16 a[252:255], v[180:183], a[92:95], a[252:255]\n"
"v_fmac_f32 v44, v3, v35\n"
"s_waitcnt lgkmcnt(4)\n"
"v_mfma_f32_16x16x32_bf16 v[80:83], v[152:155], a[64:67], v[80:83]\n"
"s_waitcnt vmcnt(38)\n"
"s_barrier\n"
"v_mfma_f32_16x16x32_bf16 v[80:83], v[156:159], a[68:71], v[80:83]\n"
"ds_read_b64_tr_b16 v[168:169], v242 offset:32768\n"
"buffer_load_dwordx4 a[0:3], v246, s[28:31], 0 offen offset:0 sc1\n"
"v_mfma_f32_16x16x32_bf16 v[84:87], v[152:155], a[72:75], v[84:87]\n"
"ds_read_b64_tr_b16 v[170:171], v242 offset:36864\n"
"v_fmac_f32 v37, v2, v36\n"
"v_mfma_f32_16x16x32_bf16 v[84:87], v[156:159], a[76:79], v[84:87]\n"
"ds_read_b64_tr_b16 v[172:173], v242 offset:40960\n"
"v_fmac_f32 v45, v2, v44\n"
"v_mfma_f32_16x16x32_bf16 v[88:91], v[152:155], a[80:83], v[88:91]\n"
"ds_read_b64_tr_b16 v[174:175], v242 offset:45056\n"
"v_fma_f32 v37, -v3, v44, v37\n"
"v_mfma_f32_16x16x32_bf16 v[88:91], v[156:159], a[84:87], v[88:91]\n"
"v_fmac_f32 v45, v3, v36\n"
"v_fmac_f32 v38, v2, v37\n"
"v_mfma_f32_16x16x32_bf16 v[92:95], v[152:155], a[88:91], v[92:95]\n"
"buffer_load_dwordx4 a[8:11], v246, s[28:31], 0 offen offset:1024 sc1\n"
"v_fmac_f32 v46, v2, v45\n"
"v_mfma_f32_16x16x32_bf16 v[92:95], v[156:159], a[92:95], v[92:95]\n"
"v_fma_f32 v38, -v3, v45, v38\n"
"s_waitcnt lgkmcnt(4)\n"
"v_mfma_f32_16x16x32_bf16 v[128:131], v[160:163], a[64:67], v[128:131]\n"
"v_fmac_f32 v46, v3, v37\n"
"v_fmac_f32 v39, v2, v38\n"
"v_mfma_f32_16x16x32_bf16 v[128:131], v[164:167], a[68:71], v[128:131]\n"
"ds_read_b64_tr_b16 v[176:177], v243 offset:32768\n"
"v_fmac_f32 v47, v2, v46\n"
"v_mfma_f32_16x16x32_bf16 v[132:135], v[160:163], a[72:75], v[132:135]\n"
"ds_read_b64_tr_b16 v[178:179], v243 offset:36864\n"
"buffer_load_dwordx4 a[4:7], v246, s[28:31], 0 offen offset:2048 sc1\n"
"v_mfma_f32_16x16x32_bf16 v[132:135], v[164:167], a[76:79], v[132:135]\n"
"ds_read_b64_tr_b16 v[180:181], v243 offset:40960\n"
"v_fma_f32 v39, -v3, v46, v39\n"
"v_mfma_f32_16x16x32_bf16 v[136:139], v[160:163], a[80:83], v[136:139]\n"
"ds_read_b64_tr_b16 v[182:183], v243 offset:45056\n"
"v_fmac_f32 v47, v3, v38\n"
"v_mfma_f32_16x16x32_bf16 v[136:139], v[164:167], a[84:87], v[136:139]\n"
"ds_read_b128 v[2:5], v235 offset:768\n"
"v_mul_f32 v14, v214, v188\n"
"v_mfma_f32_16x16x32_bf16 v[140:143], v[160:163], a[88:91], v[140:143]\n"
"v_mul_f32 v15, v214, v189\n"
"v_fma_f32 v14, -v215, v189, v14\n"
"v_mfma_f32_16x16x32_bf16 v[140:143], v[164:167], a[92:95], v[140:143]\n"
"buffer_load_dwordx4 a[12:15], v246, s[28:31], 0 offen offset:3072 sc1\n"
"s_waitcnt vmcnt(33) lgkmcnt(5)\n"
"v_mfma_f32_16x16x32_bf16 a[160:163], v[168:171], a[96:99], a[160:163]\n"
"v_fmac_f32 v15, v215, v188\n"
"v_fmac_f32 v48, v212, v14\n"
"v_mfma_f32_16x16x32_bf16 a[160:163], v[172:175], a[100:103], a[160:163]\n"
"ds_read_b64_tr_b16 v[152:153], v242 offset:33792\n"
"v_fmac_f32 v56, v212, v15\n"
"v_mfma_f32_16x16x32_bf16 a[164:167], v[168:171], a[104:107], a[164:167]\n"
"ds_read_b64_tr_b16 v[154:155], v242 offset:37888\n"
"v_fma_f32 v48, -v213, v15, v48\n"
"v_mfma_f32_16x16x32_bf16 a[164:167], v[172:175], a[108:111], a[164:167]\n"
"ds_read_b64_tr_b16 v[156:157], v242 offset:41984\n"
"v_fmac_f32 v56, v213, v14\n"
"v_mfma_f32_16x16x32_bf16 a[168:171], v[168:171], a[112:115], a[168:171]\n"
"ds_read_b64_tr_b16 v[158:159], v242 offset:46080\n"
"buffer_load_dwordx4 a[16:19], v247, s[28:31], 0 offen offset:0 sc1\n"
"v_mfma_f32_16x16x32_bf16 a[168:171], v[172:175], a[116:119], a[168:171]\n"
"v_fmac_f32 v49, v212, v48\n"
"v_fmac_f32 v57, v212, v56\n"
"v_mfma_f32_16x16x32_bf16 a[172:175], v[168:171], a[120:123], a[172:175]\n"
"v_fma_f32 v49, -v213, v56, v49\n"
"v_fmac_f32 v57, v213, v48\n"
"v_mfma_f32_16x16x32_bf16 a[172:175], v[172:175], a[124:127], a[172:175]\n"
"v_fmac_f32 v50, v212, v49\n"
"s_waitcnt lgkmcnt(5)\n"
"v_mfma_f32_16x16x32_bf16 a[176:179], v[176:179], a[96:99], a[176:179]\n"
"v_fmac_f32 v58, v212, v57\n"
"v_fma_f32 v50, -v213, v57, v50\n"
"v_mfma_f32_16x16x32_bf16 a[176:179], v[180:183], a[100:103], a[176:179]\n"
"ds_read_b64_tr_b16 v[160:161], v243 offset:33792\n"
"buffer_load_dwordx4 a[24:27], v247, s[28:31], 0 offen offset:1024 sc1\n"
"v_mfma_f32_16x16x32_bf16 a[180:183], v[176:179], a[104:107], a[180:183]\n"
"ds_read_b64_tr_b16 v[162:163], v243 offset:37888\n"
"v_fmac_f32 v58, v213, v49\n"
"v_mfma_f32_16x16x32_bf16 a[180:183], v[180:183], a[108:111], a[180:183]\n"
"ds_read_b64_tr_b16 v[164:165], v243 offset:41984\n"
"v_fmac_f32 v51, v212, v50\n"
"v_mfma_f32_16x16x32_bf16 a[184:187], v[176:179], a[112:115], a[184:187]\n"
"ds_read_b64_tr_b16 v[166:167], v243 offset:46080\n"
"v_fmac_f32 v59, v212, v58\n"
"v_mfma_f32_16x16x32_bf16 a[184:187], v[180:183], a[116:119], a[184:187]\n"
"buffer_load_dwordx4 a[20:23], v247, s[28:31], 0 offen offset:2048 sc1\n"
"v_fma_f32 v51, -v213, v58, v51\n"
"v_mfma_f32_16x16x32_bf16 a[188:191], v[176:179], a[120:123], a[188:191]\n"
"v_fmac_f32 v59, v213, v50\n"
"v_fmac_f32 v52, v212, v51\n"
"v_mfma_f32_16x16x32_bf16 a[188:191], v[180:183], a[124:127], a[188:191]\n"
"v_fmac_f32 v60, v212, v59\n"
"s_waitcnt lgkmcnt(4)\n"
"v_mfma_f32_16x16x32_bf16 a[192:195], v[152:155], a[96:99], a[192:195]\n"
"v_fma_f32 v52, -v213, v59, v52\n"
"v_fmac_f32 v60, v213, v51\n"
"v_mfma_f32_16x16x32_bf16 a[192:195], v[156:159], a[100:103], a[192:195]\n"
"ds_read_b64_tr_b16 v[168:169], v242 offset:35840\n"
"v_fmac_f32 v53, v212, v52\n"
"v_mfma_f32_16x16x32_bf16 a[196:199], v[152:155], a[104:107], a[196:199]\n"
"ds_read_b64_tr_b16 v[170:171], v242 offset:39936\n"
"buffer_load_dwordx4 a[28:31], v247, s[28:31], 0 offen offset:3072 sc1\n"
"v_mfma_f32_16x16x32_bf16 a[196:199], v[156:159], a[108:111], a[196:199]\n"
"ds_read_b64_tr_b16 v[172:173], v242 offset:44032\n"
"s_mov_b32 s28, s78\n"
"v_mfma_f32_16x16x32_bf16 a[200:203], v[152:155], a[112:115], a[200:203]\n"
"ds_read_b64_tr_b16 v[174:175], v242 offset:48128\n"
"v_fmac_f32 v61, v212, v60\n"
"v_mfma_f32_16x16x32_bf16 a[200:203], v[156:159], a[116:119], a[200:203]\n"
"v_fma_f32 v53, -v213, v60, v53\n"
"v_fmac_f32 v61, v213, v52\n"
"v_mfma_f32_16x16x32_bf16 a[204:207], v[152:155], a[120:123], a[204:207]\n"
"v_fmac_f32 v54, v212, v53\n"
"v_fmac_f32 v62, v212, v61\n"
"v_mfma_f32_16x16x32_bf16 a[204:207], v[156:159], a[124:127], a[204:207]\n"
"buffer_load_dwordx4 v238, s[20:23], 0 offen offset:0 lds\n"
"s_waitcnt lgkmcnt(4)\n"
"v_mfma_f32_16x16x32_bf16 a[208:211], v[160:163], a[96:99], a[208:211]\n"
"v_fma_f32 v54, -v213, v61, v54\n"
"v_fmac_f32 v62, v213, v53\n"
"v_mfma_f32_16x16x32_bf16 a[208:211], v[164:167], a[100:103], a[208:211]\n"
"ds_read_b64_tr_b16 v[176:177], v243 offset:35840\n"
"v_fmac_f32 v55, v212, v54\n"
"v_mfma_f32_16x16x32_bf16 a[212:215], v[160:163], a[104:107], a[212:215]\n"
"ds_read_b64_tr_b16 v[178:179], v243 offset:39936\n"
"v_fmac_f32 v63, v212, v62\n"
"v_mfma_f32_16x16x32_bf16 a[212:215], v[164:167], a[108:111], a[212:215]\n"
"ds_read_b64_tr_b16 v[180:181], v243 offset:44032\n"
"buffer_load_dwordx4 v239, s[20:23], 0 offen offset:1024 lds\n"
"v_mfma_f32_16x16x32_bf16 a[216:219], v[160:163], a[112:115], a[216:219]\n"
"ds_read_b64_tr_b16 v[182:183], v243 offset:48128\n"
"v_fma_f32 v55, -v213, v62, v55\n"
"v_mfma_f32_16x16x32_bf16 a[216:219], v[164:167], a[116:119], a[216:219]\n"
"v_fmac_f32 v63, v213, v54\n"
"v_mul_f32 v14, v4, v190\n"
"v_mfma_f32_16x16x32_bf16 a[220:223], v[160:163], a[120:123], a[220:223]\n"
"v_mul_f32 v15, v4, v191\n"
"v_fma_f32 v14, -v5, v191, v14\n"
"v_mfma_f32_16x16x32_bf16 a[220:223], v[164:167], a[124:127], a[220:223]\n"
"v_fmac_f32 v15, v5, v190\n"
"s_waitcnt lgkmcnt(4)\n"
"v_mfma_f32_16x16x32_bf16 a[224:227], v[168:171], a[96:99], a[224:227]\n"
"buffer_load_dwordx4 v240, s[20:23], 0 offen offset:2048 lds\n"
"v_fmac_f32 v64, v2, v14\n"
"v_mfma_f32_16x16x32_bf16 a[224:227], v[172:175], a[100:103], a[224:227]\n"
"ds_read_b64_tr_b16 v[152:153], v242 offset:34816\n"
"v_fmac_f32 v72, v2, v15\n"
"v_mfma_f32_16x16x32_bf16 a[228:231], v[168:171], a[104:107], a[228:231]\n"
"ds_read_b64_tr_b16 v[154:155], v242 offset:38912\n"
"v_fma_f32 v64, -v3, v15, v64\n"
"v_mfma_f32_16x16x32_bf16 a[228:231], v[172:175], a[108:111], a[228:231]\n"
"ds_read_b64_tr_b16 v[156:157], v242 offset:43008\n"
"v_fmac_f32 v72, v3, v14\n"
"v_mfma_f32_16x16x32_bf16 a[232:235], v[168:171], a[112:115], a[232:235]\n"
"ds_read_b64_tr_b16 v[158:159], v242 offset:47104\n"
"v_fmac_f32 v65, v2, v64\n"
"v_mfma_f32_16x16x32_bf16 a[232:235], v[172:175], a[116:119], a[232:235]\n"
"buffer_load_dwordx4 v241, s[20:23], 0 offen offset:3072 lds\n"
"s_lshl_b32 s92, s79, 5\n"
"v_mfma_f32_16x16x32_bf16 a[236:239], v[168:171], a[120:123], a[236:239]\n"
"s_add_u32 s20, s40, s92\n"
"s_add_u32 m0, s63, 0\n"
"v_mfma_f32_16x16x32_bf16 a[236:239], v[172:175], a[124:127], a[236:239]\n"
"v_fmac_f32 v73, v2, v72\n"
"s_waitcnt lgkmcnt(4)\n"
"v_mfma_f32_16x16x32_bf16 a[240:243], v[176:179], a[96:99], a[240:243]\n"
"v_fma_f32 v65, -v3, v72, v65\n"
"v_fmac_f32 v73, v3, v64\n"
"v_mfma_f32_16x16x32_bf16 a[240:243], v[180:183], a[100:103], a[240:243]\n"
"ds_read_b64_tr_b16 v[160:161], v243 offset:34816\n"
"buffer_load_dwordx4 a[32:35], v246, s[28:31], 0 offen offset:0 sc1\n"
"v_mfma_f32_16x16x32_bf16 a[244:247], v[176:179], a[104:107], a[244:247]\n"
"ds_read_b64_tr_b16 v[162:163], v243 offset:38912\n"
"v_fmac_f32 v66, v2, v65\n"
"v_mfma_f32_16x16x32_bf16 a[244:247], v[180:183], a[108:111], a[244:247]\n"
"ds_read_b64_tr_b16 v[164:165], v243 offset:43008\n"
"v_fmac_f32 v74, v2, v73\n"
"v_mfma_f32_16x16x32_bf16 a[248:251], v[176:179], a[112:115], a[248:251]\n"
"ds_read_b64_tr_b16 v[166:167], v243 offset:47104\n"
"v_fma_f32 v66, -v3, v73, v66\n"
"v_mfma_f32_16x16x32_bf16 a[248:251], v[180:183], a[116:119], a[248:251]\n"
"v_fmac_f32 v74, v3, v65\n"
"v_fmac_f32 v67, v2, v66\n"
"v_mfma_f32_16x16x32_bf16 a[252:255], v[176:179], a[120:123], a[252:255]\n"
"buffer_load_dwordx4 a[40:43], v246, s[28:31], 0 offen offset:1024 sc1\n"
"v_fmac_f32 v75, v2, v74\n"
"v_mfma_f32_16x16x32_bf16 a[252:255], v[180:183], a[124:127], a[252:255]\n"
"v_fma_f32 v67, -v3, v74, v67\n"
"s_waitcnt lgkmcnt(4)\n"
"v_mfma_f32_16x16x32_bf16 v[80:83], v[152:155], a[96:99], v[80:83]\n"
"s_waitcnt vmcnt(38)\n"
"s_barrier\n"
"v_mfma_f32_16x16x32_bf16 v[80:83], v[156:159], a[100:103], v[80:83]\n"
"ds_read_b64_tr_b16 v[168:169], v242 offset:49152\n"
"v_fmac_f32 v75, v3, v66\n"
"v_mfma_f32_16x16x32_bf16 v[84:87], v[152:155], a[104:107], v[84:87]\n"
"ds_read_b64_tr_b16 v[170:171], v242 offset:53248\n"
"v_fmac_f32 v68, v2, v67\n"
"v_mfma_f32_16x16x32_bf16 v[84:87], v[156:159], a[108:111], v[84:87]\n"
"ds_read_b64_tr_b16 v[172:173], v242 offset:57344\n"
"buffer_load_dwordx4 a[36:39], v246, s[28:31], 0 offen offset:2048 sc1\n"
"v_mfma_f32_16x16x32_bf16 v[88:91], v[152:155], a[112:115], v[88:91]\n"
"ds_read_b64_tr_b16 v[174:175], v242 offset:61440\n"
"v_fmac_f32 v76, v2, v75\n"
"v_mfma_f32_16x16x32_bf16 v[88:91], v[156:159], a[116:119], v[88:91]\n"
"v_fma_f32 v68, -v3, v75, v68\n"
"v_fmac_f32 v76, v3, v67\n"
"v_mfma_f32_16x16x32_bf16 v[92:95], v[152:155], a[120:123], v[92:95]\n"
"v_fmac_f32 v69, v2, v68\n"
"v_fmac_f32 v77, v2, v76\n"
"v_mfma_f32_16x16x32_bf16 v[92:95], v[156:159], a[124:127], v[92:95]\n"
"v_fma_f32 v69, -v3, v76, v69\n"
"s_waitcnt lgkmcnt(4)\n"
"v_mfma_f32_16x16x32_bf16 v[128:131], v[160:163], a[96:99], v[128:131]\n"
"buffer_load_dwordx4 a[44:47], v246, s[28:31], 0 offen offset:3072 sc1\n"
"v_fmac_f32 v77, v3, v68\n"
"v_mfma_f32_16x16x32_bf16 v[128:131], v[164:167], a[100:103], v[128:131]\n"
"ds_read_b64_tr_b16 v[176:177], v243 offset:49152\n"
"v_fmac_f32 v70, v2, v69\n"
"v_mfma_f32_16x16x32_bf16 v[132:135], v[160:163], a[104:107], v[132:135]\n"
"ds_read_b64_tr_b16 v[178:179], v243 offset:53248\n"
"v_fmac_f32 v78, v2, v77\n"
"v_mfma_f32_16x16x32_bf16 v[132:135], v[164:167], a[108:111], v[132:135]\n"
"ds_read_b64_tr_b16 v[180:181], v243 offset:57344\n"
"v_fma_f32 v70, -v3, v77, v70\n"
"v_mfma_f32_16x16x32_bf16 v[136:139], v[160:163], a[112:115], v[136:139]\n"
"ds_read_b64_tr_b16 v[182:183], v243 offset:61440\n"
"buffer_load_dwordx4 a[48:51], v247, s[28:31], 0 offen offset:0 sc1\n"
"v_mfma_f32_16x16x32_bf16 v[136:139], v[164:167], a[116:119], v[136:139]\n"
"v_fmac_f32 v78, v3, v69\n"
"v_fmac_f32 v71, v2, v70\n"
"v_mfma_f32_16x16x32_bf16 v[140:143], v[160:163], a[120:123], v[140:143]\n"
"v_fmac_f32 v79, v2, v78\n"
"v_fma_f32 v71, -v3, v78, v71\n"
"v_mfma_f32_16x16x32_bf16 v[140:143], v[164:167], a[124:127], v[140:143]\n"
"v_fmac_f32 v79, v3, v70\n"
"s_waitcnt vmcnt(33) lgkmcnt(4)\n"
"v_mfma_f32_16x16x32_bf16 a[160:163], v[168:171], a[128:131], a[160:163]\n"
"s_lshr_b32 s89, s56, 1\n"
"s_lshl_b32 s89, s89, 3\n"
"v_mfma_f32_16x16x32_bf16 a[160:163], v[172:175], a[132:135], a[160:163]\n"
"ds_read_b64_tr_b16 v[152:153], v242 offset:50176\n"
"buffer_load_dwordx4 a[56:59], v247, s[28:31], 0 offen offset:1024 sc1\n"
"v_mfma_f32_16x16x32_bf16 a[164:167], v[168:171], a[136:139], a[164:167]\n"
"ds_read_b64_tr_b16 v[154:155], v242 offset:54272\n"
"s_and_b32 s90, s56, 1\n"
"v_mfma_f32_16x16x32_bf16 a[164:167], v[172:175], a[140:143], a[164:167]\n"
"ds_read_b64_tr_b16 v[156:157], v242 offset:58368\n"
"s_add_u32 s89, s89, s90\n"
"v_mfma_f32_16x16x32_bf16 a[168:171], v[168:171], a[144:147], a[168:171]\n"
"ds_read_b64_tr_b16 v[158:159], v242 offset:62464\n"
"s_lshl_b32 s89, s89, 11\n"
"v_mfma_f32_16x16x32_bf16 a[168:171], v[172:175], a[148:151], a[168:171]\n"
"s_add_u32 s89, s89, 229376\n"
"v_mul_f32 v16, v144, v16\n"
"v_mfma_f32_16x16x32_bf16 a[172:175], v[168:171], a[152:155], a[172:175]\n"
"buffer_load_dwordx4 a[52:55], v247, s[28:31], 0 offen offset:2048 sc1\n"
"v_mul_f32 v17, v145, v17\n"
"v_mfma_f32_16x16x32_bf16 a[172:175], v[172:175], a[156:159], a[172:175]\n"
"v_mul_f32 v18, v146, v18\n"
"s_waitcnt lgkmcnt(4)\n"
"v_mfma_f32_16x16x32_bf16 a[176:179], v[176:179], a[128:131], a[176:179]\n"
"v_mul_f32 v19, v147, v19\n"
"v_mul_f32 v20, v148, v20\n"
"v_mfma_f32_16x16x32_bf16 a[176:179], v[180:183], a[132:135], a[176:179]\n"
"ds_read_b64_tr_b16 v[160:161], v243 offset:50176\n"
"v_mul_f32 v21, v149, v21\n"
"v_mfma_f32_16x16x32_bf16 a[180:183], v[176:179], a[136:139], a[180:183]\n"
"ds_read_b64_tr_b16 v[162:163], v243 offset:54272\n"
"buffer_load_dwordx4 a[60:63], v247, s[28:31], 0 offen offset:3072 sc1\n"
"v_mfma_f32_16x16x32_bf16 a[180:183], v[180:183], a[140:143], a[180:183]\n"
"ds_read_b64_tr_b16 v[164:165], v243 offset:58368\n"
"s_mov_b32 s28, s79\n"
"v_mfma_f32_16x16x32_bf16 a[184:187], v[176:179], a[144:147], a[184:187]\n"
"ds_read_b64_tr_b16 v[166:167], v243 offset:62464\n"
"v_mul_f32 v22, v150, v22\n"
"v_mfma_f32_16x16x32_bf16 a[184:187], v[180:183], a[148:151], a[184:187]\n"
"v_mul_f32 v23, v151, v23\n"
"v_cvt_pk_bf16_f32 v16, v16, v17\n"
"v_mfma_f32_16x16x32_bf16 a[188:191], v[176:179], a[152:155], a[188:191]\n"
"v_cvt_pk_bf16_f32 v17, v18, v19\n"
"v_cvt_pk_bf16_f32 v18, v20, v21\n"
"v_mfma_f32_16x16x32_bf16 a[188:191], v[180:183], a[156:159], a[188:191]\n"
"buffer_load_dwordx4 v238, s[20:23], 0 offen offset:0 lds\n"
"s_waitcnt lgkmcnt(4)\n"
"v_mfma_f32_16x16x32_bf16 a[192:195], v[152:155], a[128:131], a[192:195]\n"
"v_cvt_pk_bf16_f32 v19, v22, v23\n"
"v_mov_b32 v192, 0x3f80\n"
"v_mfma_f32_16x16x32_bf16 a[192:195], v[156:159], a[132:135], a[192:195]\n"
"ds_read_b64_tr_b16 v[168:169], v242 offset:52224\n"
"v_lshlrev_b32 v192, v193, v192\n"
"v_mfma_f32_16x16x32_bf16 a[196:199], v[152:155], a[136:139], a[196:199]\n"
"ds_read_b64_tr_b16 v[170:171], v242 offset:56320\n"
"v_cndmask_b32 v192, 0, v192, s[94:95]\n"
"v_mfma_f32_16x16x32_bf16 a[196:199], v[156:159], a[140:143], a[196:199]\n"
"ds_write_b128 v248, v[16:19] offset:0\n"
"v_mfma_f32_16x16x32_bf16 a[200:203], v[152:155], a[144:147], a[200:203]\n"
"ds_read_b64_tr_b16 v[172:173], v242 offset:60416\n"
"buffer_load_dwordx4 v239, s[20:23], 0 offen offset:1024 lds\n"
"v_mfma_f32_16x16x32_bf16 a[200:203], v[156:159], a[148:151], a[200:203]\n"
"ds_read_b64_tr_b16 v[174:175], v242 offset:64512\n"
"v_mul_f32_e64 v24, v144, -v24\n"
"v_mfma_f32_16x16x32_bf16 a[204:207], v[152:155], a[152:155], a[204:207]\n"
"v_mul_f32_e64 v25, v145, -v25\n"
"v_mul_f32_e64 v26, v146, -v26\n"
"v_mfma_f32_16x16x32_bf16 a[204:207], v[156:159], a[156:159], a[204:207]\n"
"v_mul_f32_e64 v27, v147, -v27\n"
"s_waitcnt lgkmcnt(5)\n"
"v_mfma_f32_16x16x32_bf16 a[208:211], v[160:163], a[128:131], a[208:211]\n"
"buffer_load_dwordx4 v240, s[20:23], 0 offen offset:2048 lds\n"
"v_mul_f32_e64 v28, v148, -v28\n"
"v_mfma_f32_16x16x32_bf16 a[208:211], v[164:167], a[132:135], a[208:211]\n"
"ds_read_b64_tr_b16 v[176:177], v243 offset:52224\n"
"v_mul_f32_e64 v29, v149, -v29\n"
"v_mfma_f32_16x16x32_bf16 a[212:215], v[160:163], a[136:139], a[212:215]\n"
"ds_read_b64_tr_b16 v[178:179], v243 offset:56320\n"
"v_mul_f32_e64 v30, v150, -v30\n"
"v_mfma_f32_16x16x32_bf16 a[212:215], v[164:167], a[140:143], a[212:215]\n"
"ds_read_b64_tr_b16 v[180:181], v243 offset:60416\n"
"v_mul_f32_e64 v31, v151, -v31\n"
"v_mfma_f32_16x16x32_bf16 a[216:219], v[160:163], a[144:147], a[216:219]\n"
"ds_read_b64_tr_b16 v[182:183], v243 offset:64512\n"
"v_cvt_pk_bf16_f32 v24, v24, v25\n"
"v_mfma_f32_16x16x32_bf16 a[216:219], v[164:167], a[148:151], a[216:219]\n"
"buffer_load_dwordx4 v241, s[20:23], 0 offen offset:3072 lds\n"
"s_lshl_b32 s92, s80, 5\n"
"v_mfma_f32_16x16x32_bf16 a[220:223], v[160:163], a[152:155], a[220:223]\n"
"s_add_u32 s20, s40, s92\n"
"s_add_u32 m0, s63, 16384\n"
"v_mfma_f32_16x16x32_bf16 a[220:223], v[164:167], a[156:159], a[220:223]\n"
"v_cvt_pk_bf16_f32 v25, v26, v27\n"
"s_waitcnt lgkmcnt(4)\n"
"v_mfma_f32_16x16x32_bf16 a[224:227], v[168:171], a[128:131], a[224:227]\n"
"v_cvt_pk_bf16_f32 v26, v28, v29\n"
"v_cvt_pk_bf16_f32 v27, v30, v31\n"
"v_mfma_f32_16x16x32_bf16 a[224:227], v[172:175], a[132:135], a[224:227]\n"
"ds_read_b64_tr_b16 v[152:153], v242 offset:51200\n"
"v_and_b32 v193, 2, v209\n"
"v_mfma_f32_16x16x32_bf16 a[228:231], v[168:171], a[136:139], a[228:231]\n"
"ds_read_b64_tr_b16 v[154:155], v242 offset:55296\n"
"buffer_load_dwordx4 a[64:67], v246, s[28:31], 0 offen offset:0 sc1\n"
"v_mfma_f32_16x16x32_bf16 a[228:231], v[172:175], a[140:143], a[228:231]\n"
"ds_write_b128 v248, v[24:27] offset:16384\n"
"v_mfma_f32_16x16x32_bf16 a[232:235], v[168:171], a[144:147], a[232:235]\n"
"ds_read_b64_tr_b16 v[156:157], v242 offset:59392\n"
"v_mul_f32 v32, v144, v32\n"
"v_mfma_f32_16x16x32_bf16 a[232:235], v[172:175], a[148:151], a[232:235]\n"
"ds_read_b64_tr_b16 v[158:159], v242 offset:63488\n"
"v_mul_f32 v33, v145, v33\n"
"v_mfma_f32_16x16x32_bf16 a[236:239], v[168:171], a[152:155], a[236:239]\n"
"v_mul_f32 v34, v146, v34\n"
"v_mul_f32 v35, v147, v35\n"
"v_mfma_f32_16x16x32_bf16 a[236:239], v[172:175], a[156:159], a[236:239]\n"
"buffer_load_dwordx4 a[72:75], v246, s[28:31], 0 offen offset:1024 sc1\n"
"s_waitcnt lgkmcnt(5)\n"
"v_mfma_f32_16x16x32_bf16 a[240:243], v[176:179], a[128:131], a[240:243]\n"
"v_mul_f32 v36, v148, v36\n"
"v_mul_f32 v37, v149, v37\n"
"v_mfma_f32_16x16x32_bf16 a[240:243], v[180:183], a[132:135], a[240:243]\n"
"ds_read_b64_tr_b16 v[160:161], v243 offset:51200\n"
"v_mul_f32 v38, v150, v38\n"
"v_mfma_f32_16x16x32_bf16 a[244:247], v[176:179], a[136:139], a[244:247]\n"
"ds_read_b64_tr_b16 v[162:163], v243 offset:55296\n"
"v_mul_f32 v39, v151, v39\n"
"v_mfma_f32_16x16x32_bf16 a[244:247], v[180:183], a[140:143], a[244:247]\n"
"ds_read_b64_tr_b16 v[164:165], v243 offset:59392\n"
"buffer_load_dwordx4 a[68:71], v246, s[28:31], 0 offen offset:2048 sc1\n"
"v_mfma_f32_16x16x32_bf16 a[248:251], v[176:179], a[144:147], a[248:251]\n"
"ds_read_b64_tr_b16 v[166:167], v243 offset:63488\n"
"v_cvt_pk_bf16_f32 v32, v32, v33\n"
"v_mfma_f32_16x16x32_bf16 a[248:251], v[180:183], a[148:151], a[248:251]\n"
"v_cvt_pk_bf16_f32 v33, v34, v35\n"
"v_cvt_pk_bf16_f32 v34, v36, v37\n"
"v_mfma_f32_16x16x32_bf16 a[252:255], v[176:179], a[152:155], a[252:255]\n"
"v_cvt_pk_bf16_f32 v35, v38, v39\n"
"v_cmp_eq_u32 s[94:95], 0, v193\n"
"v_mfma_f32_16x16x32_bf16 a[252:255], v[180:183], a[156:159], a[252:255]\n"
"s_waitcnt lgkmcnt(4)\n"
"v_mfma_f32_16x16x32_bf16 v[80:83], v[152:155], a[128:131], v[80:83]\n"
"s_waitcnt vmcnt(39)\n"
"s_barrier\n"
"v_mfma_f32_16x16x32_bf16 v[80:83], v[156:159], a[132:135], v[80:83]\n"
"ds_read_b64_tr_b16 v[168:169], v244 offset:0\n"
"buffer_load_dwordx4 a[76:79], v246, s[28:31], 0 offen offset:3072 sc1\n"
"v_mfma_f32_16x16x32_bf16 v[84:87], v[152:155], a[136:139], v[84:87]\n"
"ds_write_b128 v248, v[32:35] offset:1024\n"
"v_mfma_f32_16x16x32_bf16 v[84:87], v[156:159], a[140:143], v[84:87]\n"
"ds_read_b64_tr_b16 v[170:171], v244 offset:4096\n"
"v_mfma_f32_16x16x32_bf16 v[88:91], v[152:155], a[144:147], v[88:91]\n"
"ds_read_b64_tr_b16 v[172:173], v244 offset:8192\n"
"v_mfma_f32_16x16x32_bf16 v[88:91], v[156:159], a[148:151], v[88:91]\n"
"ds_read_b64_tr_b16 v[20:21], v249 offset:0\n"
"ds_read_b64_tr_b16 v[174:175], v244 offset:12288\n"
"v_mfma_f32_16x16x32_bf16 v[92:95], v[152:155], a[152:155], v[92:95]\n"
"ds_read_b64_tr_b16 v[22:23], v218 offset:256\n"
"buffer_load_dwordx4 a[80:83], v247, s[28:31], 0 offen offset:0 sc1\n"
"v_mfma_f32_16x16x32_bf16 v[92:95], v[156:159], a[156:159], v[92:95]\n"
"ds_read_b64_tr_b16 v[36:37], v249 offset:8\n"
"s_waitcnt lgkmcnt(8)\n"
"v_mfma_f32_16x16x32_bf16 v[128:131], v[160:163], a[128:131], v[128:131]\n"
"ds_read_b64_tr_b16 v[38:39], v218 offset:264\n"
"v_mul_f32_e64 v40, v144, -v40\n"
"v_mfma_f32_16x16x32_bf16 v[128:131], v[164:167], a[132:135], v[128:131]\n"
"ds_read_b64_tr_b16 v[176:177], v245 offset:0\n"
"v_mul_f32_e64 v41, v145, -v41\n"
"v_mfma_f32_16x16x32_bf16 v[132:135], v[160:163], a[136:139], v[132:135]\n"
"ds_read_b64_tr_b16 v[178:179], v245 offset:4096\n"
"buffer_load_dwordx4 a[88:91], v247, s[28:31], 0 offen offset:1024 sc1\n"
"v_mfma_f32_16x16x32_bf16 v[132:135], v[164:167], a[140:143], v[132:135]\n"
"ds_read_b64_tr_b16 v[180:181], v245 offset:8192\n"
"v_mul_f32_e64 v42, v146, -v42\n"
"v_mfma_f32_16x16x32_bf16 v[136:139], v[160:163], a[144:147], v[136:139]\n"
"ds_read_b64_tr_b16 v[182:183], v245 offset:12288\n"
"v_mul_f32_e64 v43, v147, -v43\n"
"v_mfma_f32_16x16x32_bf16 v[136:139], v[164:167], a[148:151], v[136:139]\n"
"v_mul_f32_e64 v44, v148, -v44\n"
"v_mul_f32_e64 v45, v149, -v45\n"
"v_mfma_f32_16x16x32_bf16 v[140:143], v[160:163], a[152:155], v[140:143]\n"
"buffer_load_dwordx4 a[84:87], v247, s[28:31], 0 offen offset:2048 sc1\n"
"v_mul_f32_e64 v46, v150, -v46\n"
"v_mfma_f32_16x16x32_bf16 v[140:143], v[164:167], a[156:159], v[140:143]\n"
"v_mul_f32_e64 v47, v151, -v47\n"
"s_waitcnt vmcnt(35) lgkmcnt(7)\n"
"v_mfma_f32_16x16x32_bf16 a[160:163], v[168:171], v[96:99], a[160:163]\n"
"v_cvt_pk_bf16_f32 v40, v40, v41\n"
"v_cvt_pk_bf16_f32 v41, v42, v43\n"
"v_mfma_f32_16x16x32_bf16 a[160:163], v[172:175], v[100:103], a[160:163]\n"
"ds_read_b64_tr_b16 v[152:153], v244 offset:1024\n"
"v_cvt_pk_bf16_f32 v42, v44, v45\n"
"v_mfma_f32_16x16x32_bf16 a[164:167], v[168:171], v[104:107], a[164:167]\n"
"ds_read_b64_tr_b16 v[154:155], v244 offset:5120\n"
"v_cvt_pk_bf16_f32 v43, v46, v47\n"
"v_mfma_f32_16x16x32_bf16 a[164:167], v[172:175], v[108:111], a[164:167]\n"
"ds_read_b64_tr_b16 v[156:157], v244 offset:9216\n"
"buffer_load_dwordx4 a[92:95], v247, s[28:31], 0 offen offset:3072 sc1\n"
"v_mfma_f32_16x16x32_bf16 a[168:171], v[168:171], v[112:115], a[168:171]\n"
"ds_read_b64_tr_b16 v[158:159], v244 offset:13312\n"
"s_mov_b32 s28, s80\n"
"v_mfma_f32_16x16x32_bf16 a[168:171], v[172:175], v[116:119], a[168:171]\n"
"ds_write_b128 v248, v[40:43] offset:17408\n"
"v_mfma_f32_16x16x32_bf16 a[172:175], v[168:171], v[120:123], a[172:175]\n"
"ds_read_b64_tr_b16 v[28:29], v249 offset:16384\n"
"v_mfma_f32_16x16x32_bf16 a[172:175], v[172:175], v[124:127], a[172:175]\n"
"ds_read_b64_tr_b16 v[30:31], v218 offset:16640\n"
"s_waitcnt lgkmcnt(7)\n"
"v_mfma_f32_16x16x32_bf16 a[176:179], v[176:179], v[96:99], a[176:179]\n"
"buffer_store_dwordx4 v[20:23], v200, s[36:39], s89 offen offset:0 sc1\n"
"v_mfma_f32_16x16x32_bf16 a[176:179], v[180:183], v[100:103], a[176:179]\n"
"ds_read_b64_tr_b16 v[160:161], v245 offset:1024\n"
"v_mfma_f32_16x16x32_bf16 a[180:183], v[176:179], v[104:107], a[180:183]\n"
"ds_read_b64_tr_b16 v[162:163], v245 offset:5120\n"
"v_mfma_f32_16x16x32_bf16 a[180:183], v[180:183], v[108:111], a[180:183]\n"
"ds_read_b64_tr_b16 v[44:45], v249 offset:16392\n"
"v_mfma_f32_16x16x32_bf16 a[184:187], v[176:179], v[112:115], a[184:187]\n"
"ds_read_b64_tr_b16 v[164:165], v245 offset:9216\n"
"v_mfma_f32_16x16x32_bf16 a[184:187], v[180:183], v[116:119], a[184:187]\n"
"buffer_store_dwordx4 v[36:39], v200, s[36:39], s89 offen offset:1024 sc1\n"
"v_mfma_f32_16x16x32_bf16 a[188:191], v[176:179], v[120:123], a[188:191]\n"
"ds_read_b64_tr_b16 v[166:167], v245 offset:13312\n"
"s_add_u32 s89, s89, 4096\n"
"v_mfma_f32_16x16x32_bf16 a[188:191], v[180:183], v[124:127], a[188:191]\n"
"ds_read_b64_tr_b16 v[46:47], v218 offset:16648\n"
"s_waitcnt lgkmcnt(9)\n"
"v_mfma_f32_16x16x32_bf16 a[192:195], v[152:155], v[96:99], a[192:195]\n"
"v_mul_f32 v48, v144, v48\n"
"v_mul_f32 v49, v145, v49\n"
"v_mfma_f32_16x16x32_bf16 a[192:195], v[156:159], v[100:103], a[192:195]\n"
"ds_read_b64_tr_b16 v[168:169], v244 offset:3072\n"
"v_mul_f32 v50, v146, v50\n"
"v_mfma_f32_16x16x32_bf16 a[196:199], v[152:155], v[104:107], a[196:199]\n"
"s_waitcnt lgkmcnt(7)\n"
"buffer_store_dwordx4 v[28:31], v200, s[36:39], s89 offen offset:0 sc1\n"
"v_mfma_f32_16x16x32_bf16 a[196:199], v[156:159], v[108:111], a[196:199]\n"
"ds_read_b64_tr_b16 v[170:171], v244 offset:7168\n"
"v_mul_f32 v51, v147, v51\n"
"v_mfma_f32_16x16x32_bf16 a[200:203], v[152:155], v[112:115], a[200:203]\n"
"ds_read_b64_tr_b16 v[172:173], v244 offset:11264\n"
"v_mul_f32 v52, v148, v52\n"
"v_mfma_f32_16x16x32_bf16 a[200:203], v[156:159], v[116:119], a[200:203]\n"
"ds_read_b64_tr_b16 v[174:175], v244 offset:15360\n"
"v_mul_f32 v53, v149, v53\n"
"v_mfma_f32_16x16x32_bf16 a[204:207], v[152:155], v[120:123], a[204:207]\n"
"s_waitcnt lgkmcnt(4)\n"
"buffer_store_dwordx4 v[44:47], v200, s[36:39], s89 offen offset:1024 sc1\n"
"v_mfma_f32_16x16x32_bf16 a[204:207], v[156:159], v[124:127], a[204:207]\n"
"s_add_u32 s89, s89, 4096\n"
"v_mul_f32 v54, v150, v54\n"
"v_mfma_f32_16x16x32_bf16 a[208:211], v[160:163], v[96:99], a[208:211]\n"
"v_mul_f32 v55, v151, v55\n"
"v_cvt_pk_bf16_f32 v48, v48, v49\n"
"v_mfma_f32_16x16x32_bf16 a[208:211], v[164:167], v[100:103], a[208:211]\n"
"ds_read_b64_tr_b16 v[176:177], v245 offset:3072\n"
"v_cvt_pk_bf16_f32 v49, v50, v51\n"
"v_mfma_f32_16x16x32_bf16 a[212:215], v[160:163], v[104:107], a[212:215]\n"
"ds_read_b64_tr_b16 v[178:179], v245 offset:7168\n"
"buffer_load_dwordx4 v238, s[20:23], 0 offen offset:0 lds\n"
".Lk_h9:\n"
"s_cbranch_execz .Lk_h13\n"
"v_mfma_f32_16x16x32_bf16 a[212:215], v[164:167], v[108:111], a[212:215]\n"
"ds_read_b64_tr_b16 v[180:181], v245 offset:11264\n"
"v_cvt_pk_bf16_f32 v50, v52, v53\n"
"v_mfma_f32_16x16x32_bf16 a[216:219], v[160:163], v[112:115], a[216:219]\n"
"ds_read_b64_tr_b16 v[182:183], v245 offset:15360\n"
"v_cvt_pk_bf16_f32 v51, v54, v55\n"
"v_mfma_f32_16x16x32_bf16 a[216:219], v[164:167], v[116:119], a[216:219]\n"
"ds_write_b128 v248, v[48:51] offset:2048\n"
"v_mfma_f32_16x16x32_bf16 a[220:223], v[160:163], v[120:123], a[220:223]\n"
"buffer_load_dwordx4 v239, s[20:23], 0 offen offset:1024 lds\n"
"v_mul_f32_e64 v56, v144, -v56\n"
"v_mfma_f32_16x16x32_bf16 a[220:223], v[164:167], v[124:127], a[220:223]\n"
"v_mul_f32_e64 v57, v145, -v57\n"
"s_waitcnt lgkmcnt(5)\n"
"v_mfma_f32_16x16x32_bf16 a[224:227], v[168:171], v[96:99], a[224:227]\n"
"v_mul_f32_e64 v58, v146, -v58\n"
"v_mul_f32_e64 v59, v147, -v59\n"
"v_mfma_f32_16x16x32_bf16 a[224:227], v[172:175], v[100:103], a[224:227]\n"
"ds_read_b64_tr_b16 v[152:153], v244 offset:2048\n"
"v_mul_f32_e64 v60, v148, -v60\n"
"v_mfma_f32_16x16x32_bf16 a[228:231], v[168:171], v[104:107], a[228:231]\n"
"ds_read_b64_tr_b16 v[154:155], v244 offset:6144\n"
"buffer_load_dwordx4 v240, s[20:23], 0 offen offset:2048 lds\n"
"v_mfma_f32_16x16x32_bf16 a[228:231], v[172:175], v[108:111], a[228:231]\n"
"ds_read_b64_tr_b16 v[156:157], v244 offset:10240\n"
"v_mul_f32_e64 v61, v149, -v61\n"
"v_mfma_f32_16x16x32_bf16 a[232:235], v[168:171], v[112:115], a[232:235]\n"
"ds_read_b64_tr_b16 v[158:159], v244 offset:14336\n"
"v_mul_f32_e64 v62, v150, -v62\n"
"v_mfma_f32_16x16x32_bf16 a[232:235], v[172:175], v[116:119], a[232:235]\n"
"v_mul_f32_e64 v63, v151, -v63\n"
"v_cvt_pk_bf16_f32 v56, v56, v57\n"
"v_mfma_f32_16x16x32_bf16 a[236:239], v[168:171], v[120:123], a[236:239]\n"
"buffer_load_dwordx4 v241, s[20:23], 0 offen offset:3072 lds\n"
"s_lshl_b32 s92, s81, 5\n"
"v_mfma_f32_16x16x32_bf16 a[236:239], v[172:175], v[124:127], a[236:239]\n"
"s_add_u32 s20, s40, s92\n"
"s_waitcnt lgkmcnt(5)\n"
"v_mfma_f32_16x16x32_bf16 a[240:243], v[176:179], v[96:99], a[240:243]\n"
"s_add_u32 m0, s63, 32768\n"
"v_cvt_pk_bf16_f32 v57, v58, v59\n"
"v_mfma_f32_16x16x32_bf16 a[240:243], v[180:183], v[100:103], a[240:243]\n"
"ds_read_b64_tr_b16 v[160:161], v245 offset:2048\n"
"v_cvt_pk_bf16_f32 v58, v60, v61\n"
"v_mfma_f32_16x16x32_bf16 a[244:247], v[176:179], v[104:107], a[244:247]\n"
"ds_read_b64_tr_b16 v[162:163], v245 offset:6144\n"
"buffer_load_dwordx4 a[96:99], v246, s[28:31], 0 offen offset:0 sc1\n"
"v_mfma_f32_16x16x32_bf16 a[244:247], v[180:183], v[108:111], a[244:247]\n"
"ds_read_b64_tr_b16 v[164:165], v245 offset:10240\n"
"v_cvt_pk_bf16_f32 v59, v62, v63\n"
"v_mfma_f32_16x16x32_bf16 a[248:251], v[176:179], v[112:115], a[248:251]\n"
"ds_read_b64_tr_b16 v[166:167], v245 offset:14336\n"
"v_mfma_f32_16x16x32_bf16 a[248:251], v[180:183], v[116:119], a[248:251]\n"
"ds_write_b128 v248, v[56:59] offset:18432\n"
"v_mfma_f32_16x16x32_bf16 a[252:255], v[176:179], v[120:123], a[252:255]\n"
"v_mul_f32 v64, v144, v64\n"
"v_mul_f32 v65, v145, v65\n"
"v_mfma_f32_16x16x32_bf16 a[252:255], v[180:183], v[124:127], a[252:255]\n"
"buffer_load_dwordx4 a[104:107], v246, s[28:31], 0 offen offset:1024 sc1\n"
"s_waitcnt lgkmcnt(5)\n"
"v_mfma_f32_16x16x32_bf16 v[80:83], v[152:155], v[96:99], v[80:83]\n"
"s_waitcnt vmcnt(42)\n"
"s_barrier\n"
"v_mfma_f32_16x16x32_bf16 v[80:83], v[156:159], v[100:103], v[80:83]\n"
"ds_read_b64_tr_b16 v[168:169], v244 offset:16384\n"
"v_mul_f32 v66, v146, v66\n"
"v_mfma_f32_16x16x32_bf16 v[84:87], v[152:155], v[104:107], v[84:87]\n"
"ds_read_b64_tr_b16 v[170:171], v244 offset:20480\n"
"v_mul_f32 v67, v147, v67\n"
"v_mfma_f32_16x16x32_bf16 v[84:87], v[156:159], v[108:111], v[84:87]\n"
"ds_read_b64_tr_b16 v[172:173], v244 offset:24576\n"
"s_add_u32 s67, s67, 131072\n"
"v_mfma_f32_16x16x32_bf16 v[88:91], v[152:155], v[112:115], v[88:91]\n"
"ds_read_b64_tr_b16 v[174:175], v244 offset:28672\n"
"buffer_load_dwordx4 a[100:103], v246, s[28:31], 0 offen offset:2048 sc1\n"
"v_mfma_f32_16x16x32_bf16 v[88:91], v[156:159], v[116:119], v[88:91]\n"
"v_mul_f32 v68, v148, v68\n"
"v_mul_f32 v69, v149, v69\n"
"v_mfma_f32_16x16x32_bf16 v[92:95], v[152:155], v[120:123], v[92:95]\n"
"v_mul_f32 v70, v150, v70\n"
"v_mul_f32 v71, v151, v71\n"
"v_mfma_f32_16x16x32_bf16 v[92:95], v[156:159], v[124:127], v[92:95]\n"
"v_cvt_pk_bf16_f32 v64, v64, v65\n"
"s_waitcnt lgkmcnt(5)\n"
"v_mfma_f32_16x16x32_bf16 v[128:131], v[160:163], v[96:99], v[128:131]\n"
"buffer_load_dword v232, v233, s[36:39], s67 offen sc1\n"
"v_mfma_f32_16x16x32_bf16 v[128:131], v[164:167], v[100:103], v[128:131]\n"
"ds_read_b64_tr_b16 v[176:177], v245 offset:16384\n"
"buffer_load_dwordx4 a[108:111], v246, s[28:31], 0 offen offset:3072 sc1\n"
"v_mfma_f32_16x16x32_bf16 v[132:135], v[160:163], v[104:107], v[132:135]\n"
"ds_read_b64_tr_b16 v[178:179], v245 offset:20480\n"
"v_cvt_pk_bf16_f32 v65, v66, v67\n"
"v_mfma_f32_16x16x32_bf16 v[132:135], v[164:167], v[108:111], v[132:135]\n"
"ds_read_b64_tr_b16 v[180:181], v245 offset:24576\n"
"v_cvt_pk_bf16_f32 v66, v68, v69\n"
"v_mfma_f32_16x16x32_bf16 v[136:139], v[160:163], v[112:115], v[136:139]\n"
"ds_read_b64_tr_b16 v[182:183], v245 offset:28672\n"
"v_cvt_pk_bf16_f32 v67, v70, v71\n"
"v_mfma_f32_16x16x32_bf16 v[136:139], v[164:167], v[116:119], v[136:139]\n"
"ds_write_b128 v248, v[64:67] offset:3072\n"
"v_mfma_f32_16x16x32_bf16 v[140:143], v[160:163], v[120:123], v[140:143]\n"
"ds_read_b64_tr_b16 v[52:53], v249 offset:2048\n"
"buffer_load_dwordx4 a[112:115], v247, s[28:31], 0 offen offset:0 sc1\n"
"v_mfma_f32_16x16x32_bf16 v[140:143], v[164:167], v[124:127], v[140:143]\n"
"ds_read_b64_tr_b16 v[54:55], v218 offset:2304\n"
"s_waitcnt vmcnt(38) lgkmcnt(7)\n"
"v_mfma_f32_16x16x32_bf16 a[160:163], v[168:171], a[0:3], a[160:163]\n"
"ds_read_b64_tr_b16 v[68:69], v249 offset:2056\n"
"v_mfma_f32_16x16x32_bf16 a[160:163], v[172:175], a[4:7], a[160:163]\n"
"ds_read_b64_tr_b16 v[152:153], v244 offset:17408\n"
"v_mfma_f32_16x16x32_bf16 a[164:167], v[168:171], a[8:11], a[164:167]\n"
"ds_read_b64_tr_b16 v[154:155], v244 offset:21504\n"
"buffer_load_dwordx4 a[120:123], v247, s[28:31], 0 offen offset:1024 sc1\n"
"v_mfma_f32_16x16x32_bf16 a[164:167], v[172:175], a[12:15], a[164:167]\n"
"ds_read_b64_tr_b16 v[70:71], v218 offset:2312\n"
"v_mul_f32_e64 v72, v144, -v72\n"
"v_mfma_f32_16x16x32_bf16 a[168:171], v[168:171], a[16:19], a[168:171]\n"
"ds_read_b64_tr_b16 v[156:157], v244 offset:25600\n"
"v_mul_f32_e64 v73, v145, -v73\n"
"v_mfma_f32_16x16x32_bf16 a[168:171], v[172:175], a[20:23], a[168:171]\n"
"ds_read_b64_tr_b16 v[158:159], v244 offset:29696\n"
"v_mul_f32_e64 v74, v146, -v74\n"
"v_mfma_f32_16x16x32_bf16 a[172:175], v[168:171], a[24:27], a[172:175]\n"
"v_mul_f32_e64 v75, v147, -v75\n"
"v_mul_f32_e64 v76, v148, -v76\n"
"v_mfma_f32_16x16x32_bf16 a[172:175], v[172:175], a[28:31], a[172:175]\n"
"buffer_load_dwordx4 a[116:119], v247, s[28:31], 0 offen offset:2048 sc1\n"
"s_waitcnt lgkmcnt(9)\n"
"v_mfma_f32_16x16x32_bf16 a[176:179], v[176:179], a[0:3], a[176:179]\n"
"v_mul_f32_e64 v77, v149, -v77\n"
"v_mul_f32_e64 v78, v150, -v78\n"
"v_mfma_f32_16x16x32_bf16 a[176:179], v[180:183], a[4:7], a[176:179]\n"
"ds_read_b64_tr_b16 v[160:161], v245 offset:17408\n"
"v_mul_f32_e64 v79, v151, -v79\n"
"v_mfma_f32_16x16x32_bf16 a[180:183], v[176:179], a[8:11], a[180:183]\n"
"ds_read_b64_tr_b16 v[162:163], v245 offset:21504\n"
"v_cvt_pk_bf16_f32 v72, v72, v73\n"
"v_mfma_f32_16x16x32_bf16 a[180:183], v[180:183], a[12:15], a[180:183]\n"
"ds_read_b64_tr_b16 v[164:165], v245 offset:25600\n"
"buffer_load_dwordx4 a[124:127], v247, s[28:31], 0 offen offset:3072 sc1\n"
"v_mfma_f32_16x16x32_bf16 a[184:187], v[176:179], a[16:19], a[184:187]\n"
"ds_read_b64_tr_b16 v[166:167], v245 offset:29696\n"
"s_mov_b32 s28, s81\n"
"v_mfma_f32_16x16x32_bf16 a[184:187], v[180:183], a[20:23], a[184:187]\n"
"v_cvt_pk_bf16_f32 v73, v74, v75\n"
"v_cvt_pk_bf16_f32 v74, v76, v77\n"
"v_mfma_f32_16x16x32_bf16 a[188:191], v[176:179], a[24:27], a[188:191]\n"
"v_cvt_pk_bf16_f32 v75, v78, v79\n"
"v_mfma_f32_16x16x32_bf16 a[188:191], v[180:183], a[28:31], a[188:191]\n"
"s_waitcnt lgkmcnt(4)\n"
"v_mfma_f32_16x16x32_bf16 a[192:195], v[152:155], a[0:3], a[192:195]\n"
"ds_write_b128 v248, v[72:75] offset:19456\n"
"v_mfma_f32_16x16x32_bf16 a[192:195], v[156:159], a[4:7], a[192:195]\n"
"ds_read_b64_tr_b16 v[168:169], v244 offset:19456\n"
"buffer_load_dwordx4 v238, s[20:23], 0 offen offset:0 lds\n"
"v_mfma_f32_16x16x32_bf16 a[196:199], v[152:155], a[8:11], a[196:199]\n"
"ds_read_b64_tr_b16 v[170:171], v244 offset:23552\n"
"v_mfma_f32_16x16x32_bf16 a[196:199], v[156:159], a[12:15], a[196:199]\n"
"ds_read_b64_tr_b16 v[60:61], v249 offset:18432\n"
"v_mfma_f32_16x16x32_bf16 a[200:203], v[152:155], a[16:19], a[200:203]\n"
"ds_read_b64_tr_b16 v[172:173], v244 offset:27648\n"
"v_mfma_f32_16x16x32_bf16 a[200:203], v[156:159], a[20:23], a[200:203]\n"
"ds_read_b64_tr_b16 v[174:175], v244 offset:31744\n"
"v_mfma_f32_16x16x32_bf16 a[204:207], v[152:155], a[24:27], a[204:207]\n"
"buffer_store_dwordx4 v[52:55], v200, s[36:39], s89 offen offset:0 sc1\n"
"v_mfma_f32_16x16x32_bf16 a[204:207], v[156:159], a[28:31], a[204:207]\n"
"ds_read_b64_tr_b16 v[62:63], v218 offset:18688\n"
"s_waitcnt lgkmcnt(7)\n"
"v_mfma_f32_16x16x32_bf16 a[208:211], v[160:163], a[0:3], a[208:211]\n"
"s_waitcnt vmcnt(15)\n"
"buffer_store_dword v219, v220, s[36:39], s43 offen sc1\n"
"v_mfma_f32_16x16x32_bf16 a[208:211], v[164:167], a[4:7], a[208:211]\n"
"ds_read_b64_tr_b16 v[176:177], v245 offset:19456\n"
"s_add_u32 s43, s43, 131072\n"
"v_mfma_f32_16x16x32_bf16 a[212:215], v[160:163], a[8:11], a[212:215]\n"
"ds_read_b64_tr_b16 v[178:179], v245 offset:23552\n"
"v_mfma_f32_16x16x32_bf16 a[212:215], v[164:167], a[12:15], a[212:215]\n"
"buffer_store_dwordx4 v[68:71], v200, s[36:39], s89 offen offset:1024 sc1\n"
"v_mfma_f32_16x16x32_bf16 a[216:219], v[160:163], a[16:19], a[216:219]\n"
"ds_read_b64_tr_b16 v[76:77], v249 offset:18440\n"
"s_add_u32 s89, s89, 4096\n"
"v_mfma_f32_16x16x32_bf16 a[216:219], v[164:167], a[20:23], a[216:219]\n"
"ds_read_b64_tr_b16 v[180:181], v245 offset:27648\n"
"ds_read_b64_tr_b16 v[182:183], v245 offset:31744\n"
"v_mfma_f32_16x16x32_bf16 a[220:223], v[160:163], a[24:27], a[220:223]\n"
"ds_read_b64_tr_b16 v[78:79], v218 offset:18696\n"
"v_mfma_f32_16x16x32_bf16 a[220:223], v[164:167], a[28:31], a[220:223]\n"
"s_waitcnt lgkmcnt(7)\n"
"v_mfma_f32_16x16x32_bf16 a[224:227], v[168:171], a[0:3], a[224:227]\n"
"s_waitcnt lgkmcnt(6)\n"
"buffer_store_dwordx4 v[60:63], v200, s[36:39], s89 offen offset:0 sc1\n"
"v_mfma_f32_16x16x32_bf16 a[224:227], v[172:175], a[4:7], a[224:227]\n"
"ds_read_b64_tr_b16 v[152:153], v244 offset:18432\n"
"v_mfma_f32_16x16x32_bf16 a[228:231], v[168:171], a[8:11], a[228:231]\n"
"ds_read_b64_tr_b16 v[154:155], v244 offset:22528\n"
"v_mfma_f32_16x16x32_bf16 a[228:231], v[172:175], a[12:15], a[228:231]\n"
"ds_read_b64_tr_b16 v[156:157], v244 offset:26624\n"
"v_mfma_f32_16x16x32_bf16 a[232:235], v[168:171], a[16:19], a[232:235]\n"
"ds_read_b64_tr_b16 v[158:159], v244 offset:30720\n"
"buffer_load_dwordx4 v239, s[20:23], 0 offen offset:1024 lds\n"
"v_mfma_f32_16x16x32_bf16 a[232:235], v[172:175], a[20:23], a[232:235]\n"
"v_mfma_f32_16x16x32_bf16 a[236:239], v[168:171], a[24:27], a[236:239]\n"
"v_mfma_f32_16x16x32_bf16 a[236:239], v[172:175], a[28:31], a[236:239]\n"
"s_waitcnt lgkmcnt(5)\n"
"v_mfma_f32_16x16x32_bf16 a[240:243], v[176:179], a[0:3], a[240:243]\n"
"s_waitcnt lgkmcnt(4)\n"
"buffer_store_dwordx4 v[76:79], v200, s[36:39], s89 offen offset:1024 sc1\n"
"v_mfma_f32_16x16x32_bf16 a[240:243], v[180:183], a[4:7], a[240:243]\n"
"ds_read_b64_tr_b16 v[160:161], v245 offset:18432\n"
"s_add_u32 s89, s89, 4096\n"
"v_mfma_f32_16x16x32_bf16 a[244:247], v[176:179], a[8:11], a[244:247]\n"
"ds_read_b64_tr_b16 v[162:163], v245 offset:22528\n"
"v_mov_b32 v200, 0\n"
"v_mfma_f32_16x16x32_bf16 a[244:247], v[180:183], a[12:15], a[244:247]\n"
"ds_read_b64_tr_b16 v[164:165], v245 offset:26624\n"
"v_mov_b32 v201, 0\n"
"v_mfma_f32_16x16x32_bf16 a[248:251], v[176:179], a[16:19], a[248:251]\n"
"ds_read_b64_tr_b16 v[166:167], v245 offset:30720\n"
"buffer_load_dwordx4 v240, s[20:23], 0 offen offset:2048 lds\n"
"v_mfma_f32_16x16x32_bf16 a[248:251], v[180:183], a[20:23], a[248:251]\n"
"v_mov_b32 v204, 0\n"
"v_mov_b32 v205, 0\n"
"v_mfma_f32_16x16x32_bf16 a[252:255], v[176:179], a[24:27], a[252:255]\n"
"v_cndmask_b32 v202, 0, v192, s[94:95]\n"
"v_cndmask_b32 v203, v192, 0, s[94:95]\n"
"v_mfma_f32_16x16x32_bf16 a[252:255], v[180:183], a[28:31], a[252:255]\n"
"s_lshl_b32 s72, s58, 2\n"
"s_waitcnt lgkmcnt(4)\n"
"v_mfma_f32_16x16x32_bf16 v[80:83], v[152:155], a[0:3], v[80:83]\n"
"s_barrier\n"
"buffer_load_dwordx4 v241, s[20:23], 0 offen offset:3072 lds\n"
"v_mfma_f32_16x16x32_bf16 v[80:83], v[156:159], a[4:7], v[80:83]\n"
"ds_read_b64_tr_b16 v[168:169], v244 offset:32768\n"
"s_lshl_b32 s92, s82, 5\n"
"v_mfma_f32_16x16x32_bf16 v[84:87], v[152:155], a[8:11], v[84:87]\n"
"ds_read_b64_tr_b16 v[170:171], v244 offset:36864\n"
"s_add_u32 s20, s40, s92\n"
"v_mfma_f32_16x16x32_bf16 v[84:87], v[156:159], a[12:15], v[84:87]\n"
"ds_read_b64_tr_b16 v[172:173], v244 offset:40960\n"
"s_add_u32 m0, s63, 49152\n"
"v_mfma_f32_16x16x32_bf16 v[88:91], v[152:155], a[16:19], v[88:91]\n"
"ds_read_b64_tr_b16 v[174:175], v244 offset:45056\n"
"buffer_load_dwordx4 a[128:131], v246, s[28:31], 0 offen offset:0 sc1\n"
"v_mfma_f32_16x16x32_bf16 v[88:91], v[156:159], a[20:23], v[88:91]\n"
"s_add_u32 s72, s72, s54\n"
"s_lshl_b32 s73, s72, 8\n"
"v_mfma_f32_16x16x32_bf16 v[92:95], v[152:155], a[24:27], v[92:95]\n"
"s_lshl_b32 s74, s57, 15\n"
"s_add_u32 s73, s73, s74\n"
"v_mfma_f32_16x16x32_bf16 v[92:95], v[156:159], a[28:31], v[92:95]\n"
"s_add_u32 s73, s73, 39059456\n"
"s_waitcnt lgkmcnt(4)\n"
"v_mfma_f32_16x16x32_bf16 v[128:131], v[160:163], a[0:3], v[128:131]\n"
"buffer_load_dwordx4 a[136:139], v246, s[28:31], 0 offen offset:1024 sc1\n"
"v_lshlrev_b32 v230, 5, v225\n"
"v_mfma_f32_16x16x32_bf16 v[128:131], v[164:167], a[4:7], v[128:131]\n"
"ds_read_b64_tr_b16 v[176:177], v245 offset:32768\n"
"v_mfma_f32_16x16x32_bf16 v[132:135], v[160:163], a[8:11], v[132:135]\n"
"s_waitcnt vmcnt(16)\n"
"v_cmp_ne_u32 vcc, s61, v232\n"
"s_cbranch_vccnz .Lk_slow2\n"
".Lk_back2:\n"
"v_mfma_f32_16x16x32_bf16 v[132:135], v[164:167], a[12:15], v[132:135]\n"
"ds_read_b64_tr_b16 v[178:179], v245 offset:36864\n"
"v_mfma_f32_16x16x32_bf16 v[136:139], v[160:163], a[16:19], v[136:139]\n"
"ds_read_b64_tr_b16 v[180:181], v245 offset:40960\n"
"buffer_load_dwordx4 a[132:135], v246, s[28:31], 0 offen offset:2048 sc1\n"
"v_mfma_f32_16x16x32_bf16 v[136:139], v[164:167], a[20:23], v[136:139]\n"
"ds_read_b64_tr_b16 v[182:183], v245 offset:45056\n"
"v_mfma_f32_16x16x32_bf16 v[140:143], v[160:163], a[24:27], v[140:143]\n"
"v_mfma_f32_16x16x32_bf16 v[140:143], v[164:167], a[28:31], v[140:143]\n"
"s_waitcnt lgkmcnt(4)\n"
"v_mfma_f32_16x16x32_bf16 a[160:163], v[168:171], a[32:35], a[160:163]\n"
"v_mfma_f32_16x16x32_bf16 a[160:163], v[172:175], a[36:39], a[160:163]\n"
"ds_read_b64_tr_b16 v[152:153], v244 offset:33792\n"
"buffer_load_dwordx4 a[140:143], v246, s[28:31], 0 offen offset:3072 sc1\n"
"v_mfma_f32_16x16x32_bf16 a[164:167], v[168:171], a[40:43], a[164:167]\n"
"ds_read_b64_tr_b16 v[154:155], v244 offset:37888\n"
"v_mfma_f32_16x16x32_bf16 a[164:167], v[172:175], a[44:47], a[164:167]\n"
"ds_read_b64_tr_b16 v[156:157], v244 offset:41984\n"
"v_mfma_f32_16x16x32_bf16 a[168:171], v[168:171], a[48:51], a[168:171]\n"
"ds_read_b64_tr_b16 v[158:159], v244 offset:46080\n"
"v_mfma_f32_16x16x32_bf16 a[168:171], v[172:175], a[52:55], a[168:171]\n"
"v_mfma_f32_16x16x32_bf16 a[172:175], v[168:171], a[56:59], a[172:175]\n"
"buffer_load_dwordx4 a[144:147], v247, s[28:31], 0 offen offset:0 sc1\n"
"v_mfma_f32_16x16x32_bf16 a[172:175], v[172:175], a[60:63], a[172:175]\n"
"s_waitcnt lgkmcnt(4)\n"
"v_mfma_f32_16x16x32_bf16 a[176:179], v[176:179], a[32:35], a[176:179]\n"
"v_mfma_f32_16x16x32_bf16 a[176:179], v[180:183], a[36:39], a[176:179]\n"
"ds_read_b64_tr_b16 v[160:161], v245 offset:33792\n"
"v_mfma_f32_16x16x32_bf16 a[180:183], v[176:179], a[40:43], a[180:183]\n"
"ds_read_b64_tr_b16 v[162:163], v245 offset:37888\n"
"buffer_load_dwordx4 a[152:155], v247, s[28:31], 0 offen offset:1024 sc1\n"
"v_mfma_f32_16x16x32_bf16 a[180:183], v[180:183], a[44:47], a[180:183]\n"
"ds_read_b64_tr_b16 v[164:165], v245 offset:41984\n"
"v_mfma_f32_16x16x32_bf16 a[184:187], v[176:179], a[48:51], a[184:187]\n"
"ds_read_b64_tr_b16 v[166:167], v245 offset:46080\n"
"v_mfma_f32_16x16x32_bf16 a[184:187], v[180:183], a[52:55], a[184:187]\n"
"v_mfma_f32_16x16x32_bf16 a[188:191], v[176:179], a[56:59], a[188:191]\n"
"v_mfma_f32_16x16x32_bf16 a[188:191], v[180:183], a[60:63], a[188:191]\n"
"buffer_load_dwordx4 a[148:151], v247, s[28:31], 0 offen offset:2048 sc1\n"
"s_waitcnt lgkmcnt(4)\n"
"v_mfma_f32_16x16x32_bf16 a[192:195], v[152:155], a[32:35], a[192:195]\n"
"v_mfma_f32_16x16x32_bf16 a[192:195], v[156:159], a[36:39], a[192:195]\n"
"ds_read_b64_tr_b16 v[168:169], v244 offset:35840\n"
"v_mfma_f32_16x16x32_bf16 a[196:199], v[152:155], a[40:43], a[196:199]\n"
"ds_read_b64_tr_b16 v[170:171], v244 offset:39936\n"
"v_mfma_f32_16x16x32_bf16 a[196:199], v[156:159], a[44:47], a[196:199]\n"
"ds_read_b64_tr_b16 v[172:173], v244 offset:44032\n"
"v_mfma_f32_16x16x32_bf16 a[200:203], v[152:155], a[48:51], a[200:203]\n"
"ds_read_b64_tr_b16 v[174:175], v244 offset:48128\n"
"buffer_load_dwordx4 a[156:159], v247, s[28:31], 0 offen offset:3072 sc1\n"
"v_mfma_f32_16x16x32_bf16 a[200:203], v[156:159], a[52:55], a[200:203]\n"
"s_mov_b32 s28, s82\n"
"s_lshl_b32 s82, s58, 3\n"
"v_mfma_f32_16x16x32_bf16 a[204:207], v[152:155], a[56:59], a[204:207]\n"
"s_add_u32 s82, s82, s59\n"
"s_lshl_b32 s82, s82, 2\n"
"v_mfma_f32_16x16x32_bf16 a[204:207], v[156:159], a[60:63], a[204:207]\n"
"s_add_u32 s88, s88, s82\n"
"s_waitcnt lgkmcnt(4)\n"
"v_mfma_f32_16x16x32_bf16 a[208:211], v[160:163], a[32:35], a[208:211]\n"
"s_mul_i32 s88, s88, 12\n"
"s_add_u32 s88, s88, s54\n"
"v_mfma_f32_16x16x32_bf16 a[208:211], v[164:167], a[36:39], a[208:211]\n"
"ds_read_b64_tr_b16 v[176:177], v245 offset:35840\n"
"buffer_load_dwordx4 v238, s[20:23], 0 offen offset:0 lds\n"
"v_mfma_f32_16x16x32_bf16 a[212:215], v[160:163], a[40:43], a[212:215]\n"
"ds_read_b64_tr_b16 v[178:179], v245 offset:39936\n"
"s_add_u32 s88, s88, 0\n"
"v_mfma_f32_16x16x32_bf16 a[212:215], v[164:167], a[44:47], a[212:215]\n"
"ds_read_b64_tr_b16 v[180:181], v245 offset:44032\n"
"s_lshl_b32 s89, s88, 12\n"
"v_mfma_f32_16x16x32_bf16 a[216:219], v[160:163], a[48:51], a[216:219]\n"
"ds_read_b64_tr_b16 v[182:183], v245 offset:48128\n"
"s_add_u32 s89, s89, 25165824\n"
"v_mfma_f32_16x16x32_bf16 a[216:219], v[164:167], a[52:55], a[216:219]\n"
"buffer_load_dwordx4 v239, s[20:23], 0 offen offset:1024 lds\n"
"s_lshl_b32 s90, s88, 7\n"
"v_mfma_f32_16x16x32_bf16 a[220:223], v[160:163], a[56:59], a[220:223]\n"
"s_add_u32 s90, s90, 39976960\n"
"s_add_u32 s70, s82, s57\n"
"v_mfma_f32_16x16x32_bf16 a[220:223], v[164:167], a[60:63], a[220:223]\n"
"s_mul_i32 s70, s70, 12\n"
"s_waitcnt lgkmcnt(4)\n"
"v_mfma_f32_16x16x32_bf16 a[224:227], v[168:171], a[32:35], a[224:227]\n"
"s_add_u32 s70, s70, s54\n"
"s_lshl_b32 s71, s70, 7\n"
"v_mfma_f32_16x16x32_bf16 a[224:227], v[172:175], a[36:39], a[224:227]\n"
"ds_read_b64_tr_b16 v[152:153], v244 offset:34816\n"
"s_add_u32 s71, s71, 39976960\n"
"v_mfma_f32_16x16x32_bf16 a[228:231], v[168:171], a[40:43], a[228:231]\n"
"ds_read_b64_tr_b16 v[154:155], v244 offset:38912\n"
"buffer_load_dwordx4 v240, s[20:23], 0 offen offset:2048 lds\n"
"v_mfma_f32_16x16x32_bf16 a[228:231], v[172:175], a[44:47], a[228:231]\n"
"ds_read_b64_tr_b16 v[156:157], v244 offset:43008\n"
"s_lshl_b32 s70, s70, 12\n"
"v_mfma_f32_16x16x32_bf16 a[232:235], v[168:171], a[48:51], a[232:235]\n"
"ds_read_b64_tr_b16 v[158:159], v244 offset:47104\n"
"s_add_u32 s70, s70, 25165824\n"
"v_mfma_f32_16x16x32_bf16 a[232:235], v[172:175], a[52:55], a[232:235]\n"
"s_add_u32 s16, s70, 16384\n"
"s_add_u32 s17, s70, 32768\n"
"v_mfma_f32_16x16x32_bf16 a[236:239], v[168:171], a[56:59], a[236:239]\n"
"v_mfma_f32_16x16x32_bf16 a[236:239], v[172:175], a[60:63], a[236:239]\n"
"buffer_load_dwordx4 v241, s[20:23], 0 offen offset:3072 lds\n"
"s_waitcnt lgkmcnt(4)\n"
"v_mfma_f32_16x16x32_bf16 a[240:243], v[176:179], a[32:35], a[240:243]\n"
"s_lshl_b32 s92, s83, 5\n"
"s_add_u32 s20, s40, s92\n"
"v_mfma_f32_16x16x32_bf16 a[240:243], v[180:183], a[36:39], a[240:243]\n"
"ds_read_b64_tr_b16 v[160:161], v245 offset:34816\n"
"s_add_u32 m0, s63, 65536\n"
"v_mfma_f32_16x16x32_bf16 a[244:247], v[176:179], a[40:43], a[244:247]\n"
"s_waitcnt vmcnt(14)\n"
"buffer_store_dword v219, v220, s[36:39], s43 offen sc1\n"
"v_mfma_f32_16x16x32_bf16 a[244:247], v[180:183], a[44:47], a[244:247]\n"
"ds_read_b64_tr_b16 v[162:163], v245 offset:38912\n"
"buffer_load_dwordx4 v[96:99], v246, s[28:31], 0 offen offset:0 sc1\n"
"v_mfma_f32_16x16x32_bf16 a[248:251], v[176:179], a[48:51], a[248:251]\n"
"ds_read_b64_tr_b16 v[164:165], v245 offset:43008\n"
"v_mfma_f32_16x16x32_bf16 a[248:251], v[180:183], a[52:55], a[248:251]\n"
"ds_read_b64_tr_b16 v[166:167], v245 offset:47104\n"
"v_mfma_f32_16x16x32_bf16 a[252:255], v[176:179], a[56:59], a[252:255]\n"
"v_mfma_f32_16x16x32_bf16 a[252:255], v[180:183], a[60:63], a[252:255]\n"
"s_waitcnt lgkmcnt(4)\n"
"v_mfma_f32_16x16x32_bf16 v[80:83], v[152:155], a[32:35], v[80:83]\n"
"s_barrier\n"
"buffer_load_dwordx4 v[104:107], v246, s[28:31], 0 offen offset:1024 sc1\n"
"v_mfma_f32_16x16x32_bf16 v[80:83], v[156:159], a[36:39], v[80:83]\n"
"ds_read_b64_tr_b16 v[168:169], v242 offset:0\n"
"v_mfma_f32_16x16x32_bf16 v[84:87], v[152:155], a[40:43], v[84:87]\n"
"ds_read_b64_tr_b16 v[170:171], v242 offset:4096\n"
"v_mfma_f32_16x16x32_bf16 v[84:87], v[156:159], a[44:47], v[84:87]\n"
"ds_read_b64_tr_b16 v[172:173], v242 offset:8192\n"
"v_mfma_f32_16x16x32_bf16 v[88:91], v[152:155], a[48:51], v[88:91]\n"
"ds_read_b64_tr_b16 v[174:175], v242 offset:12288\n"
"v_mfma_f32_16x16x32_bf16 v[88:91], v[156:159], a[52:55], v[88:91]\n"
"buffer_load_dwordx4 v[100:103], v246, s[28:31], 0 offen offset:2048 sc1\n"
"v_mfma_f32_16x16x32_bf16 v[92:95], v[152:155], a[56:59], v[92:95]\n"
"v_mfma_f32_16x16x32_bf16 v[92:95], v[156:159], a[60:63], v[92:95]\n"
"s_waitcnt lgkmcnt(4)\n"
"v_mfma_f32_16x16x32_bf16 v[128:131], v[160:163], a[32:35], v[128:131]\n"
"v_mfma_f32_16x16x32_bf16 v[128:131], v[164:167], a[36:39], v[128:131]\n"
"ds_read_b64_tr_b16 v[176:177], v243 offset:0\n"
"buffer_load_dwordx4 v[108:111], v246, s[28:31], 0 offen offset:3072 sc1\n"
"v_mfma_f32_16x16x32_bf16 v[132:135], v[160:163], a[40:43], v[132:135]\n"
"ds_read_b64_tr_b16 v[178:179], v243 offset:4096\n"
"v_mfma_f32_16x16x32_bf16 v[132:135], v[164:167], a[44:47], v[132:135]\n"
"ds_read_b64_tr_b16 v[180:181], v243 offset:8192\n"
"v_mfma_f32_16x16x32_bf16 v[136:139], v[160:163], a[48:51], v[136:139]\n"
"ds_read_b64_tr_b16 v[182:183], v243 offset:12288\n"
"v_mfma_f32_16x16x32_bf16 v[136:139], v[164:167], a[52:55], v[136:139]\n"
"v_mfma_f32_16x16x32_bf16 v[140:143], v[160:163], a[56:59], v[140:143]\n"
"buffer_load_dwordx4 v[112:115], v247, s[28:31], 0 offen offset:0 sc1\n"
"v_mfma_f32_16x16x32_bf16 v[140:143], v[164:167], a[60:63], v[140:143]\n"
"s_waitcnt lgkmcnt(4)\n"
"v_mfma_f32_16x16x32_bf16 a[160:163], v[168:171], a[64:67], a[160:163]\n"
"v_mfma_f32_16x16x32_bf16 a[160:163], v[172:175], a[68:71], a[160:163]\n"
"ds_read_b64_tr_b16 v[152:153], v242 offset:1024\n"
"v_mfma_f32_16x16x32_bf16 a[164:167], v[168:171], a[72:75], a[164:167]\n"
"ds_read_b64_tr_b16 v[154:155], v242 offset:5120\n"
"s_add_u32 s67, s67, 131072\n"
"v_mfma_f32_16x16x32_bf16 a[164:167], v[172:175], a[76:79], a[164:167]\n"
"ds_read_b64_tr_b16 v[156:157], v242 offset:9216\n"
"buffer_load_dwordx4 v[120:123], v247, s[28:31], 0 offen offset:1024 sc1\n"
"v_mfma_f32_16x16x32_bf16 a[168:171], v[168:171], a[80:83], a[168:171]\n"
"ds_read_b64_tr_b16 v[158:159], v242 offset:13312\n"
"v_mfma_f32_16x16x32_bf16 a[168:171], v[172:175], a[84:87], a[168:171]\n"
"v_mfma_f32_16x16x32_bf16 a[172:175], v[168:171], a[88:91], a[172:175]\n"
"buffer_load_dword v232, v233, s[36:39], s67 offen sc1\n"
"v_mfma_f32_16x16x32_bf16 a[172:175], v[172:175], a[92:95], a[172:175]\n"
"s_waitcnt lgkmcnt(4)\n"
"v_mfma_f32_16x16x32_bf16 a[176:179], v[176:179], a[64:67], a[176:179]\n"
"buffer_load_dwordx4 v[116:119], v247, s[28:31], 0 offen offset:2048 sc1\n"
"v_mfma_f32_16x16x32_bf16 a[176:179], v[180:183], a[68:71], a[176:179]\n"
"ds_read_b64_tr_b16 v[160:161], v243 offset:1024\n"
"v_mfma_f32_16x16x32_bf16 a[180:183], v[176:179], a[72:75], a[180:183]\n"
"ds_read_b64_tr_b16 v[162:163], v243 offset:5120\n"
"v_mfma_f32_16x16x32_bf16 a[180:183], v[180:183], a[76:79], a[180:183]\n"
"ds_read_b64_tr_b16 v[164:165], v243 offset:9216\n"
"v_mfma_f32_16x16x32_bf16 a[184:187], v[176:179], a[80:83], a[184:187]\n"
"ds_read_b64_tr_b16 v[166:167], v243 offset:13312\n"
"buffer_load_dwordx4 v[124:127], v247, s[28:31], 0 offen offset:3072 sc1\n"
"v_mfma_f32_16x16x32_bf16 a[184:187], v[180:183], a[84:87], a[184:187]\n"
"s_mov_b32 s28, s83\n"
"v_mfma_f32_16x16x32_bf16 a[188:191], v[176:179], a[88:91], a[188:191]\n"
"v_mfma_f32_16x16x32_bf16 a[188:191], v[180:183], a[92:95], a[188:191]\n"
"s_waitcnt lgkmcnt(4)\n"
"v_mfma_f32_16x16x32_bf16 a[192:195], v[152:155], a[64:67], a[192:195]\n"
"v_mfma_f32_16x16x32_bf16 a[192:195], v[156:159], a[68:71], a[192:195]\n"
"ds_read_b64_tr_b16 v[168:169], v242 offset:3072\n"
"buffer_load_dwordx4 v238, s[20:23], 0 offen offset:0 lds\n"
"v_mfma_f32_16x16x32_bf16 a[196:199], v[152:155], a[72:75], a[196:199]\n"
"ds_read_b64_tr_b16 v[170:171], v242 offset:7168\n"
"v_mfma_f32_16x16x32_bf16 a[196:199], v[156:159], a[76:79], a[196:199]\n"
"ds_read_b64_tr_b16 v[172:173], v242 offset:11264\n"
"v_mfma_f32_16x16x32_bf16 a[200:203], v[152:155], a[80:83], a[200:203]\n"
"ds_read_b64_tr_b16 v[174:175], v242 offset:15360\n"
"v_mfma_f32_16x16x32_bf16 a[200:203], v[156:159], a[84:87], a[200:203]\n"
"v_mfma_f32_16x16x32_bf16 a[204:207], v[152:155], a[88:91], a[204:207]\n"
"buffer_load_dwordx4 v239, s[20:23], 0 offen offset:1024 lds\n"
"v_mfma_f32_16x16x32_bf16 a[204:207], v[156:159], a[92:95], a[204:207]\n"
"s_waitcnt lgkmcnt(4)\n"
"v_mfma_f32_16x16x32_bf16 a[208:211], v[160:163], a[64:67], a[208:211]\n"
"v_mfma_f32_16x16x32_bf16 a[208:211], v[164:167], a[68:71], a[208:211]\n"
"ds_read_b64_tr_b16 v[176:177], v243 offset:3072\n"
"v_mfma_f32_16x16x32_bf16 a[212:215], v[160:163], a[72:75], a[212:215]\n"
"ds_read_b64_tr_b16 v[178:179], v243 offset:7168\n"
"buffer_load_dwordx4 v240, s[20:23], 0 offen offset:2048 lds\n"
"v_mfma_f32_16x16x32_bf16 a[212:215], v[164:167], a[76:79], a[212:215]\n"
"ds_read_b64_tr_b16 v[180:181], v243 offset:11264\n"
"v_mfma_f32_16x16x32_bf16 a[216:219], v[160:163], a[80:83], a[216:219]\n"
"ds_read_b64_tr_b16 v[182:183], v243 offset:15360\n"
"v_mfma_f32_16x16x32_bf16 a[216:219], v[164:167], a[84:87], a[216:219]\n"
"v_mfma_f32_16x16x32_bf16 a[220:223], v[160:163], a[88:91], a[220:223]\n"
"v_mfma_f32_16x16x32_bf16 a[220:223], v[164:167], a[92:95], a[220:223]\n"
"buffer_load_dwordx4 v241, s[20:23], 0 offen offset:3072 lds\n"
"s_waitcnt lgkmcnt(4)\n"
"v_mfma_f32_16x16x32_bf16 a[224:227], v[168:171], a[64:67], a[224:227]\n"
"s_add_u32 s24, s84, s65\n"
"s_add_u32 m0, s63, 81920\n"
"v_mfma_f32_16x16x32_bf16 a[224:227], v[172:175], a[68:71], a[224:227]\n"
"ds_read_b64_tr_b16 v[152:153], v242 offset:2048\n"
"v_mfma_f32_16x16x32_bf16 a[228:231], v[168:171], a[72:75], a[228:231]\n"
"ds_read_b64_tr_b16 v[154:155], v242 offset:6144\n"
"v_mfma_f32_16x16x32_bf16 a[228:231], v[172:175], a[76:79], a[228:231]\n"
"ds_read_b64_tr_b16 v[156:157], v242 offset:10240\n"
"v_mfma_f32_16x16x32_bf16 a[232:235], v[168:171], a[80:83], a[232:235]\n"
"ds_read_b64_tr_b16 v[158:159], v242 offset:14336\n"
"buffer_load_dwordx4 a[0:3], v246, s[28:31], 0 offen offset:0 sc1\n"
"v_mfma_f32_16x16x32_bf16 a[232:235], v[172:175], a[84:87], a[232:235]\n"
"v_mfma_f32_16x16x32_bf16 a[236:239], v[168:171], a[88:91], a[236:239]\n"
"v_mfma_f32_16x16x32_bf16 a[236:239], v[172:175], a[92:95], a[236:239]\n"
"s_waitcnt lgkmcnt(4)\n"
"v_mfma_f32_16x16x32_bf16 a[240:243], v[176:179], a[64:67], a[240:243]\n"
"buffer_load_dwordx4 a[8:11], v246, s[28:31], 0 offen offset:1024 sc1\n"
"v_mfma_f32_16x16x32_bf16 a[240:243], v[180:183], a[68:71], a[240:243]\n"
"ds_read_b64_tr_b16 v[160:161], v243 offset:2048\n"
"v_mfma_f32_16x16x32_bf16 a[244:247], v[176:179], a[72:75], a[244:247]\n"
"ds_read_b64_tr_b16 v[162:163], v243 offset:6144\n"
"v_mfma_f32_16x16x32_bf16 a[244:247], v[180:183], a[76:79], a[244:247]\n"
"ds_read_b64_tr_b16 v[164:165], v243 offset:10240\n"
"v_mfma_f32_16x16x32_bf16 a[248:251], v[176:179], a[80:83], a[248:251]\n"
"ds_read_b64_tr_b16 v[166:167], v243 offset:14336\n"
"v_mfma_f32_16x16x32_bf16 a[248:251], v[180:183], a[84:87], a[248:251]\n"
"buffer_load_dwordx4 a[4:7], v246, s[28:31], 0 offen offset:2048 sc1\n"
"v_mfma_f32_16x16x32_bf16 a[252:255], v[176:179], a[88:91], a[252:255]\n"
"v_mfma_f32_16x16x32_bf16 a[252:255], v[180:183], a[92:95], a[252:255]\n"
"s_lshl_b32 s68, s57, 15\n"
"s_waitcnt lgkmcnt(4)\n"
"v_mfma_f32_16x16x32_bf16 v[80:83], v[152:155], a[64:67], v[80:83]\n"
"s_barrier\n"
"s_and_b32 s69, s28, 0x80000000\n"
"v_mfma_f32_16x16x32_bf16 v[80:83], v[156:159], a[68:71], v[80:83]\n"
"ds_read_b64_tr_b16 v[168:169], v242 offset:16384\n"
"s_add_u32 s68, s68, s69\n"
"v_mfma_f32_16x16x32_bf16 v[84:87], v[152:155], a[72:75], v[84:87]\n"
"ds_read_b64_tr_b16 v[170:171], v242 offset:20480\n"
"buffer_load_dwordx4 a[12:15], v246, s[28:31], 0 offen offset:3072 sc1\n"
"v_mfma_f32_16x16x32_bf16 v[84:87], v[156:159], a[76:79], v[84:87]\n"
"ds_read_b64_tr_b16 v[172:173], v242 offset:24576\n"
"s_add_u32 s68, s68, 229376\n"
"v_mfma_f32_16x16x32_bf16 v[88:91], v[152:155], a[80:83], v[88:91]\n"
"ds_read_b64_tr_b16 v[174:175], v242 offset:28672\n"
"v_mfma_f32_16x16x32_bf16 v[88:91], v[156:159], a[84:87], v[88:91]\n"
"v_mfma_f32_16x16x32_bf16 v[92:95], v[152:155], a[88:91], v[92:95]\n"
"buffer_load_dwordx4 a[16:19], v247, s[28:31], 0 offen offset:0 sc1\n"
"v_mfma_f32_16x16x32_bf16 v[92:95], v[156:159], a[92:95], v[92:95]\n"
"s_waitcnt lgkmcnt(4)\n"
"v_mfma_f32_16x16x32_bf16 v[128:131], v[160:163], a[64:67], v[128:131]\n"
"v_mfma_f32_16x16x32_bf16 v[128:131], v[164:167], a[68:71], v[128:131]\n"
"ds_read_b64_tr_b16 v[176:177], v243 offset:16384\n"
"v_mfma_f32_16x16x32_bf16 v[132:135], v[160:163], a[72:75], v[132:135]\n"
"ds_read_b64_tr_b16 v[178:179], v243 offset:20480\n"
"v_mfma_f32_16x16x32_bf16 v[132:135], v[164:167], a[76:79], v[132:135]\n"
"ds_read_b64_tr_b16 v[180:181], v243 offset:24576\n"
"buffer_load_dwordx4 a[24:27], v247, s[28:31], 0 offen offset:1024 sc1\n"
"v_mfma_f32_16x16x32_bf16 v[136:139], v[160:163], a[80:83], v[136:139]\n"
"ds_read_b64_tr_b16 v[182:183], v243 offset:28672\n"
"v_mfma_f32_16x16x32_bf16 v[136:139], v[164:167], a[84:87], v[136:139]\n"
"v_mfma_f32_16x16x32_bf16 v[140:143], v[160:163], a[88:91], v[140:143]\n"
"v_mfma_f32_16x16x32_bf16 v[140:143], v[164:167], a[92:95], v[140:143]\n"
"s_waitcnt lgkmcnt(4)\n"
"v_mfma_f32_16x16x32_bf16 a[160:163], v[168:171], a[96:99], a[160:163]\n"
"buffer_load_dwordx4 a[20:23], v247, s[28:31], 0 offen offset:2048 sc1\n"
"v_mfma_f32_16x16x32_bf16 a[160:163], v[172:175], a[100:103], a[160:163]\n"
"ds_read_b64_tr_b16 v[152:153], v242 offset:17408\n"
"v_mfma_f32_16x16x32_bf16 a[164:167], v[168:171], a[104:107], a[164:167]\n"
"ds_read_b64_tr_b16 v[154:155], v242 offset:21504\n"
"v_mfma_f32_16x16x32_bf16 a[164:167], v[172:175], a[108:111], a[164:167]\n"
"ds_read_b64_tr_b16 v[156:157], v242 offset:25600\n"
"v_mfma_f32_16x16x32_bf16 a[168:171], v[168:171], a[112:115], a[168:171]\n"
"ds_read_b64_tr_b16 v[158:159], v242 offset:29696\n"
"v_mfma_f32_16x16x32_bf16 a[168:171], v[172:175], a[116:119], a[168:171]\n"
".Lk_h10:\n"
"s_cbranch_execz .Lk_hend\n"
"v_mfma_f32_16x16x32_bf16 a[172:175], v[168:171], a[120:123], a[172:175]\n"
"buffer_load_dwordx4 a[28:31], v247, s[28:31], 0 offen offset:3072 sc1\n"
"v_mfma_f32_16x16x32_bf16 a[172:175], v[172:175], a[124:127], a[172:175]\n"
"s_waitcnt lgkmcnt(4)\n"
"v_mfma_f32_16x16x32_bf16 a[176:179], v[176:179], a[96:99], a[176:179]\n"
"s_waitcnt vmcnt(14)\n"
"v_cmp_ne_u32 vcc, s61, v232\n"
"s_cbranch_vccnz .Lk_slow3\n"
".Lk_back3:\n"
"v_mfma_f32_16x16x32_bf16 a[176:179], v[180:183], a[100:103], a[176:179]\n"
"ds_read_b64_tr_b16 v[160:161], v243 offset:17408\n"
"v_mfma_f32_16x16x32_bf16 a[180:183], v[176:179], a[104:107], a[180:183]\n"
"ds_read_b64_tr_b16 v[162:163], v243 offset:21504\n"
"buffer_load_dwordx4 v12, s[24:27], 0 offen offset:0 lds\n"
"v_mfma_f32_16x16x32_bf16 a[180:183], v[180:183], a[108:111], a[180:183]\n"
"ds_read_b64_tr_b16 v[164:165], v243 offset:25600\n"
"s_mov_b32 s28, s68\n"
"v_mfma_f32_16x16x32_bf16 a[184:187], v[176:179], a[112:115], a[184:187]\n"
"ds_read_b64_tr_b16 v[166:167], v243 offset:29696\n"
"v_mfma_f32_16x16x32_bf16 a[184:187], v[180:183], a[116:119], a[184:187]\n"
"v_mfma_f32_16x16x32_bf16 a[188:191], v[176:179], a[120:123], a[188:191]\n"
"buffer_load_dwordx4 v13, s[24:27], 0 offen offset:1024 lds\n"
"s_add_u32 s24, s24, 30720\n"
"v_mfma_f32_16x16x32_bf16 a[188:191], v[180:183], a[124:127], a[188:191]\n"
"s_waitcnt lgkmcnt(4)\n"
"v_mfma_f32_16x16x32_bf16 a[192:195], v[152:155], a[96:99], a[192:195]\n"
"v_mfma_f32_16x16x32_bf16 a[192:195], v[156:159], a[100:103], a[192:195]\n"
"ds_read_b64_tr_b16 v[168:169], v242 offset:19456\n"
"v_mfma_f32_16x16x32_bf16 a[196:199], v[152:155], a[104:107], a[196:199]\n"
"ds_read_b64_tr_b16 v[170:171], v242 offset:23552\n"
"v_mfma_f32_16x16x32_bf16 a[196:199], v[156:159], a[108:111], a[196:199]\n"
"ds_read_b64_tr_b16 v[172:173], v242 offset:27648\n"
"buffer_load_dwordx4 v12, s[24:27], 0 offen offset:2048 lds\n"
"v_mfma_f32_16x16x32_bf16 a[200:203], v[152:155], a[112:115], a[200:203]\n"
"ds_read_b64_tr_b16 v[174:175], v242 offset:31744\n"
"v_mfma_f32_16x16x32_bf16 a[200:203], v[156:159], a[116:119], a[200:203]\n"
"v_mfma_f32_16x16x32_bf16 a[204:207], v[152:155], a[120:123], a[204:207]\n"
"v_mfma_f32_16x16x32_bf16 a[204:207], v[156:159], a[124:127], a[204:207]\n"
"buffer_load_dwordx4 v13, s[24:27], 0 offen offset:3072 lds\n"
"s_waitcnt lgkmcnt(4)\n"
"v_mfma_f32_16x16x32_bf16 a[208:211], v[160:163], a[96:99], a[208:211]\n"
"s_add_u32 s24, s85, s65\n"
"s_add_u32 m0, s63, 98304\n"
"v_mfma_f32_16x16x32_bf16 a[208:211], v[164:167], a[100:103], a[208:211]\n"
"ds_read_b64_tr_b16 v[176:177], v243 offset:19456\n"
"v_mfma_f32_16x16x32_bf16 a[212:215], v[160:163], a[104:107], a[212:215]\n"
"ds_read_b64_tr_b16 v[178:179], v243 offset:23552\n"
"v_mfma_f32_16x16x32_bf16 a[212:215], v[164:167], a[108:111], a[212:215]\n"
"ds_read_b64_tr_b16 v[180:181], v243 offset:27648\n"
"v_mfma_f32_16x16x32_bf16 a[216:219], v[160:163], a[112:115], a[216:219]\n"
"ds_read_b64_tr_b16 v[182:183], v243 offset:31744\n"
"buffer_load_dwordx4 a[32:35], v246, s[28:31], 0 offen offset:0 sc1\n"
"v_mfma_f32_16x16x32_bf16 a[216:219], v[164:167], a[116:119], a[216:219]\n"
"v_mfma_f32_16x16x32_bf16 a[220:223], v[160:163], a[120:123], a[220:223]\n"
"v_mfma_f32_16x16x32_bf16 a[220:223], v[164:167], a[124:127], a[220:223]\n"
"s_waitcnt lgkmcnt(4)\n"
"v_mfma_f32_16x16x32_bf16 a[224:227], v[168:171], a[96:99], a[224:227]\n"
"v_mfma_f32_16x16x32_bf16 a[224:227], v[172:175], a[100:103], a[224:227]\n"
"ds_read_b64_tr_b16 v[152:153], v242 offset:18432\n"
"buffer_load_dwordx4 a[40:43], v246, s[28:31], 0 offen offset:1024 sc1\n"
"v_mfma_f32_16x16x32_bf16 a[228:231], v[168:171], a[104:107], a[228:231]\n"
"ds_read_b64_tr_b16 v[154:155], v242 offset:22528\n"
"v_mfma_f32_16x16x32_bf16 a[228:231], v[172:175], a[108:111], a[228:231]\n"
"ds_read_b64_tr_b16 v[156:157], v242 offset:26624\n"
"v_mfma_f32_16x16x32_bf16 a[232:235], v[168:171], a[112:115], a[232:235]\n"
"ds_read_b64_tr_b16 v[158:159], v242 offset:30720\n"
"v_mfma_f32_16x16x32_bf16 a[232:235], v[172:175], a[116:119], a[232:235]\n"
"buffer_load_dwordx4 a[36:39], v246, s[28:31], 0 offen offset:2048 sc1\n"
"v_mfma_f32_16x16x32_bf16 a[236:239], v[168:171], a[120:123], a[236:239]\n"
"v_mfma_f32_16x16x32_bf16 a[236:239], v[172:175], a[124:127], a[236:239]\n"
"s_waitcnt lgkmcnt(4)\n"
"v_mfma_f32_16x16x32_bf16 a[240:243], v[176:179], a[96:99], a[240:243]\n"
"s_add_u32 s68, s68, 4096\n"
"v_mfma_f32_16x16x32_bf16 a[240:243], v[180:183], a[100:103], a[240:243]\n"
"ds_read_b64_tr_b16 v[160:161], v243 offset:18432\n"
"v_mfma_f32_16x16x32_bf16 a[244:247], v[176:179], a[104:107], a[244:247]\n"
"ds_read_b64_tr_b16 v[162:163], v243 offset:22528\n"
"buffer_load_dwordx4 a[44:47], v246, s[28:31], 0 offen offset:3072 sc1\n"
"v_mfma_f32_16x16x32_bf16 a[244:247], v[180:183], a[108:111], a[244:247]\n"
"ds_read_b64_tr_b16 v[164:165], v243 offset:26624\n"
"v_mfma_f32_16x16x32_bf16 a[248:251], v[176:179], a[112:115], a[248:251]\n"
"ds_read_b64_tr_b16 v[166:167], v243 offset:30720\n"
"v_mfma_f32_16x16x32_bf16 a[248:251], v[180:183], a[116:119], a[248:251]\n"
"v_mfma_f32_16x16x32_bf16 a[252:255], v[176:179], a[120:123], a[252:255]\n"
"v_mfma_f32_16x16x32_bf16 a[252:255], v[180:183], a[124:127], a[252:255]\n"
"buffer_load_dwordx4 a[48:51], v247, s[28:31], 0 offen offset:0 sc1\n"
"s_waitcnt lgkmcnt(4)\n"
"v_mfma_f32_16x16x32_bf16 v[80:83], v[152:155], a[96:99], v[80:83]\n"
"s_barrier\n"
"v_mfma_f32_16x16x32_bf16 v[80:83], v[156:159], a[100:103], v[80:83]\n"
"ds_read_b64_tr_b16 v[168:169], v242 offset:32768\n"
"v_mfma_f32_16x16x32_bf16 v[84:87], v[152:155], a[104:107], v[84:87]\n"
"ds_read_b64_tr_b16 v[170:171], v242 offset:36864\n"
"v_mfma_f32_16x16x32_bf16 v[84:87], v[156:159], a[108:111], v[84:87]\n"
"ds_read_b64_tr_b16 v[172:173], v242 offset:40960\n"
"v_mfma_f32_16x16x32_bf16 v[88:91], v[152:155], a[112:115], v[88:91]\n"
"ds_read_b64_tr_b16 v[174:175], v242 offset:45056\n"
"buffer_load_dwordx4 a[56:59], v247, s[28:31], 0 offen offset:1024 sc1\n"
"v_mfma_f32_16x16x32_bf16 v[88:91], v[156:159], a[116:119], v[88:91]\n"
"v_mfma_f32_16x16x32_bf16 v[92:95], v[152:155], a[120:123], v[92:95]\n"
"v_mfma_f32_16x16x32_bf16 v[92:95], v[156:159], a[124:127], v[92:95]\n"
"s_waitcnt lgkmcnt(4)\n"
"v_mfma_f32_16x16x32_bf16 v[128:131], v[160:163], a[96:99], v[128:131]\n"
"buffer_load_dwordx4 a[52:55], v247, s[28:31], 0 offen offset:2048 sc1\n"
"v_mfma_f32_16x16x32_bf16 v[128:131], v[164:167], a[100:103], v[128:131]\n"
"ds_read_b64_tr_b16 v[176:177], v243 offset:32768\n"
"v_mfma_f32_16x16x32_bf16 v[132:135], v[160:163], a[104:107], v[132:135]\n"
"ds_read_b64_tr_b16 v[178:179], v243 offset:36864\n"
"v_mfma_f32_16x16x32_bf16 v[132:135], v[164:167], a[108:111], v[132:135]\n"
"ds_read_b64_tr_b16 v[180:181], v243 offset:40960\n"
"v_mfma_f32_16x16x32_bf16 v[136:139], v[160:163], a[112:115], v[136:139]\n"
"ds_read_b64_tr_b16 v[182:183], v243 offset:45056\n"
"v_mfma_f32_16x16x32_bf16 v[136:139], v[164:167], a[116:119], v[136:139]\n"
"buffer_load_dwordx4 v12, s[24:27], 0 offen offset:0 lds\n"
"v_mfma_f32_16x16x32_bf16 v[140:143], v[160:163], a[120:123], v[140:143]\n"
"v_mfma_f32_16x16x32_bf16 v[140:143], v[164:167], a[124:127], v[140:143]\n"
"s_waitcnt lgkmcnt(4)\n"
"v_mfma_f32_16x16x32_bf16 a[160:163], v[168:171], a[128:131], a[160:163]\n"
"v_mfma_f32_16x16x32_bf16 a[160:163], v[172:175], a[132:135], a[160:163]\n"
"ds_read_b64_tr_b16 v[152:153], v242 offset:33792\n"
"v_mfma_f32_16x16x32_bf16 a[164:167], v[168:171], a[136:139], a[164:167]\n"
"ds_read_b64_tr_b16 v[154:155], v242 offset:37888\n"
"buffer_load_dwordx4 a[60:63], v247, s[28:31], 0 offen offset:3072 sc1\n"
"v_mfma_f32_16x16x32_bf16 a[164:167], v[172:175], a[140:143], a[164:167]\n"
"ds_read_b64_tr_b16 v[156:157], v242 offset:41984\n"
"s_mov_b32 s28, s68\n"
"v_mfma_f32_16x16x32_bf16 a[168:171], v[168:171], a[144:147], a[168:171]\n"
"ds_read_b64_tr_b16 v[158:159], v242 offset:46080\n"
"v_mfma_f32_16x16x32_bf16 a[168:171], v[172:175], a[148:151], a[168:171]\n"
"v_mfma_f32_16x16x32_bf16 a[172:175], v[168:171], a[152:155], a[172:175]\n"
"buffer_load_dwordx4 v[16:19], v246, s[28:31], 0 offen offset:0 sc1\n"
"v_mfma_f32_16x16x32_bf16 a[172:175], v[172:175], a[156:159], a[172:175]\n"
"s_waitcnt lgkmcnt(4)\n"
"v_mfma_f32_16x16x32_bf16 a[176:179], v[176:179], a[128:131], a[176:179]\n"
"v_mfma_f32_16x16x32_bf16 a[176:179], v[180:183], a[132:135], a[176:179]\n"
"ds_read_b64_tr_b16 v[160:161], v243 offset:33792\n"
"v_mfma_f32_16x16x32_bf16 a[180:183], v[176:179], a[136:139], a[180:183]\n"
"ds_read_b64_tr_b16 v[162:163], v243 offset:37888\n"
"v_mfma_f32_16x16x32_bf16 a[180:183], v[180:183], a[140:143], a[180:183]\n"
"ds_read_b64_tr_b16 v[164:165], v243 offset:41984\n"
"v_mfma_f32_16x16x32_bf16 a[184:187], v[176:179], a[144:147], a[184:187]\n"
"ds_read_b64_tr_b16 v[166:167], v243 offset:46080\n"
"buffer_load_dwordx4 v[24:27], v246, s[28:31], 0 offen offset:1024 sc1\n"
"v_mfma_f32_16x16x32_bf16 a[184:187], v[180:183], a[148:151], a[184:187]\n"
"v_mfma_f32_16x16x32_bf16 a[188:191], v[176:179], a[152:155], a[188:191]\n"
"v_mfma_f32_16x16x32_bf16 a[188:191], v[180:183], a[156:159], a[188:191]\n"
"s_waitcnt lgkmcnt(4)\n"
"v_mfma_f32_16x16x32_bf16 a[192:195], v[152:155], a[128:131], a[192:195]\n"
"buffer_load_dwordx4 v13, s[24:27], 0 offen offset:1024 lds\n"
"s_add_u32 s24, s24, 30720\n"
"v_mfma_f32_16x16x32_bf16 a[192:195], v[156:159], a[132:135], a[192:195]\n"
"ds_read_b64_tr_b16 v[168:169], v242 offset:35840\n"
"v_mfma_f32_16x16x32_bf16 a[196:199], v[152:155], a[136:139], a[196:199]\n"
"ds_read_b64_tr_b16 v[170:171], v242 offset:39936\n"
"v_mfma_f32_16x16x32_bf16 a[196:199], v[156:159], a[140:143], a[196:199]\n"
"ds_read_b64_tr_b16 v[172:173], v242 offset:44032\n"
"v_mfma_f32_16x16x32_bf16 a[200:203], v[152:155], a[144:147], a[200:203]\n"
"ds_read_b64_tr_b16 v[174:175], v242 offset:48128\n"
"buffer_load_dwordx4 v[20:23], v246, s[28:31], 0 offen offset:2048 sc1\n"
"v_mfma_f32_16x16x32_bf16 a[200:203], v[156:159], a[148:151], a[200:203]\n"
"s_add_u32 s68, s68, 12288\n"
"v_mfma_f32_16x16x32_bf16 a[204:207], v[152:155], a[152:155], a[204:207]\n"
"v_mfma_f32_16x16x32_bf16 a[204:207], v[156:159], a[156:159], a[204:207]\n"
"s_waitcnt lgkmcnt(4)\n"
"v_mfma_f32_16x16x32_bf16 a[208:211], v[160:163], a[128:131], a[208:211]\n"
"v_mfma_f32_16x16x32_bf16 a[208:211], v[164:167], a[132:135], a[208:211]\n"
"ds_read_b64_tr_b16 v[176:177], v243 offset:35840\n"
"buffer_load_dwordx4 v[28:31], v246, s[28:31], 0 offen offset:3072 sc1\n"
"v_mfma_f32_16x16x32_bf16 a[212:215], v[160:163], a[136:139], a[212:215]\n"
"ds_read_b64_tr_b16 v[178:179], v243 offset:39936\n"
"v_mfma_f32_16x16x32_bf16 a[212:215], v[164:167], a[140:143], a[212:215]\n"
"ds_read_b64_tr_b16 v[180:181], v243 offset:44032\n"
"v_mfma_f32_16x16x32_bf16 a[216:219], v[160:163], a[144:147], a[216:219]\n"
"ds_read_b64_tr_b16 v[182:183], v243 offset:48128\n"
"v_mfma_f32_16x16x32_bf16 a[216:219], v[164:167], a[148:151], a[216:219]\n"
"v_mfma_f32_16x16x32_bf16 a[220:223], v[160:163], a[152:155], a[220:223]\n"
"buffer_load_dwordx4 v[32:35], v247, s[28:31], 0 offen offset:0 sc1\n"
"v_mfma_f32_16x16x32_bf16 a[220:223], v[164:167], a[156:159], a[220:223]\n"
"s_waitcnt lgkmcnt(4)\n"
"v_mfma_f32_16x16x32_bf16 a[224:227], v[168:171], a[128:131], a[224:227]\n"
"v_mfma_f32_16x16x32_bf16 a[224:227], v[172:175], a[132:135], a[224:227]\n"
"ds_read_b64_tr_b16 v[152:153], v242 offset:34816\n"
"v_mfma_f32_16x16x32_bf16 a[228:231], v[168:171], a[136:139], a[228:231]\n"
"ds_read_b64_tr_b16 v[154:155], v242 offset:38912\n"
"v_mfma_f32_16x16x32_bf16 a[228:231], v[172:175], a[140:143], a[228:231]\n"
"ds_read_b64_tr_b16 v[156:157], v242 offset:43008\n"
"buffer_load_dwordx4 v[40:43], v247, s[28:31], 0 offen offset:1024 sc1\n"
"v_mfma_f32_16x16x32_bf16 a[232:235], v[168:171], a[144:147], a[232:235]\n"
"ds_read_b64_tr_b16 v[158:159], v242 offset:47104\n"
"v_mfma_f32_16x16x32_bf16 a[232:235], v[172:175], a[148:151], a[232:235]\n"
"v_mfma_f32_16x16x32_bf16 a[236:239], v[168:171], a[152:155], a[236:239]\n"
"v_mfma_f32_16x16x32_bf16 a[236:239], v[172:175], a[156:159], a[236:239]\n"
"s_waitcnt lgkmcnt(4)\n"
"v_mfma_f32_16x16x32_bf16 a[240:243], v[176:179], a[128:131], a[240:243]\n"
"buffer_load_dwordx4 v[36:39], v247, s[28:31], 0 offen offset:2048 sc1\n"
"v_mfma_f32_16x16x32_bf16 a[240:243], v[180:183], a[132:135], a[240:243]\n"
"ds_read_b64_tr_b16 v[160:161], v243 offset:34816\n"
"v_mfma_f32_16x16x32_bf16 a[244:247], v[176:179], a[136:139], a[244:247]\n"
"ds_read_b64_tr_b16 v[162:163], v243 offset:38912\n"
"v_mfma_f32_16x16x32_bf16 a[244:247], v[180:183], a[140:143], a[244:247]\n"
"ds_read_b64_tr_b16 v[164:165], v243 offset:43008\n"
"v_mfma_f32_16x16x32_bf16 a[248:251], v[176:179], a[144:147], a[248:251]\n"
"ds_read_b64_tr_b16 v[166:167], v243 offset:47104\n"
"buffer_load_dwordx4 v12, s[24:27], 0 offen offset:2048 lds\n"
"v_mfma_f32_16x16x32_bf16 a[248:251], v[180:183], a[148:151], a[248:251]\n"
"v_mfma_f32_16x16x32_bf16 a[252:255], v[176:179], a[152:155], a[252:255]\n"
"v_mfma_f32_16x16x32_bf16 a[252:255], v[180:183], a[156:159], a[252:255]\n"
"s_waitcnt lgkmcnt(4)\n"
"v_mfma_f32_16x16x32_bf16 v[80:83], v[152:155], a[128:131], v[80:83]\n"
"s_barrier\n"
"v_mfma_f32_16x16x32_bf16 v[80:83], v[156:159], a[132:135], v[80:83]\n"
"ds_read_b64_tr_b16 v[168:169], v242 offset:49152\n"
"buffer_load_dwordx4 v[44:47], v247, s[28:31], 0 offen offset:3072 sc1\n"
"v_mfma_f32_16x16x32_bf16 v[84:87], v[152:155], a[136:139], v[84:87]\n"
"ds_read_b64_tr_b16 v[170:171], v242 offset:53248\n"
"s_mov_b32 s28, s68\n"
"v_mfma_f32_16x16x32_bf16 v[84:87], v[156:159], a[140:143], v[84:87]\n"
"ds_read_b64_tr_b16 v[172:173], v242 offset:57344\n"
"v_mfma_f32_16x16x32_bf16 v[88:91], v[152:155], a[144:147], v[88:91]\n"
"ds_read_b64_tr_b16 v[174:175], v242 offset:61440\n"
"v_mfma_f32_16x16x32_bf16 v[88:91], v[156:159], a[148:151], v[88:91]\n"
"buffer_load_dwordx4 a[64:67], v246, s[28:31], 0 offen offset:0 sc1\n"
"v_mfma_f32_16x16x32_bf16 v[92:95], v[152:155], a[152:155], v[92:95]\n"
"s_add_u32 s68, s68, 4096\n"
"v_mfma_f32_16x16x32_bf16 v[92:95], v[156:159], a[156:159], v[92:95]\n"
"s_waitcnt lgkmcnt(4)\n"
"v_mfma_f32_16x16x32_bf16 v[128:131], v[160:163], a[128:131], v[128:131]\n"
"v_mfma_f32_16x16x32_bf16 v[128:131], v[164:167], a[132:135], v[128:131]\n"
"ds_read_b64_tr_b16 v[176:177], v243 offset:49152\n"
"v_mfma_f32_16x16x32_bf16 v[132:135], v[160:163], a[136:139], v[132:135]\n"
"ds_read_b64_tr_b16 v[178:179], v243 offset:53248\n"
"buffer_load_dwordx4 a[72:75], v246, s[28:31], 0 offen offset:1024 sc1\n"
"v_mfma_f32_16x16x32_bf16 v[132:135], v[164:167], a[140:143], v[132:135]\n"
"ds_read_b64_tr_b16 v[180:181], v243 offset:57344\n"
"v_mfma_f32_16x16x32_bf16 v[136:139], v[160:163], a[144:147], v[136:139]\n"
"ds_read_b64_tr_b16 v[182:183], v243 offset:61440\n"
"v_mfma_f32_16x16x32_bf16 v[136:139], v[164:167], a[148:151], v[136:139]\n"
"v_mfma_f32_16x16x32_bf16 v[140:143], v[160:163], a[152:155], v[140:143]\n"
"v_mfma_f32_16x16x32_bf16 v[140:143], v[164:167], a[156:159], v[140:143]\n"
"buffer_load_dwordx4 a[68:71], v246, s[28:31], 0 offen offset:2048 sc1\n"
"s_waitcnt vmcnt(38) lgkmcnt(4)\n"
"v_mfma_f32_16x16x32_bf16 a[160:163], v[168:171], v[96:99], a[160:163]\n"
"v_mfma_f32_16x16x32_bf16 a[160:163], v[172:175], v[100:103], a[160:163]\n"
"ds_read_b64_tr_b16 v[152:153], v242 offset:50176\n"
"v_mfma_f32_16x16x32_bf16 a[164:167], v[168:171], v[104:107], a[164:167]\n"
"ds_read_b64_tr_b16 v[154:155], v242 offset:54272\n"
"v_mfma_f32_16x16x32_bf16 a[164:167], v[172:175], v[108:111], a[164:167]\n"
"ds_read_b64_tr_b16 v[156:157], v242 offset:58368\n"
"buffer_load_dwordx4 a[76:79], v246, s[28:31], 0 offen offset:3072 sc1\n"
"v_mfma_f32_16x16x32_bf16 a[168:171], v[168:171], v[112:115], a[168:171]\n"
"ds_read_b64_tr_b16 v[158:159], v242 offset:62464\n"
"v_mfma_f32_16x16x32_bf16 a[168:171], v[172:175], v[116:119], a[168:171]\n"
"v_mfma_f32_16x16x32_bf16 a[172:175], v[168:171], v[120:123], a[172:175]\n"
"v_mfma_f32_16x16x32_bf16 a[172:175], v[172:175], v[124:127], a[172:175]\n"
"s_waitcnt lgkmcnt(4)\n"
"v_mfma_f32_16x16x32_bf16 a[176:179], v[176:179], v[96:99], a[176:179]\n"
"buffer_load_dwordx4 a[80:83], v247, s[28:31], 0 offen offset:0 sc1\n"
"v_mfma_f32_16x16x32_bf16 a[176:179], v[180:183], v[100:103], a[176:179]\n"
"ds_read_b64_tr_b16 v[160:161], v243 offset:50176\n"
"v_mfma_f32_16x16x32_bf16 a[180:183], v[176:179], v[104:107], a[180:183]\n"
"ds_read_b64_tr_b16 v[162:163], v243 offset:54272\n"
"v_mfma_f32_16x16x32_bf16 a[180:183], v[180:183], v[108:111], a[180:183]\n"
"ds_read_b64_tr_b16 v[164:165], v243 offset:58368\n"
"v_mfma_f32_16x16x32_bf16 a[184:187], v[176:179], v[112:115], a[184:187]\n"
"ds_read_b64_tr_b16 v[166:167], v243 offset:62464\n"
"v_mfma_f32_16x16x32_bf16 a[184:187], v[180:183], v[116:119], a[184:187]\n"
"buffer_load_dwordx4 a[88:91], v247, s[28:31], 0 offen offset:1024 sc1\n"
"v_mfma_f32_16x16x32_bf16 a[188:191], v[176:179], v[120:123], a[188:191]\n"
"v_mfma_f32_16x16x32_bf16 a[188:191], v[180:183], v[124:127], a[188:191]\n"
"s_waitcnt lgkmcnt(4)\n"
"v_mfma_f32_16x16x32_bf16 a[192:195], v[152:155], v[96:99], a[192:195]\n"
"v_mfma_f32_16x16x32_bf16 a[192:195], v[156:159], v[100:103], a[192:195]\n"
"ds_read_b64_tr_b16 v[168:169], v242 offset:52224\n"
"v_mfma_f32_16x16x32_bf16 a[196:199], v[152:155], v[104:107], a[196:199]\n"
"ds_read_b64_tr_b16 v[170:171], v242 offset:56320\n"
"buffer_load_dwordx4 a[84:87], v247, s[28:31], 0 offen offset:2048 sc1\n"
"v_mfma_f32_16x16x32_bf16 a[196:199], v[156:159], v[108:111], a[196:199]\n"
"ds_read_b64_tr_b16 v[172:173], v242 offset:60416\n"
"v_mfma_f32_16x16x32_bf16 a[200:203], v[152:155], v[112:115], a[200:203]\n"
"ds_read_b64_tr_b16 v[174:175], v242 offset:64512\n"
"v_mfma_f32_16x16x32_bf16 a[200:203], v[156:159], v[116:119], a[200:203]\n"
"v_mfma_f32_16x16x32_bf16 a[204:207], v[152:155], v[120:123], a[204:207]\n"
"buffer_load_dwordx4 a[92:95], v247, s[28:31], 0 offen offset:3072 sc1\n"
"s_mov_b32 s28, s68\n"
"v_mfma_f32_16x16x32_bf16 a[204:207], v[156:159], v[124:127], a[204:207]\n"
"s_waitcnt lgkmcnt(4)\n"
"v_mfma_f32_16x16x32_bf16 a[208:211], v[160:163], v[96:99], a[208:211]\n"
"v_mfma_f32_16x16x32_bf16 a[208:211], v[164:167], v[100:103], a[208:211]\n"
"ds_read_b64_tr_b16 v[176:177], v243 offset:52224\n"
"v_mfma_f32_16x16x32_bf16 a[212:215], v[160:163], v[104:107], a[212:215]\n"
"ds_read_b64_tr_b16 v[178:179], v243 offset:56320\n"
"v_mfma_f32_16x16x32_bf16 a[212:215], v[164:167], v[108:111], a[212:215]\n"
"ds_read_b64_tr_b16 v[180:181], v243 offset:60416\n"
"buffer_load_dwordx4 a[96:99], v246, s[28:31], 0 offen offset:0 sc1\n"
"v_mfma_f32_16x16x32_bf16 a[216:219], v[160:163], v[112:115], a[216:219]\n"
"ds_read_b64_tr_b16 v[182:183], v243 offset:64512\n"
"v_mfma_f32_16x16x32_bf16 a[216:219], v[164:167], v[116:119], a[216:219]\n"
"v_mfma_f32_16x16x32_bf16 a[220:223], v[160:163], v[120:123], a[220:223]\n"
"s_add_u32 s67, s67, 131072\n"
"v_mfma_f32_16x16x32_bf16 a[220:223], v[164:167], v[124:127], a[220:223]\n"
"s_waitcnt lgkmcnt(4)\n"
"v_mfma_f32_16x16x32_bf16 a[224:227], v[168:171], v[96:99], a[224:227]\n"
"buffer_load_dwordx4 a[104:107], v246, s[28:31], 0 offen offset:1024 sc1\n"
"v_mfma_f32_16x16x32_bf16 a[224:227], v[172:175], v[100:103], a[224:227]\n"
"ds_read_b64_tr_b16 v[152:153], v242 offset:51200\n"
"v_mfma_f32_16x16x32_bf16 a[228:231], v[168:171], v[104:107], a[228:231]\n"
"ds_read_b64_tr_b16 v[154:155], v242 offset:55296\n"
"v_mfma_f32_16x16x32_bf16 a[228:231], v[172:175], v[108:111], a[228:231]\n"
"ds_read_b64_tr_b16 v[156:157], v242 offset:59392\n"
"v_mfma_f32_16x16x32_bf16 a[232:235], v[168:171], v[112:115], a[232:235]\n"
"buffer_load_dword v232, v233, s[36:39], s67 offen sc1\n"
"v_mfma_f32_16x16x32_bf16 a[232:235], v[172:175], v[116:119], a[232:235]\n"
"ds_read_b64_tr_b16 v[158:159], v242 offset:63488\n"
"buffer_load_dwordx4 a[100:103], v246, s[28:31], 0 offen offset:2048 sc1\n"
"v_mfma_f32_16x16x32_bf16 a[236:239], v[168:171], v[120:123], a[236:239]\n"
"v_mfma_f32_16x16x32_bf16 a[236:239], v[172:175], v[124:127], a[236:239]\n"
"s_waitcnt lgkmcnt(4)\n"
"v_mfma_f32_16x16x32_bf16 a[240:243], v[176:179], v[96:99], a[240:243]\n"
"v_mfma_f32_16x16x32_bf16 a[240:243], v[180:183], v[100:103], a[240:243]\n"
"ds_read_b64_tr_b16 v[160:161], v243 offset:51200\n"
"buffer_load_dwordx4 v13, s[24:27], 0 offen offset:3072 lds\n"
"v_mfma_f32_16x16x32_bf16 a[244:247], v[176:179], v[104:107], a[244:247]\n"
"ds_read_b64_tr_b16 v[162:163], v243 offset:55296\n"
"s_add_u32 s24, s86, s65\n"
"v_mfma_f32_16x16x32_bf16 a[244:247], v[180:183], v[108:111], a[244:247]\n"
"ds_read_b64_tr_b16 v[164:165], v243 offset:59392\n"
"s_add_u32 m0, s63, 0\n"
"v_mfma_f32_16x16x32_bf16 a[248:251], v[176:179], v[112:115], a[248:251]\n"
"ds_read_b64_tr_b16 v[166:167], v243 offset:63488\n"
"v_mfma_f32_16x16x32_bf16 a[248:251], v[180:183], v[116:119], a[248:251]\n"
"v_mfma_f32_16x16x32_bf16 a[252:255], v[176:179], v[120:123], a[252:255]\n"
"buffer_load_dwordx4 a[108:111], v246, s[28:31], 0 offen offset:3072 sc1\n"
"v_mfma_f32_16x16x32_bf16 a[252:255], v[180:183], v[124:127], a[252:255]\n"
"s_waitcnt lgkmcnt(4)\n"
"v_mfma_f32_16x16x32_bf16 v[80:83], v[152:155], v[96:99], v[80:83]\n"
"s_waitcnt vmcnt(45)\n"
"s_barrier\n"
"v_mfma_f32_16x16x32_bf16 v[80:83], v[156:159], v[100:103], v[80:83]\n"
"ds_read_b64_tr_b16 v[168:169], v244 offset:0\n"
"v_mfma_f32_16x16x32_bf16 v[84:87], v[152:155], v[104:107], v[84:87]\n"
"ds_read_b64_tr_b16 v[170:171], v244 offset:4096\n"
"buffer_load_dwordx4 v12, s[24:27], 0 offen offset:0 lds\n"
"v_mfma_f32_16x16x32_bf16 v[84:87], v[156:159], v[108:111], v[84:87]\n"
"ds_read_b64_tr_b16 v[172:173], v244 offset:8192\n"
"v_mfma_f32_16x16x32_bf16 v[88:91], v[152:155], v[112:115], v[88:91]\n"
"ds_read_b64_tr_b16 v[174:175], v244 offset:12288\n"
"v_mfma_f32_16x16x32_bf16 v[88:91], v[156:159], v[116:119], v[88:91]\n"
"v_mfma_f32_16x16x32_bf16 v[92:95], v[152:155], v[120:123], v[92:95]\n"
"v_mfma_f32_16x16x32_bf16 v[92:95], v[156:159], v[124:127], v[92:95]\n"
"buffer_load_dwordx4 a[112:115], v247, s[28:31], 0 offen offset:0 sc1\n"
"s_waitcnt lgkmcnt(4)\n"
"v_mfma_f32_16x16x32_bf16 v[128:131], v[160:163], v[96:99], v[128:131]\n"
"v_mfma_f32_16x16x32_bf16 v[128:131], v[164:167], v[100:103], v[128:131]\n"
"ds_read_b64_tr_b16 v[176:177], v245 offset:0\n"
"v_mfma_f32_16x16x32_bf16 v[132:135], v[160:163], v[104:107], v[132:135]\n"
"ds_read_b64_tr_b16 v[178:179], v245 offset:4096\n"
"v_mfma_f32_16x16x32_bf16 v[132:135], v[164:167], v[108:111], v[132:135]\n"
"ds_read_b64_tr_b16 v[180:181], v245 offset:8192\n"
"v_mfma_f32_16x16x32_bf16 v[136:139], v[160:163], v[112:115], v[136:139]\n"
"ds_read_b64_tr_b16 v[182:183], v245 offset:12288\n"
"buffer_load_dwordx4 a[120:123], v247, s[28:31], 0 offen offset:1024 sc1\n"
"v_mfma_f32_16x16x32_bf16 v[136:139], v[164:167], v[116:119], v[136:139]\n"
"v_mfma_f32_16x16x32_bf16 v[140:143], v[160:163], v[120:123], v[140:143]\n"
"v_mfma_f32_16x16x32_bf16 v[140:143], v[164:167], v[124:127], v[140:143]\n"
"s_waitcnt vmcnt(40) lgkmcnt(4)\n"
"v_mfma_f32_16x16x32_bf16 a[160:163], v[168:171], a[0:3], a[160:163]\n"
"v_mfma_f32_16x16x32_bf16 a[160:163], v[172:175], a[4:7], a[160:163]\n"
"ds_read_b64_tr_b16 v[152:153], v244 offset:1024\n"
"buffer_load_dwordx4 v13, s[24:27], 0 offen offset:1024 lds\n"
"v_mfma_f32_16x16x32_bf16 a[164:167], v[168:171], a[8:11], a[164:167]\n"
"ds_read_b64_tr_b16 v[154:155], v244 offset:5120\n"
"s_add_u32 s24, s24, 30720\n"
"v_mfma_f32_16x16x32_bf16 a[164:167], v[172:175], a[12:15], a[164:167]\n"
"ds_read_b64_tr_b16 v[156:157], v244 offset:9216\n"
"v_mfma_f32_16x16x32_bf16 a[168:171], v[168:171], a[16:19], a[168:171]\n"
"ds_read_b64_tr_b16 v[158:159], v244 offset:13312\n"
"v_mfma_f32_16x16x32_bf16 a[168:171], v[172:175], a[20:23], a[168:171]\n"
"buffer_load_dwordx4 a[116:119], v247, s[28:31], 0 offen offset:2048 sc1\n"
"v_mfma_f32_16x16x32_bf16 a[172:175], v[168:171], a[24:27], a[172:175]\n"
"v_mfma_f32_16x16x32_bf16 a[172:175], v[172:175], a[28:31], a[172:175]\n"
"s_waitcnt lgkmcnt(4)\n"
"v_mfma_f32_16x16x32_bf16 a[176:179], v[176:179], a[0:3], a[176:179]\n"
"v_mfma_f32_16x16x32_bf16 a[176:179], v[180:183], a[4:7], a[176:179]\n"
"ds_read_b64_tr_b16 v[160:161], v245 offset:1024\n"
"v_mfma_f32_16x16x32_bf16 a[180:183], v[176:179], a[8:11], a[180:183]\n"
"ds_read_b64_tr_b16 v[162:163], v245 offset:5120\n"
"buffer_load_dwordx4 v12, s[24:27], 0 offen offset:2048 lds\n"
"v_mfma_f32_16x16x32_bf16 a[180:183], v[180:183], a[12:15], a[180:183]\n"
"ds_read_b64_tr_b16 v[164:165], v245 offset:9216\n"
"s_sub_u32 s68, s68, 12288\n"
"v_mfma_f32_16x16x32_bf16 a[184:187], v[176:179], a[16:19], a[184:187]\n"
"ds_read_b64_tr_b16 v[166:167], v245 offset:13312\n"
"v_mfma_f32_16x16x32_bf16 a[184:187], v[180:183], a[20:23], a[184:187]\n"
"v_mfma_f32_16x16x32_bf16 a[188:191], v[176:179], a[24:27], a[188:191]\n"
"v_mfma_f32_16x16x32_bf16 a[188:191], v[180:183], a[28:31], a[188:191]\n"
"buffer_load_dwordx4 a[124:127], v247, s[28:31], 0 offen offset:3072 sc1\n"
"s_waitcnt lgkmcnt(4)\n"
"v_mfma_f32_16x16x32_bf16 a[192:195], v[152:155], a[0:3], a[192:195]\n"
"v_mfma_f32_16x16x32_bf16 a[192:195], v[156:159], a[4:7], a[192:195]\n"
"ds_read_b64_tr_b16 v[168:169], v244 offset:3072\n"
"v_mfma_f32_16x16x32_bf16 a[196:199], v[152:155], a[8:11], a[196:199]\n"
"ds_read_b64_tr_b16 v[170:171], v244 offset:7168\n"
"v_mfma_f32_16x16x32_bf16 a[196:199], v[156:159], a[12:15], a[196:199]\n"
"ds_read_b64_tr_b16 v[172:173], v244 offset:11264\n"
"buffer_load_dwordx4 v13, s[24:27], 0 offen offset:3072 lds\n"
"v_mfma_f32_16x16x32_bf16 a[200:203], v[152:155], a[16:19], a[200:203]\n"
"ds_read_b64_tr_b16 v[174:175], v244 offset:15360\n"
"s_add_u32 s24, s84, s66\n"
"v_mfma_f32_16x16x32_bf16 a[200:203], v[156:159], a[20:23], a[200:203]\n"
"s_add_u32 m0, s63, 16384\n"
"v_mfma_f32_16x16x32_bf16 a[204:207], v[152:155], a[24:27], a[204:207]\n"
"v_mfma_f32_16x16x32_bf16 a[204:207], v[156:159], a[28:31], a[204:207]\n"
"s_waitcnt lgkmcnt(4)\n"
"v_mfma_f32_16x16x32_bf16 a[208:211], v[160:163], a[0:3], a[208:211]\n"
"buffer_load_dwordx4 v12, s[24:27], 0 offen offset:0 lds\n"
"v_mfma_f32_16x16x32_bf16 a[208:211], v[164:167], a[4:7], a[208:211]\n"
"ds_read_b64_tr_b16 v[176:177], v245 offset:3072\n"
"v_mfma_f32_16x16x32_bf16 a[212:215], v[160:163], a[8:11], a[212:215]\n"
"ds_read_b64_tr_b16 v[178:179], v245 offset:7168\n"
"v_mfma_f32_16x16x32_bf16 a[212:215], v[164:167], a[12:15], a[212:215]\n"
"ds_read_b64_tr_b16 v[180:181], v245 offset:11264\n"
"v_mfma_f32_16x16x32_bf16 a[216:219], v[160:163], a[16:19], a[216:219]\n"
"ds_read_b64_tr_b16 v[182:183], v245 offset:15360\n"
"v_mfma_f32_16x16x32_bf16 a[216:219], v[164:167], a[20:23], a[216:219]\n"
"buffer_load_dwordx4 v13, s[24:27], 0 offen offset:1024 lds\n"
"s_add_u32 s24, s24, 30720\n"
"v_mfma_f32_16x16x32_bf16 a[220:223], v[160:163], a[24:27], a[220:223]\n"
"v_mfma_f32_16x16x32_bf16 a[220:223], v[164:167], a[28:31], a[220:223]\n"
"s_waitcnt lgkmcnt(4)\n"
"v_mfma_f32_16x16x32_bf16 a[224:227], v[168:171], a[0:3], a[224:227]\n"
"v_mfma_f32_16x16x32_bf16 a[224:227], v[172:175], a[4:7], a[224:227]\n"
"ds_read_b64_tr_b16 v[152:153], v244 offset:2048\n"
"buffer_load_dwordx4 v12, s[24:27], 0 offen offset:2048 lds\n"
"v_mfma_f32_16x16x32_bf16 a[228:231], v[168:171], a[8:11], a[228:231]\n"
"ds_read_b64_tr_b16 v[154:155], v244 offset:6144\n"
"v_mfma_f32_16x16x32_bf16 a[228:231], v[172:175], a[12:15], a[228:231]\n"
"ds_read_b64_tr_b16 v[156:157], v244 offset:10240\n"
"v_mfma_f32_16x16x32_bf16 a[232:235], v[168:171], a[16:19], a[232:235]\n"
"ds_read_b64_tr_b16 v[158:159], v244 offset:14336\n"
"v_mfma_f32_16x16x32_bf16 a[232:235], v[172:175], a[20:23], a[232:235]\n"
"s_waitcnt vmcnt(14)\n"
"v_cmp_ne_u32 vcc, s61, v232\n"
"s_cbranch_vccnz .Lk_slow4\n"
".Lk_back4:\n"
"v_mfma_f32_16x16x32_bf16 a[236:239], v[168:171], a[24:27], a[236:239]\n"
"buffer_load_dwordx4 v13, s[24:27], 0 offen offset:3072 lds\n"
"s_add_u32 s24, s85, s66\n"
"v_mfma_f32_16x16x32_bf16 a[236:239], v[172:175], a[28:31], a[236:239]\n"
"s_add_u32 m0, s63, 32768\n"
"s_waitcnt lgkmcnt(4)\n"
"v_mfma_f32_16x16x32_bf16 a[240:243], v[176:179], a[0:3], a[240:243]\n"
"s_mov_b32 s28, s68\n"
"v_mfma_f32_16x16x32_bf16 a[240:243], v[180:183], a[4:7], a[240:243]\n"
"ds_read_b64_tr_b16 v[160:161], v245 offset:2048\n"
"v_mfma_f32_16x16x32_bf16 a[244:247], v[176:179], a[8:11], a[244:247]\n"
"ds_read_b64_tr_b16 v[162:163], v245 offset:6144\n"
"v_mfma_f32_16x16x32_bf16 a[244:247], v[180:183], a[12:15], a[244:247]\n"
"ds_read_b64_tr_b16 v[164:165], v245 offset:10240\n"
"buffer_load_dwordx4 v[48:51], v246, s[28:31], 0 offen offset:0 sc1\n"
"v_mfma_f32_16x16x32_bf16 a[248:251], v[176:179], a[16:19], a[248:251]\n"
"ds_read_b64_tr_b16 v[166:167], v245 offset:14336\n"
"v_mfma_f32_16x16x32_bf16 a[248:251], v[180:183], a[20:23], a[248:251]\n"
"v_mfma_f32_16x16x32_bf16 a[252:255], v[176:179], a[24:27], a[252:255]\n"
"v_mfma_f32_16x16x32_bf16 a[252:255], v[180:183], a[28:31], a[252:255]\n"
"s_waitcnt lgkmcnt(4)\n"
"v_mfma_f32_16x16x32_bf16 v[80:83], v[152:155], a[0:3], v[80:83]\n"
"s_barrier\n"
"buffer_load_dwordx4 v[56:59], v246, s[28:31], 0 offen offset:1024 sc1\n"
"v_mfma_f32_16x16x32_bf16 v[80:83], v[156:159], a[4:7], v[80:83]\n"
"ds_read_b128 v[168:171], v223 offset:16384\n"
"v_mfma_f32_16x16x32_bf16 v[84:87], v[152:155], a[8:11], v[84:87]\n"
"ds_read_b128 v[172:175], v224 offset:16384\n"
"v_mfma_f32_16x16x32_bf16 v[84:87], v[156:159], a[12:15], v[84:87]\n"
"ds_read_b128 v[176:179], v223 offset:20480\n"
"v_mfma_f32_16x16x32_bf16 v[88:91], v[152:155], a[16:19], v[88:91]\n"
"ds_read_b128 v[180:183], v224 offset:20480\n"
"buffer_load_dwordx4 v12, s[24:27], 0 offen offset:0 lds\n"
"v_mfma_f32_16x16x32_bf16 v[88:91], v[156:159], a[20:23], v[88:91]\n"
"v_mfma_f32_16x16x32_bf16 v[92:95], v[152:155], a[24:27], v[92:95]\n"
"v_mfma_f32_16x16x32_bf16 v[92:95], v[156:159], a[28:31], v[92:95]\n"
"s_waitcnt lgkmcnt(4)\n"
"v_mfma_f32_16x16x32_bf16 v[128:131], v[160:163], a[0:3], v[128:131]\n"
"v_mfma_f32_16x16x32_bf16 v[128:131], v[164:167], a[4:7], v[128:131]\n"
"buffer_load_dwordx4 v[52:55], v246, s[28:31], 0 offen offset:2048 sc1\n"
"v_mfma_f32_16x16x32_bf16 v[132:135], v[160:163], a[8:11], v[132:135]\n"
"v_mfma_f32_16x16x32_bf16 v[132:135], v[164:167], a[12:15], v[132:135]\n"
"v_mfma_f32_16x16x32_bf16 v[136:139], v[160:163], a[16:19], v[136:139]\n"
"v_mfma_f32_16x16x32_bf16 v[136:139], v[164:167], a[20:23], v[136:139]\n"
"v_mfma_f32_16x16x32_bf16 v[140:143], v[160:163], a[24:27], v[140:143]\n"
"buffer_load_dwordx4 v[60:63], v246, s[28:31], 0 offen offset:3072 sc1\n"
"v_mfma_f32_16x16x32_bf16 v[140:143], v[164:167], a[28:31], v[140:143]\n"
"s_add_u32 s68, s68, 4096\n"
"s_waitcnt lgkmcnt(0)\n"
"v_mfma_f32_16x16x32_bf16 a[160:163], v[168:171], a[32:35], a[160:163]\n"
"v_mfma_f32_16x16x32_bf16 a[160:163], v[172:175], a[36:39], a[160:163]\n"
"ds_read_b128 v[152:155], v223 offset:18432\n"
"v_mfma_f32_16x16x32_bf16 a[160:163], v[176:179], v[16:19], a[160:163]\n"
"ds_read_b128 v[156:159], v224 offset:18432\n"
"v_mfma_f32_16x16x32_bf16 a[160:163], v[180:183], v[20:23], a[160:163]\n"
"ds_read_b128 v[160:163], v223 offset:22528\n"
"buffer_load_dwordx4 v[64:67], v247, s[28:31], 0 offen offset:0 sc1\n"
"v_mfma_f32_16x16x32_bf16 a[164:167], v[168:171], a[40:43], a[164:167]\n"
"ds_read_b128 v[164:167], v224 offset:22528\n"
"v_mfma_f32_16x16x32_bf16 a[164:167], v[172:175], a[44:47], a[164:167]\n"
"v_mfma_f32_16x16x32_bf16 a[164:167], v[176:179], v[24:27], a[164:167]\n"
"v_mfma_f32_16x16x32_bf16 a[164:167], v[180:183], v[28:31], a[164:167]\n"
"buffer_load_dwordx4 v13, s[24:27], 0 offen offset:1024 lds\n"
"s_add_u32 s24, s24, 30720\n"
"v_mfma_f32_16x16x32_bf16 a[168:171], v[168:171], a[48:51], a[168:171]\n"
"v_mfma_f32_16x16x32_bf16 a[168:171], v[172:175], a[52:55], a[168:171]\n"
"v_mfma_f32_16x16x32_bf16 a[168:171], v[176:179], v[32:35], a[168:171]\n"
"v_mfma_f32_16x16x32_bf16 a[168:171], v[180:183], v[36:39], a[168:171]\n"
"v_mfma_f32_16x16x32_bf16 a[172:175], v[168:171], a[56:59], a[172:175]\n"
"buffer_load_dwordx4 v[72:75], v247, s[28:31], 0 offen offset:1024 sc1\n"
"v_mfma_f32_16x16x32_bf16 a[172:175], v[172:175], a[60:63], a[172:175]\n"
"v_mfma_f32_16x16x32_bf16 a[172:175], v[176:179], v[40:43], a[172:175]\n"
"v_mfma_f32_16x16x32_bf16 a[172:175], v[180:183], v[44:47], a[172:175]\n"
"s_waitcnt lgkmcnt(0)\n"
"v_mfma_f32_16x16x32_bf16 a[176:179], v[152:155], a[32:35], a[176:179]\n"
"buffer_load_dwordx4 v[68:71], v247, s[28:31], 0 offen offset:2048 sc1\n"
"v_mfma_f32_16x16x32_bf16 a[176:179], v[156:159], a[36:39], a[176:179]\n"
"ds_read_b128 v[168:171], v223 offset:24576\n"
"v_mfma_f32_16x16x32_bf16 a[176:179], v[160:163], v[16:19], a[176:179]\n"
"ds_read_b128 v[172:175], v224 offset:24576\n"
"v_mfma_f32_16x16x32_bf16 a[176:179], v[164:167], v[20:23], a[176:179]\n"
"ds_read_b128 v[176:179], v223 offset:28672\n"
"v_mfma_f32_16x16x32_bf16 a[180:183], v[152:155], a[40:43], a[180:183]\n"
"ds_read_b128 v[180:183], v224 offset:28672\n"
"v_mfma_f32_16x16x32_bf16 a[180:183], v[156:159], a[44:47], a[180:183]\n"
"buffer_load_dwordx4 v12, s[24:27], 0 offen offset:2048 lds\n"
"v_mfma_f32_16x16x32_bf16 a[180:183], v[160:163], v[24:27], a[180:183]\n"
"v_mfma_f32_16x16x32_bf16 a[180:183], v[164:167], v[28:31], a[180:183]\n"
"v_mfma_f32_16x16x32_bf16 a[184:187], v[152:155], a[48:51], a[184:187]\n"
"v_mfma_f32_16x16x32_bf16 a[184:187], v[156:159], a[52:55], a[184:187]\n"
"v_mfma_f32_16x16x32_bf16 a[184:187], v[160:163], v[32:35], a[184:187]\n"
"buffer_load_dwordx4 v[76:79], v247, s[28:31], 0 offen offset:3072 sc1\n"
"s_mov_b32 s28, s68\n"
"v_mfma_f32_16x16x32_bf16 a[184:187], v[164:167], v[36:39], a[184:187]\n"
"v_mfma_f32_16x16x32_bf16 a[188:191], v[152:155], a[56:59], a[188:191]\n"
"v_mfma_f32_16x16x32_bf16 a[188:191], v[156:159], a[60:63], a[188:191]\n"
"v_mfma_f32_16x16x32_bf16 a[188:191], v[160:163], v[40:43], a[188:191]\n"
".Lk_h11:\n"
"s_cbranch_execz .Lk_hend\n"
"v_mfma_f32_16x16x32_bf16 a[188:191], v[164:167], v[44:47], a[188:191]\n"
"buffer_load_dwordx4 a[128:131], v246, s[28:31], 0 offen offset:0 sc1\n"
"s_waitcnt vmcnt(17) lgkmcnt(0)\n"
"v_mfma_f32_16x16x32_bf16 a[160:163], v[168:171], a[64:67], a[160:163]\n"
"v_mfma_f32_16x16x32_bf16 a[160:163], v[172:175], a[68:71], a[160:163]\n"
"ds_read_b128 v[152:155], v223 offset:26624\n"
"v_mfma_f32_16x16x32_bf16 a[160:163], v[176:179], a[96:99], a[160:163]\n"
"ds_read_b128 v[156:159], v224 offset:26624\n"
"v_mfma_f32_16x16x32_bf16 a[160:163], v[180:183], a[100:103], a[160:163]\n"
"ds_read_b128 v[160:163], v223 offset:30720\n"
"v_mfma_f32_16x16x32_bf16 a[164:167], v[168:171], a[72:75], a[164:167]\n"
"ds_read_b128 v[164:167], v224 offset:30720\n"
"buffer_load_dwordx4 a[136:139], v246, s[28:31], 0 offen offset:1024 sc1\n"
"v_mfma_f32_16x16x32_bf16 a[164:167], v[172:175], a[76:79], a[164:167]\n"
"v_mfma_f32_16x16x32_bf16 a[164:167], v[176:179], a[104:107], a[164:167]\n"
"v_mfma_f32_16x16x32_bf16 a[164:167], v[180:183], a[108:111], a[164:167]\n"
"v_mfma_f32_16x16x32_bf16 a[168:171], v[168:171], a[80:83], a[168:171]\n"
"buffer_load_dwordx4 v13, s[24:27], 0 offen offset:3072 lds\n"
"s_add_u32 s24, s86, s66\n"
"v_mfma_f32_16x16x32_bf16 a[168:171], v[172:175], a[84:87], a[168:171]\n"
"s_add_u32 m0, s63, 49152\n"
"v_mfma_f32_16x16x32_bf16 a[168:171], v[176:179], a[112:115], a[168:171]\n"
"v_mfma_f32_16x16x32_bf16 a[168:171], v[180:183], a[116:119], a[168:171]\n"
"v_mfma_f32_16x16x32_bf16 a[172:175], v[168:171], a[88:91], a[172:175]\n"
"v_mfma_f32_16x16x32_bf16 a[172:175], v[172:175], a[92:95], a[172:175]\n"
"buffer_load_dwordx4 a[132:135], v246, s[28:31], 0 offen offset:2048 sc1\n"
"v_mfma_f32_16x16x32_bf16 a[172:175], v[176:179], a[120:123], a[172:175]\n"
"v_mfma_f32_16x16x32_bf16 a[172:175], v[180:183], a[124:127], a[172:175]\n"
"s_waitcnt lgkmcnt(0)\n"
"v_mfma_f32_16x16x32_bf16 a[176:179], v[152:155], a[64:67], a[176:179]\n"
"s_barrier\n"
"v_mfma_f32_16x16x32_bf16 a[176:179], v[156:159], a[68:71], a[176:179]\n"
"ds_read_b128 v[168:171], v223 offset:32768\n"
"v_mfma_f32_16x16x32_bf16 a[176:179], v[160:163], a[96:99], a[176:179]\n"
"ds_read_b128 v[172:175], v224 offset:32768\n"
"buffer_load_dwordx4 a[140:143], v246, s[28:31], 0 offen offset:3072 sc1\n"
"v_mfma_f32_16x16x32_bf16 a[176:179], v[164:167], a[100:103], a[176:179]\n"
"ds_read_b128 v[176:179], v223 offset:36864\n"
"v_mfma_f32_16x16x32_bf16 a[180:183], v[152:155], a[72:75], a[180:183]\n"
"ds_read_b128 v[180:183], v224 offset:36864\n"
"v_mfma_f32_16x16x32_bf16 a[180:183], v[156:159], a[76:79], a[180:183]\n"
"v_mfma_f32_16x16x32_bf16 a[180:183], v[160:163], a[104:107], a[180:183]\n"
"buffer_load_dwordx4 v12, s[24:27], 0 offen offset:0 lds\n"
"s_add_u32 s68, s68, 12288\n"
"v_mfma_f32_16x16x32_bf16 a[180:183], v[164:167], a[108:111], a[180:183]\n"
"v_mfma_f32_16x16x32_bf16 a[184:187], v[152:155], a[80:83], a[184:187]\n"
"v_mfma_f32_16x16x32_bf16 a[184:187], v[156:159], a[84:87], a[184:187]\n"
"v_mfma_f32_16x16x32_bf16 a[184:187], v[160:163], a[112:115], a[184:187]\n"
"v_mfma_f32_16x16x32_bf16 a[184:187], v[164:167], a[116:119], a[184:187]\n"
"buffer_load_dwordx4 a[144:147], v247, s[28:31], 0 offen offset:0 sc1\n"
"v_mfma_f32_16x16x32_bf16 a[188:191], v[152:155], a[88:91], a[188:191]\n"
"v_mfma_f32_16x16x32_bf16 a[188:191], v[156:159], a[92:95], a[188:191]\n"
"v_mfma_f32_16x16x32_bf16 a[188:191], v[160:163], a[120:123], a[188:191]\n"
"v_mfma_f32_16x16x32_bf16 a[188:191], v[164:167], a[124:127], a[188:191]\n"
"s_waitcnt lgkmcnt(0)\n"
"v_mfma_f32_16x16x32_bf16 a[192:195], v[168:171], a[32:35], a[192:195]\n"
"buffer_load_dwordx4 a[152:155], v247, s[28:31], 0 offen offset:1024 sc1\n"
"v_mfma_f32_16x16x32_bf16 a[192:195], v[172:175], a[36:39], a[192:195]\n"
"ds_read_b128 v[152:155], v223 offset:34816\n"
"v_mfma_f32_16x16x32_bf16 a[192:195], v[176:179], v[16:19], a[192:195]\n"
"ds_read_b128 v[156:159], v224 offset:34816\n"
"v_mfma_f32_16x16x32_bf16 a[192:195], v[180:183], v[20:23], a[192:195]\n"
"ds_read_b128 v[160:163], v223 offset:38912\n"
"v_mfma_f32_16x16x32_bf16 a[196:199], v[168:171], a[40:43], a[196:199]\n"
"ds_read_b128 v[164:167], v224 offset:38912\n"
"buffer_load_dwordx4 v13, s[24:27], 0 offen offset:1024 lds\n"
"v_mfma_f32_16x16x32_bf16 a[196:199], v[172:175], a[44:47], a[196:199]\n"
"s_add_u32 s24, s24, 30720\n"
"v_mfma_f32_16x16x32_bf16 a[196:199], v[176:179], v[24:27], a[196:199]\n"
"v_mfma_f32_16x16x32_bf16 a[196:199], v[180:183], v[28:31], a[196:199]\n"
"v_mfma_f32_16x16x32_bf16 a[200:203], v[168:171], a[48:51], a[200:203]\n"
"v_mfma_f32_16x16x32_bf16 a[200:203], v[172:175], a[52:55], a[200:203]\n"
"buffer_load_dwordx4 a[148:151], v247, s[28:31], 0 offen offset:2048 sc1\n"
"v_mfma_f32_16x16x32_bf16 a[200:203], v[176:179], v[32:35], a[200:203]\n"
"v_mfma_f32_16x16x32_bf16 a[200:203], v[180:183], v[36:39], a[200:203]\n"
"v_mfma_f32_16x16x32_bf16 a[204:207], v[168:171], a[56:59], a[204:207]\n"
"v_mfma_f32_16x16x32_bf16 a[204:207], v[172:175], a[60:63], a[204:207]\n"
"v_mfma_f32_16x16x32_bf16 a[204:207], v[176:179], v[40:43], a[204:207]\n"
"buffer_load_dwordx4 a[156:159], v247, s[28:31], 0 offen offset:3072 sc1\n"
"v_mfma_f32_16x16x32_bf16 a[204:207], v[180:183], v[44:47], a[204:207]\n"
"s_waitcnt lgkmcnt(0)\n"
"v_mfma_f32_16x16x32_bf16 a[208:211], v[152:155], a[32:35], a[208:211]\n"
"v_mfma_f32_16x16x32_bf16 a[208:211], v[156:159], a[36:39], a[208:211]\n"
"ds_read_b128 v[168:171], v223 offset:40960\n"
"s_mov_b32 s28, s68\n"
"v_mfma_f32_16x16x32_bf16 a[208:211], v[160:163], v[16:19], a[208:211]\n"
"ds_read_b128 v[172:175], v224 offset:40960\n"
"buffer_load_dwordx4 v12, s[24:27], 0 offen offset:2048 lds\n"
"v_mfma_f32_16x16x32_bf16 a[208:211], v[164:167], v[20:23], a[208:211]\n"
"ds_read_b128 v[176:179], v223 offset:45056\n"
"v_mfma_f32_16x16x32_bf16 a[212:215], v[152:155], a[40:43], a[212:215]\n"
"ds_read_b128 v[180:183], v224 offset:45056\n"
"s_add_u32 s68, s68, 4096\n"
"v_mfma_f32_16x16x32_bf16 a[212:215], v[156:159], a[44:47], a[212:215]\n"
"v_mfma_f32_16x16x32_bf16 a[212:215], v[160:163], v[24:27], a[212:215]\n"
"v_mfma_f32_16x16x32_bf16 a[212:215], v[164:167], v[28:31], a[212:215]\n"
"buffer_load_dwordx4 v[96:99], v246, s[28:31], 0 offen offset:0 sc1\n"
"v_mfma_f32_16x16x32_bf16 a[216:219], v[152:155], a[48:51], a[216:219]\n"
"v_mfma_f32_16x16x32_bf16 a[216:219], v[156:159], a[52:55], a[216:219]\n"
"v_mfma_f32_16x16x32_bf16 a[216:219], v[160:163], v[32:35], a[216:219]\n"
"v_mfma_f32_16x16x32_bf16 a[216:219], v[164:167], v[36:39], a[216:219]\n"
"v_mfma_f32_16x16x32_bf16 a[220:223], v[152:155], a[56:59], a[220:223]\n"
"buffer_load_dwordx4 v[104:107], v246, s[28:31], 0 offen offset:1024 sc1\n"
"v_mfma_f32_16x16x32_bf16 a[220:223], v[156:159], a[60:63], a[220:223]\n"
"v_mfma_f32_16x16x32_bf16 a[220:223], v[160:163], v[40:43], a[220:223]\n"
"v_mfma_f32_16x16x32_bf16 a[220:223], v[164:167], v[44:47], a[220:223]\n"
"s_waitcnt lgkmcnt(0)\n"
"v_mfma_f32_16x16x32_bf16 a[192:195], v[168:171], a[64:67], a[192:195]\n"
"buffer_load_dwordx4 v[100:103], v246, s[28:31], 0 offen offset:2048 sc1\n"
"v_mfma_f32_16x16x32_bf16 a[192:195], v[172:175], a[68:71], a[192:195]\n"
"ds_read_b128 v[152:155], v223 offset:43008\n"
"v_mfma_f32_16x16x32_bf16 a[192:195], v[176:179], a[96:99], a[192:195]\n"
"ds_read_b128 v[156:159], v224 offset:43008\n"
"v_mfma_f32_16x16x32_bf16 a[192:195], v[180:183], a[100:103], a[192:195]\n"
"ds_read_b128 v[160:163], v223 offset:47104\n"
"v_mfma_f32_16x16x32_bf16 a[196:199], v[168:171], a[72:75], a[196:199]\n"
"ds_read_b128 v[164:167], v224 offset:47104\n"
"v_mfma_f32_16x16x32_bf16 a[196:199], v[172:175], a[76:79], a[196:199]\n"
"buffer_load_dwordx4 v[108:111], v246, s[28:31], 0 offen offset:3072 sc1\n"
"v_mfma_f32_16x16x32_bf16 a[196:199], v[176:179], a[104:107], a[196:199]\n"
"v_mfma_f32_16x16x32_bf16 a[196:199], v[180:183], a[108:111], a[196:199]\n"
"v_mfma_f32_16x16x32_bf16 a[200:203], v[168:171], a[80:83], a[200:203]\n"
"v_mfma_f32_16x16x32_bf16 a[200:203], v[172:175], a[84:87], a[200:203]\n"
"v_mfma_f32_16x16x32_bf16 a[200:203], v[176:179], a[112:115], a[200:203]\n"
"buffer_load_dwordx4 v[112:115], v247, s[28:31], 0 offen offset:0 sc1\n"
"v_mfma_f32_16x16x32_bf16 a[200:203], v[180:183], a[116:119], a[200:203]\n"
"v_mfma_f32_16x16x32_bf16 a[204:207], v[168:171], a[88:91], a[204:207]\n"
"v_mfma_f32_16x16x32_bf16 a[204:207], v[172:175], a[92:95], a[204:207]\n"
"v_mfma_f32_16x16x32_bf16 a[204:207], v[176:179], a[120:123], a[204:207]\n"
"buffer_load_dwordx4 v[120:123], v247, s[28:31], 0 offen offset:1024 sc1\n"
"v_mfma_f32_16x16x32_bf16 a[204:207], v[180:183], a[124:127], a[204:207]\n"
"s_waitcnt lgkmcnt(0)\n"
"v_mfma_f32_16x16x32_bf16 a[208:211], v[152:155], a[64:67], a[208:211]\n"
"s_waitcnt vmcnt(33)\n"
"s_barrier\n"
"v_mfma_f32_16x16x32_bf16 a[208:211], v[156:159], a[68:71], a[208:211]\n"
"ds_read_b128 v[168:171], v221 offset:0\n"
"v_mfma_f32_16x16x32_bf16 a[208:211], v[160:163], a[96:99], a[208:211]\n"
"ds_read_b128 v[172:175], v222 offset:0\n"
"v_mfma_f32_16x16x32_bf16 a[208:211], v[164:167], a[100:103], a[208:211]\n"
"ds_read_b128 v[176:179], v221 offset:4096\n"
"buffer_load_dwordx4 v[116:119], v247, s[28:31], 0 offen offset:2048 sc1\n"
"v_mfma_f32_16x16x32_bf16 a[212:215], v[152:155], a[72:75], a[212:215]\n"
"ds_read_b128 v[180:183], v222 offset:4096\n"
"v_mfma_f32_16x16x32_bf16 a[212:215], v[156:159], a[76:79], a[212:215]\n"
"v_mfma_f32_16x16x32_bf16 a[212:215], v[160:163], a[104:107], a[212:215]\n"
"v_mfma_f32_16x16x32_bf16 a[212:215], v[164:167], a[108:111], a[212:215]\n"
"v_mfma_f32_16x16x32_bf16 a[216:219], v[152:155], a[80:83], a[216:219]\n"
"buffer_load_dwordx4 v[124:127], v247, s[28:31], 0 offen offset:3072 sc1\n"
"s_mov_b32 s28, s68\n"
"v_mfma_f32_16x16x32_bf16 a[216:219], v[156:159], a[84:87], a[216:219]\n"
"v_mfma_f32_16x16x32_bf16 a[216:219], v[160:163], a[112:115], a[216:219]\n"
"v_mfma_f32_16x16x32_bf16 a[216:219], v[164:167], a[116:119], a[216:219]\n"
"v_mfma_f32_16x16x32_bf16 a[220:223], v[152:155], a[88:91], a[220:223]\n"
"v_mfma_f32_16x16x32_bf16 a[220:223], v[156:159], a[92:95], a[220:223]\n"
"buffer_load_dwordx4 a[0:3], v246, s[28:31], 0 offen offset:0 sc1\n"
"v_mfma_f32_16x16x32_bf16 a[220:223], v[160:163], a[120:123], a[220:223]\n"
"v_mfma_f32_16x16x32_bf16 a[220:223], v[164:167], a[124:127], a[220:223]\n"
"s_waitcnt lgkmcnt(0)\n"
"v_mfma_f32_16x16x32_bf16 v[80:83], v[168:171], a[32:35], v[80:83]\n"
"v_mfma_f32_16x16x32_bf16 v[80:83], v[172:175], a[36:39], v[80:83]\n"
"ds_read_b128 v[152:155], v221 offset:2048\n"
"buffer_load_dwordx4 a[8:11], v246, s[28:31], 0 offen offset:1024 sc1\n"
"v_mfma_f32_16x16x32_bf16 v[80:83], v[176:179], v[16:19], v[80:83]\n"
"ds_read_b128 v[156:159], v222 offset:2048\n"
"v_mfma_f32_16x16x32_bf16 v[80:83], v[180:183], v[20:23], v[80:83]\n"
"ds_read_b128 v[160:163], v221 offset:6144\n"
"v_mfma_f32_16x16x32_bf16 v[84:87], v[168:171], a[40:43], v[84:87]\n"
"ds_read_b128 v[164:167], v222 offset:6144\n"
"v_mfma_f32_16x16x32_bf16 v[84:87], v[172:175], a[44:47], v[84:87]\n"
"v_mfma_f32_16x16x32_bf16 v[84:87], v[176:179], v[24:27], v[84:87]\n"
"buffer_load_dwordx4 a[4:7], v246, s[28:31], 0 offen offset:2048 sc1\n"
"v_mfma_f32_16x16x32_bf16 v[84:87], v[180:183], v[28:31], v[84:87]\n"
"v_mfma_f32_16x16x32_bf16 v[88:91], v[168:171], a[48:51], v[88:91]\n"
"v_mfma_f32_16x16x32_bf16 v[88:91], v[172:175], a[52:55], v[88:91]\n"
"v_mfma_f32_16x16x32_bf16 v[88:91], v[176:179], v[32:35], v[88:91]\n"
"v_mfma_f32_16x16x32_bf16 v[88:91], v[180:183], v[36:39], v[88:91]\n"
"buffer_load_dwordx4 a[12:15], v246, s[28:31], 0 offen offset:3072 sc1\n"
"v_mfma_f32_16x16x32_bf16 v[92:95], v[168:171], a[56:59], v[92:95]\n"
"v_mfma_f32_16x16x32_bf16 v[92:95], v[172:175], a[60:63], v[92:95]\n"
"v_mfma_f32_16x16x32_bf16 v[92:95], v[176:179], v[40:43], v[92:95]\n"
"v_mfma_f32_16x16x32_bf16 v[92:95], v[180:183], v[44:47], v[92:95]\n"
"buffer_load_dwordx4 a[16:19], v247, s[28:31], 0 offen offset:0 sc1\n"
"s_waitcnt lgkmcnt(0)\n"
"v_mfma_f32_16x16x32_bf16 v[128:131], v[152:155], a[32:35], v[128:131]\n"
"v_mfma_f32_16x16x32_bf16 v[128:131], v[156:159], a[36:39], v[128:131]\n"
"ds_read_b128 v[168:171], v221 offset:8192\n"
"v_mfma_f32_16x16x32_bf16 v[128:131], v[160:163], v[16:19], v[128:131]\n"
"ds_read_b128 v[172:175], v222 offset:8192\n"
"v_mfma_f32_16x16x32_bf16 v[128:131], v[164:167], v[20:23], v[128:131]\n"
"ds_read_b128 v[176:179], v221 offset:12288\n"
"v_mfma_f32_16x16x32_bf16 v[132:135], v[152:155], a[40:43], v[132:135]\n"
"ds_read_b128 v[180:183], v222 offset:12288\n"
"buffer_load_dwordx4 a[24:27], v247, s[28:31], 0 offen offset:1024 sc1\n"
"v_mfma_f32_16x16x32_bf16 v[132:135], v[156:159], a[44:47], v[132:135]\n"
"v_mfma_f32_16x16x32_bf16 v[132:135], v[160:163], v[24:27], v[132:135]\n"
"v_mfma_f32_16x16x32_bf16 v[132:135], v[164:167], v[28:31], v[132:135]\n"
"v_mfma_f32_16x16x32_bf16 v[136:139], v[152:155], a[48:51], v[136:139]\n"
"v_mfma_f32_16x16x32_bf16 v[136:139], v[156:159], a[52:55], v[136:139]\n"
"buffer_load_dwordx4 a[20:23], v247, s[28:31], 0 offen offset:2048 sc1\n"
"v_mfma_f32_16x16x32_bf16 v[136:139], v[160:163], v[32:35], v[136:139]\n"
"v_mfma_f32_16x16x32_bf16 v[136:139], v[164:167], v[36:39], v[136:139]\n"
"v_mfma_f32_16x16x32_bf16 v[140:143], v[152:155], a[56:59], v[140:143]\n"
"v_mfma_f32_16x16x32_bf16 v[140:143], v[156:159], a[60:63], v[140:143]\n"
"buffer_load_dwordx4 a[28:31], v247, s[28:31], 0 offen offset:3072 sc1\n"
"v_mfma_f32_16x16x32_bf16 v[140:143], v[160:163], v[40:43], v[140:143]\n"
"v_mfma_f32_16x16x32_bf16 v[140:143], v[164:167], v[44:47], v[140:143]\n"
"s_waitcnt lgkmcnt(0)\n"
"v_mfma_f32_16x16x32_bf16 v[80:83], v[168:171], a[64:67], v[80:83]\n"
"v_mfma_f32_16x16x32_bf16 v[80:83], v[172:175], a[68:71], v[80:83]\n"
"ds_read_b128 v[152:155], v221 offset:10240\n"
"v_mfma_f32_16x16x32_bf16 v[80:83], v[176:179], a[96:99], v[80:83]\n"
"ds_read_b128 v[156:159], v222 offset:10240\n"
"buffer_load_dwordx4 v13, s[24:27], 0 offen offset:3072 lds\n"
"v_mfma_f32_16x16x32_bf16 v[80:83], v[180:183], a[100:103], v[80:83]\n"
"ds_read_b128 v[160:163], v221 offset:14336\n"
"s_add_u32 s24, s87, s65\n"
"v_mfma_f32_16x16x32_bf16 v[84:87], v[168:171], a[72:75], v[84:87]\n"
"ds_read_b128 v[164:167], v222 offset:14336\n"
"s_add_u32 m0, s63, 65536\n"
"v_mfma_f32_16x16x32_bf16 v[84:87], v[172:175], a[76:79], v[84:87]\n"
"v_mfma_f32_16x16x32_bf16 v[84:87], v[176:179], a[104:107], v[84:87]\n"
"v_mfma_f32_16x16x32_bf16 v[84:87], v[180:183], a[108:111], v[84:87]\n"
"buffer_load_dwordx4 v12, s[24:27], 0 offen offset:0 lds\n"
"v_mfma_f32_16x16x32_bf16 v[88:91], v[168:171], a[80:83], v[88:91]\n"
"v_mfma_f32_16x16x32_bf16 v[88:91], v[172:175], a[84:87], v[88:91]\n"
"v_mfma_f32_16x16x32_bf16 v[88:91], v[176:179], a[112:115], v[88:91]\n"
"v_mfma_f32_16x16x32_bf16 v[88:91], v[180:183], a[116:119], v[88:91]\n"
"v_mfma_f32_16x16x32_bf16 v[92:95], v[168:171], a[88:91], v[92:95]\n"
"buffer_load_dwordx4 v13, s[24:27], 0 offen offset:1024 lds\n"
"s_add_u32 s24, s24, 30720\n"
"v_mfma_f32_16x16x32_bf16 v[92:95], v[172:175], a[92:95], v[92:95]\n"
"v_mfma_f32_16x16x32_bf16 v[92:95], v[176:179], a[120:123], v[92:95]\n"
"v_mfma_f32_16x16x32_bf16 v[92:95], v[180:183], a[124:127], v[92:95]\n"
"s_waitcnt lgkmcnt(0)\n"
"v_mfma_f32_16x16x32_bf16 v[128:131], v[152:155], a[64:67], v[128:131]\n"
"s_waitcnt vmcnt(42)\n"
"s_barrier\n"
"v_mfma_f32_16x16x32_bf16 v[128:131], v[156:159], a[68:71], v[128:131]\n"
"ds_read_b128 v[168:171], v221 offset:16384\n"
"buffer_load_dwordx4 v12, s[24:27], 0 offen offset:2048 lds\n"
"v_mfma_f32_16x16x32_bf16 v[128:131], v[160:163], a[96:99], v[128:131]\n"
"ds_read_b128 v[172:175], v222 offset:16384\n"
"v_mfma_f32_16x16x32_bf16 v[128:131], v[164:167], a[100:103], v[128:131]\n"
"ds_read_b128 v[176:179], v221 offset:20480\n"
"v_mfma_f32_16x16x32_bf16 v[132:135], v[152:155], a[72:75], v[132:135]\n"
"ds_read_b128 v[180:183], v222 offset:20480\n"
"v_mfma_f32_16x16x32_bf16 v[132:135], v[156:159], a[76:79], v[132:135]\n"
"buffer_load_dwordx4 v13, s[24:27], 0 offen offset:3072 lds\n"
"s_add_u32 s24, s87, s66\n"
"v_mfma_f32_16x16x32_bf16 v[132:135], v[160:163], a[104:107], v[132:135]\n"
"s_add_u32 m0, s63, 81920\n"
"v_mfma_f32_16x16x32_bf16 v[132:135], v[164:167], a[108:111], v[132:135]\n"
"v_mfma_f32_16x16x32_bf16 v[136:139], v[152:155], a[80:83], v[136:139]\n"
"v_mfma_f32_16x16x32_bf16 v[136:139], v[156:159], a[84:87], v[136:139]\n"
"v_mfma_f32_16x16x32_bf16 v[136:139], v[160:163], a[112:115], v[136:139]\n"
"buffer_load_dwordx4 v12, s[24:27], 0 offen offset:0 lds\n"
"v_mfma_f32_16x16x32_bf16 v[136:139], v[164:167], a[116:119], v[136:139]\n"
"v_mfma_f32_16x16x32_bf16 v[140:143], v[152:155], a[88:91], v[140:143]\n"
"v_mfma_f32_16x16x32_bf16 v[140:143], v[156:159], a[92:95], v[140:143]\n"
"v_mfma_f32_16x16x32_bf16 v[140:143], v[160:163], a[120:123], v[140:143]\n"
"buffer_load_dwordx4 v[192:195], v230, s[36:39], s73 offen offset:0 sc1\n"
"v_mfma_f32_16x16x32_bf16 v[140:143], v[164:167], a[124:127], v[140:143]\n"
"s_waitcnt vmcnt(24) lgkmcnt(0)\n"
"v_mfma_f32_16x16x32_bf16 a[160:163], v[168:171], v[48:51], a[160:163]\n"
"v_mfma_f32_16x16x32_bf16 a[160:163], v[172:175], v[52:55], a[160:163]\n"
"ds_read_b128 v[152:155], v221 offset:18432\n"
"v_mfma_f32_16x16x32_bf16 a[160:163], v[176:179], a[128:131], a[160:163]\n"
"ds_read_b128 v[156:159], v222 offset:18432\n"
"v_mfma_f32_16x16x32_bf16 a[160:163], v[180:183], a[132:135], a[160:163]\n"
"ds_read_b128 v[160:163], v221 offset:22528\n"
"buffer_load_dwordx4 v13, s[24:27], 0 offen offset:1024 lds\n"
"v_mfma_f32_16x16x32_bf16 a[164:167], v[168:171], v[56:59], a[164:167]\n"
"ds_read_b128 v[164:167], v222 offset:22528\n"
"s_add_u32 s24, s24, 30720\n"
"v_mfma_f32_16x16x32_bf16 a[164:167], v[172:175], v[60:63], a[164:167]\n"
"v_mfma_f32_16x16x32_bf16 a[164:167], v[176:179], a[136:139], a[164:167]\n"
"v_mfma_f32_16x16x32_bf16 a[164:167], v[180:183], a[140:143], a[164:167]\n"
"v_mfma_f32_16x16x32_bf16 a[168:171], v[168:171], v[64:67], a[168:171]\n"
"buffer_load_dwordx4 v12, s[24:27], 0 offen offset:2048 lds\n"
"v_mfma_f32_16x16x32_bf16 a[168:171], v[172:175], v[68:71], a[168:171]\n"
"v_mfma_f32_16x16x32_bf16 a[168:171], v[176:179], a[144:147], a[168:171]\n"
"v_mfma_f32_16x16x32_bf16 a[168:171], v[180:183], a[148:151], a[168:171]\n"
"v_mfma_f32_16x16x32_bf16 a[172:175], v[168:171], v[72:75], a[172:175]\n"
"buffer_load_dwordx4 v[196:199], v230, s[36:39], s73 offen offset:16 sc1\n"
"v_mfma_f32_16x16x32_bf16 a[172:175], v[172:175], v[76:79], a[172:175]\n"
"s_lshl_b32 s73, s59, 2\n"
"s_add_u32 s73, s73, s57\n"
"v_mfma_f32_16x16x32_bf16 a[172:175], v[176:179], a[152:155], a[172:175]\n"
"s_lshl_b32 s73, s73, 6\n"
"v_lshl_add_u32 v231, v211, 3, s73\n"
"v_mfma_f32_16x16x32_bf16 a[172:175], v[180:183], a[156:159], a[172:175]\n"
"s_waitcnt lgkmcnt(0)\n"
"v_mfma_f32_16x16x32_bf16 a[176:179], v[152:155], v[48:51], a[176:179]\n"
"v_mfma_f32_16x16x32_bf16 a[176:179], v[156:159], v[52:55], a[176:179]\n"
"ds_read_b128 v[168:171], v221 offset:24576\n"
"buffer_load_dwordx4 v13, s[24:27], 0 offen offset:3072 lds\n"
"v_mfma_f32_16x16x32_bf16 a[176:179], v[160:163], a[128:131], a[176:179]\n"
"ds_read_b128 v[172:175], v222 offset:24576\n"
"v_mfma_f32_16x16x32_bf16 a[176:179], v[164:167], a[132:135], a[176:179]\n"
"ds_read_b128 v[176:179], v221 offset:28672\n"
"v_mfma_f32_16x16x32_bf16 a[180:183], v[152:155], v[56:59], a[180:183]\n"
"ds_read_b128 v[180:183], v222 offset:28672\n"
"v_mfma_f32_16x16x32_bf16 a[180:183], v[156:159], v[60:63], a[180:183]\n"
"v_mfma_f32_16x16x32_bf16 a[180:183], v[160:163], a[136:139], a[180:183]\n"
"global_load_dwordx2 v[238:239], v231, s[50:51]\n"
"v_mfma_f32_16x16x32_bf16 a[180:183], v[164:167], a[140:143], a[180:183]\n"
"v_mfma_f32_16x16x32_bf16 a[184:187], v[152:155], v[64:67], a[184:187]\n"
"v_mfma_f32_16x16x32_bf16 a[184:187], v[156:159], v[68:71], a[184:187]\n"
"v_mfma_f32_16x16x32_bf16 a[184:187], v[160:163], a[144:147], a[184:187]\n"
"v_mfma_f32_16x16x32_bf16 a[184:187], v[164:167], a[148:151], a[184:187]\n"
"global_load_dwordx2 v[242:243], v231, s[52:53]\n"
"v_lshl_add_u32 v231, v225, 14, v231\n"
"v_mfma_f32_16x16x32_bf16 a[188:191], v[152:155], v[72:75], a[188:191]\n"
"s_lshl_b32 s72, s72, 17\n"
"s_mov_b32 s4, s42\n"
"v_mfma_f32_16x16x32_bf16 a[188:191], v[156:159], v[76:79], a[188:191]\n"
"v_mfma_f32_16x16x32_bf16 a[188:191], v[160:163], a[152:155], a[188:191]\n"
"v_mfma_f32_16x16x32_bf16 a[188:191], v[164:167], a[156:159], a[188:191]\n"
"s_waitcnt vmcnt(13) lgkmcnt(0)\n"
"v_mfma_f32_16x16x32_bf16 a[160:163], v[168:171], v[96:99], a[160:163]\n"
"buffer_load_dwordx2 v[228:229], v231, s[4:7], s72 offen offset:0\n"
"v_mfma_f32_16x16x32_bf16 a[160:163], v[172:175], v[100:103], a[160:163]\n"
"ds_read_b128 v[152:155], v221 offset:26624\n"
"v_mfma_f32_16x16x32_bf16 a[160:163], v[176:179], a[0:3], a[160:163]\n"
"ds_read_b128 v[156:159], v222 offset:26624\n"
"v_mfma_f32_16x16x32_bf16 a[160:163], v[180:183], a[4:7], a[160:163]\n"
"ds_read_b128 v[160:163], v221 offset:30720\n"
"v_mfma_f32_16x16x32_bf16 a[164:167], v[168:171], v[104:107], a[164:167]\n"
"ds_read_b128 v[164:167], v222 offset:30720\n"
"v_mfma_f32_16x16x32_bf16 a[164:167], v[172:175], v[108:111], a[164:167]\n"
"buffer_load_dwordx2 v[250:251], v231, s[4:7], s72 offen offset:2048\n"
"v_mfma_f32_16x16x32_bf16 a[164:167], v[176:179], a[8:11], a[164:167]\n"
"s_add_u32 s72, s72, 4096\n"
"v_mfma_f32_16x16x32_bf16 a[164:167], v[180:183], a[12:15], a[164:167]\n"
"v_accvgpr_read_b32 v14, a160\n"
"v_accvgpr_read_b32 v15, a161\n"
"v_mfma_f32_16x16x32_bf16 a[168:171], v[168:171], v[112:115], a[168:171]\n"
"v_cvt_pk_bf16_f32 v234, v14, v15\n"
"v_accvgpr_read_b32 v14, a162\n"
"v_mfma_f32_16x16x32_bf16 a[168:171], v[172:175], v[116:119], a[168:171]\n"
"buffer_load_dwordx2 v[254:255], v231, s[4:7], s72 offen offset:0\n"
"v_mfma_f32_16x16x32_bf16 a[168:171], v[176:179], a[16:19], a[168:171]\n"
"v_accvgpr_read_b32 v15, a163\n"
"v_cvt_pk_bf16_f32 v235, v14, v15\n"
"v_mfma_f32_16x16x32_bf16 a[168:171], v[180:183], a[20:23], a[168:171]\n"
"v_mfma_f32_16x16x32_bf16 a[172:175], v[168:171], v[120:123], a[172:175]\n"
"v_mfma_f32_16x16x32_bf16 a[172:175], v[172:175], v[124:127], a[172:175]\n"
"buffer_load_dwordx2 v[214:215], v231, s[4:7], s72 offen offset:2048\n"
"v_mfma_f32_16x16x32_bf16 a[172:175], v[176:179], a[24:27], a[172:175]\n"
"s_add_u32 s72, s72, 4096\n"
"v_mfma_f32_16x16x32_bf16 a[172:175], v[180:183], a[28:31], a[172:175]\n"
"s_waitcnt lgkmcnt(0)\n"
"v_mfma_f32_16x16x32_bf16 a[176:179], v[152:155], v[96:99], a[176:179]\n"
"s_barrier\n"
"v_mfma_f32_16x16x32_bf16 a[176:179], v[156:159], v[100:103], a[176:179]\n"
"ds_read_b128 v[168:171], v221 offset:32768\n"
"v_mfma_f32_16x16x32_bf16 a[176:179], v[160:163], a[0:3], a[176:179]\n"
"ds_read_b128 v[172:175], v222 offset:32768\n"
"v_mfma_f32_16x16x32_bf16 a[176:179], v[164:167], a[4:7], a[176:179]\n"
"ds_read_b128 v[176:179], v221 offset:36864\n"
"v_mfma_f32_16x16x32_bf16 a[180:183], v[152:155], v[104:107], a[180:183]\n"
"buffer_load_dwordx2 v[146:147], v231, s[4:7], s72 offen offset:0\n"
"v_mfma_f32_16x16x32_bf16 a[180:183], v[156:159], v[108:111], a[180:183]\n"
"ds_read_b128 v[180:183], v222 offset:36864\n"
"v_mfma_f32_16x16x32_bf16 a[180:183], v[160:163], a[8:11], a[180:183]\n"
"v_mfma_f32_16x16x32_bf16 a[180:183], v[164:167], a[12:15], a[180:183]\n"
"v_mfma_f32_16x16x32_bf16 a[184:187], v[152:155], v[112:115], a[184:187]\n"
"buffer_load_dwordx2 v[150:151], v231, s[4:7], s72 offen offset:2048\n"
"v_mfma_f32_16x16x32_bf16 a[184:187], v[156:159], v[116:119], a[184:187]\n"
"v_accvgpr_read_b32 v14, a176\n"
"v_accvgpr_read_b32 v15, a177\n"
"v_mfma_f32_16x16x32_bf16 a[184:187], v[160:163], a[16:19], a[184:187]\n"
"v_cvt_pk_bf16_f32 v236, v14, v15\n"
"v_accvgpr_read_b32 v14, a178\n"
"v_mfma_f32_16x16x32_bf16 a[184:187], v[164:167], a[20:23], a[184:187]\n"
"v_accvgpr_read_b32 v15, a179\n"
"v_cvt_pk_bf16_f32 v237, v14, v15\n"
"v_mfma_f32_16x16x32_bf16 a[188:191], v[152:155], v[120:123], a[188:191]\n"
"buffer_store_dwordx4 v[234:237], v7, s[36:39], s89 offen offset:0 sc1\n"
"v_mfma_f32_16x16x32_bf16 a[188:191], v[156:159], v[124:127], a[188:191]\n"
"v_accvgpr_read_b32 v2, a164\n"
"v_accvgpr_read_b32 v3, a165\n"
"v_mfma_f32_16x16x32_bf16 a[188:191], v[160:163], a[24:27], a[188:191]\n"
"v_cvt_pk_bf16_f32 v8, v2, v3\n"
"v_accvgpr_read_b32 v2, a166\n"
"v_mfma_f32_16x16x32_bf16 a[188:191], v[164:167], a[28:31], a[188:191]\n"
"v_accvgpr_read_b32 v3, a167\n"
"s_waitcnt lgkmcnt(0)\n"
"v_mfma_f32_16x16x32_bf16 a[192:195], v[168:171], v[48:51], a[192:195]\n"
"v_cvt_pk_bf16_f32 v9, v2, v3\n"
"v_accvgpr_read_b32 v2, a180\n"
"v_mfma_f32_16x16x32_bf16 a[192:195], v[172:175], v[52:55], a[192:195]\n"
"ds_read_b128 v[152:155], v221 offset:34816\n"
"v_accvgpr_read_b32 v3, a181\n"
"v_mfma_f32_16x16x32_bf16 a[192:195], v[176:179], a[128:131], a[192:195]\n"
"ds_read_b128 v[156:159], v222 offset:34816\n"
"v_cvt_pk_bf16_f32 v10, v2, v3\n"
"v_mfma_f32_16x16x32_bf16 a[192:195], v[180:183], a[132:135], a[192:195]\n"
"ds_read_b128 v[160:163], v221 offset:38912\n"
"v_accvgpr_read_b32 v2, a182\n"
"v_mfma_f32_16x16x32_bf16 a[196:199], v[168:171], v[56:59], a[196:199]\n"
"ds_read_b128 v[164:167], v222 offset:38912\n"
"v_accvgpr_read_b32 v3, a183\n"
"v_mfma_f32_16x16x32_bf16 a[196:199], v[172:175], v[60:63], a[196:199]\n"
"v_cvt_pk_bf16_f32 v11, v2, v3\n"
"v_accvgpr_read_b32 v14, a168\n"
"v_mfma_f32_16x16x32_bf16 a[196:199], v[176:179], a[136:139], a[196:199]\n"
"buffer_store_dwordx4 v[8:11], v7, s[36:39], s89 offen offset:1024 sc1\n"
"v_mfma_f32_16x16x32_bf16 a[196:199], v[180:183], a[140:143], a[196:199]\n"
"v_accvgpr_read_b32 v15, a169\n"
"v_cvt_pk_bf16_f32 v234, v14, v15\n"
"v_mfma_f32_16x16x32_bf16 a[200:203], v[168:171], v[64:67], a[200:203]\n"
"v_accvgpr_read_b32 v14, a170\n"
"v_accvgpr_read_b32 v15, a171\n"
"v_mfma_f32_16x16x32_bf16 a[200:203], v[172:175], v[68:71], a[200:203]\n"
"v_cvt_pk_bf16_f32 v235, v14, v15\n"
"v_accvgpr_read_b32 v14, a184\n"
"v_mfma_f32_16x16x32_bf16 a[200:203], v[176:179], a[144:147], a[200:203]\n"
"v_accvgpr_read_b32 v15, a185\n"
"v_cvt_pk_bf16_f32 v236, v14, v15\n"
"v_mfma_f32_16x16x32_bf16 a[200:203], v[180:183], a[148:151], a[200:203]\n"
"v_accvgpr_read_b32 v14, a186\n"
"v_accvgpr_read_b32 v15, a187\n"
"v_mfma_f32_16x16x32_bf16 a[204:207], v[168:171], v[72:75], a[204:207]\n"
"v_cvt_pk_bf16_f32 v237, v14, v15\n"
"v_accvgpr_read_b32 v2, a172\n"
"v_mfma_f32_16x16x32_bf16 a[204:207], v[172:175], v[76:79], a[204:207]\n"
"buffer_store_dwordx4 v[234:237], v7, s[36:39], s89 offen offset:2048 sc1\n"
"v_mfma_f32_16x16x32_bf16 a[204:207], v[176:179], a[152:155], a[204:207]\n"
"v_accvgpr_read_b32 v3, a173\n"
"v_cvt_pk_bf16_f32 v8, v2, v3\n"
"v_mfma_f32_16x16x32_bf16 a[204:207], v[180:183], a[156:159], a[204:207]\n"
"v_accvgpr_read_b32 v2, a174\n"
"s_waitcnt lgkmcnt(0)\n"
"v_mfma_f32_16x16x32_bf16 a[208:211], v[152:155], v[48:51], a[208:211]\n"
"v_accvgpr_read_b32 v3, a175\n"
"v_cvt_pk_bf16_f32 v9, v2, v3\n"
"v_mfma_f32_16x16x32_bf16 a[208:211], v[156:159], v[52:55], a[208:211]\n"
"ds_read_b128 v[168:171], v221 offset:40960\n"
"v_accvgpr_read_b32 v2, a188\n"
"v_mfma_f32_16x16x32_bf16 a[208:211], v[160:163], a[128:131], a[208:211]\n"
"ds_read_b128 v[172:175], v222 offset:40960\n"
"v_accvgpr_read_b32 v3, a189\n"
"v_mfma_f32_16x16x32_bf16 a[208:211], v[164:167], a[132:135], a[208:211]\n"
"ds_read_b128 v[176:179], v221 offset:45056\n"
"v_cvt_pk_bf16_f32 v10, v2, v3\n"
"v_mfma_f32_16x16x32_bf16 a[212:215], v[152:155], v[56:59], a[212:215]\n"
"ds_read_b128 v[180:183], v222 offset:45056\n"
"v_accvgpr_read_b32 v2, a190\n"
"v_mfma_f32_16x16x32_bf16 a[212:215], v[156:159], v[60:63], a[212:215]\n"
"v_accvgpr_read_b32 v3, a191\n"
"v_cvt_pk_bf16_f32 v11, v2, v3\n"
"v_mfma_f32_16x16x32_bf16 a[212:215], v[160:163], a[136:139], a[212:215]\n"
"buffer_store_dwordx4 v[8:11], v7, s[36:39], s89 offen offset:3072 sc1\n"
"v_mfma_f32_16x16x32_bf16 a[212:215], v[164:167], a[140:143], a[212:215]\n"
"s_add_u32 s88, s57, 2\n"
"s_and_b32 s88, s88, 3\n"
"v_mfma_f32_16x16x32_bf16 a[216:219], v[152:155], v[64:67], a[216:219]\n"
"s_add_u32 s88, s88, s82\n"
"s_mul_i32 s88, s88, 12\n"
"v_mfma_f32_16x16x32_bf16 a[216:219], v[156:159], v[68:71], a[216:219]\n"
"s_add_u32 s88, s88, s54\n"
"s_add_u32 s88, s88, 4\n"
"v_mfma_f32_16x16x32_bf16 a[216:219], v[160:163], a[144:147], a[216:219]\n"
"s_lshl_b32 s68, s88, 12\n"
"s_add_u32 s68, s68, 25165824\n"
"v_mfma_f32_16x16x32_bf16 a[216:219], v[164:167], a[148:151], a[216:219]\n"
"s_lshl_b32 s69, s88, 7\n"
"s_add_u32 s69, s69, 39976960\n"
"v_mfma_f32_16x16x32_bf16 a[220:223], v[152:155], v[72:75], a[220:223]\n"
"s_add_u32 s72, s72, 4096\n"
"v_mfma_f32_16x16x32_bf16 a[220:223], v[156:159], v[76:79], a[220:223]\n"
"buffer_load_dwordx2 v[186:187], v231, s[4:7], s72 offen offset:0\n"
"v_mfma_f32_16x16x32_bf16 a[220:223], v[160:163], a[152:155], a[220:223]\n"
"v_mfma_f32_16x16x32_bf16 a[220:223], v[164:167], a[156:159], a[220:223]\n"
"s_waitcnt lgkmcnt(0)\n"
"v_mfma_f32_16x16x32_bf16 a[192:195], v[168:171], v[96:99], a[192:195]\n"
"v_mfma_f32_16x16x32_bf16 a[192:195], v[172:175], v[100:103], a[192:195]\n"
"ds_read_b128 v[152:155], v221 offset:43008\n"
"v_mfma_f32_16x16x32_bf16 a[192:195], v[176:179], a[0:3], a[192:195]\n"
"ds_read_b128 v[156:159], v222 offset:43008\n"
"v_mfma_f32_16x16x32_bf16 a[192:195], v[180:183], a[4:7], a[192:195]\n"
"buffer_load_dwordx2 v[190:191], v231, s[4:7], s72 offen offset:2048\n"
"v_mfma_f32_16x16x32_bf16 a[196:199], v[168:171], v[104:107], a[196:199]\n"
"ds_read_b128 v[160:163], v221 offset:47104\n"
"s_add_u32 s72, s72, 4096\n"
"v_mfma_f32_16x16x32_bf16 a[196:199], v[172:175], v[108:111], a[196:199]\n"
"ds_read_b128 v[164:167], v222 offset:47104\n"
"v_lshlrev_b32 v231, 1, v231\n"
"v_mfma_f32_16x16x32_bf16 a[196:199], v[176:179], a[8:11], a[196:199]\n"
"v_accvgpr_read_b32 v14, a192\n"
"v_accvgpr_read_b32 v15, a193\n"
"v_mfma_f32_16x16x32_bf16 a[196:199], v[180:183], a[12:15], a[196:199]\n"
"v_cvt_pk_bf16_f32 v234, v14, v15\n"
"v_accvgpr_read_b32 v14, a194\n"
"v_mfma_f32_16x16x32_bf16 a[200:203], v[168:171], v[112:115], a[200:203]\n"
"v_accvgpr_read_b32 v15, a195\n"
"v_cvt_pk_bf16_f32 v235, v14, v15\n"
"v_mfma_f32_16x16x32_bf16 a[200:203], v[172:175], v[116:119], a[200:203]\n"
"s_lshl_b32 s72, s72, 1\n"
"s_sub_u32 s74, s72, 32768\n"
"v_mfma_f32_16x16x32_bf16 a[200:203], v[176:179], a[16:19], a[200:203]\n"
"s_sub_u32 s75, s72, 28672\n"
"s_sub_u32 s76, s72, 24576\n"
"v_mfma_f32_16x16x32_bf16 a[200:203], v[180:183], a[20:23], a[200:203]\n"
"s_sub_u32 s77, s72, 20480\n"
"s_sub_u32 s78, s72, 16384\n"
"v_mfma_f32_16x16x32_bf16 a[204:207], v[168:171], v[120:123], a[204:207]\n"
"s_sub_u32 s79, s72, 12288\n"
"s_sub_u32 s80, s72, 8192\n"
"v_mfma_f32_16x16x32_bf16 a[204:207], v[172:175], v[124:127], a[204:207]\n"
"s_sub_u32 s81, s72, 4096\n"
"v_mfma_f32_16x16x32_bf16 a[204:207], v[176:179], a[24:27], a[204:207]\n"
"s_waitcnt vmcnt(13)\n"
"v_and_b32 v241, 0xffff0000, v239\n"
"v_mfma_f32_16x16x32_bf16 a[204:207], v[180:183], a[28:31], a[204:207]\n"
"v_lshlrev_b32 v240, 16, v239\n"
"s_waitcnt lgkmcnt(0)\n"
"v_mfma_f32_16x16x32_bf16 a[208:211], v[152:155], v[96:99], a[208:211]\n"
"s_barrier\n"
"v_and_b32 v239, 0xffff0000, v238\n"
"v_mfma_f32_16x16x32_bf16 a[208:211], v[156:159], v[100:103], a[208:211]\n"
"ds_read_b128 v[168:171], v221 offset:49152\n"
"v_lshlrev_b32 v238, 16, v238\n"
"v_mfma_f32_16x16x32_bf16 a[208:211], v[160:163], a[0:3], a[208:211]\n"
"ds_read_b128 v[172:175], v222 offset:49152\n"
"v_mfma_f32_16x16x32_bf16 a[208:211], v[164:167], a[4:7], a[208:211]\n"
"ds_read_b128 v[176:179], v221 offset:53248\n"
"v_mfma_f32_16x16x32_bf16 a[212:215], v[152:155], v[104:107], a[212:215]\n"
"s_waitcnt vmcnt(12)\n"
"v_and_b32 v245, 0xffff0000, v243\n"
"v_mfma_f32_16x16x32_bf16 a[212:215], v[156:159], v[108:111], a[212:215]\n"
"ds_read_b128 v[180:183], v222 offset:53248\n"
"v_lshlrev_b32 v244, 16, v243\n"
"v_mfma_f32_16x16x32_bf16 a[212:215], v[160:163], a[8:11], a[212:215]\n"
".Lk_h12:\n"
"s_cbranch_execz .Lk_hend\n"
"v_accvgpr_read_b32 v14, a208\n"
"v_accvgpr_read_b32 v15, a209\n"
"v_mfma_f32_16x16x32_bf16 a[212:215], v[164:167], a[12:15], a[212:215]\n"
"v_cvt_pk_bf16_f32 v236, v14, v15\n"
"v_accvgpr_read_b32 v14, a210\n"
"v_mfma_f32_16x16x32_bf16 a[216:219], v[152:155], v[112:115], a[216:219]\n"
"v_accvgpr_read_b32 v15, a211\n"
"v_cvt_pk_bf16_f32 v237, v14, v15\n"
"v_mfma_f32_16x16x32_bf16 a[216:219], v[156:159], v[116:119], a[216:219]\n"
"buffer_store_dwordx4 v[234:237], v7, s[36:39], s68 offen offset:0 sc1\n"
"v_mfma_f32_16x16x32_bf16 a[216:219], v[160:163], a[16:19], a[216:219]\n"
"v_accvgpr_read_b32 v2, a196\n"
"v_accvgpr_read_b32 v3, a197\n"
"v_mfma_f32_16x16x32_bf16 a[216:219], v[164:167], a[20:23], a[216:219]\n"
"v_cvt_pk_bf16_f32 v8, v2, v3\n"
"v_accvgpr_read_b32 v2, a198\n"
"v_mfma_f32_16x16x32_bf16 a[220:223], v[152:155], v[120:123], a[220:223]\n"
"v_accvgpr_read_b32 v3, a199\n"
"v_cvt_pk_bf16_f32 v9, v2, v3\n"
"v_mfma_f32_16x16x32_bf16 a[220:223], v[156:159], v[124:127], a[220:223]\n"
"v_accvgpr_read_b32 v2, a212\n"
"v_accvgpr_read_b32 v3, a213\n"
"v_mfma_f32_16x16x32_bf16 a[220:223], v[160:163], a[24:27], a[220:223]\n"
"v_cvt_pk_bf16_f32 v10, v2, v3\n"
"v_accvgpr_read_b32 v2, a214\n"
"v_mfma_f32_16x16x32_bf16 a[220:223], v[164:167], a[28:31], a[220:223]\n"
"v_accvgpr_read_b32 v3, a215\n"
"s_waitcnt lgkmcnt(0)\n"
"v_mfma_f32_16x16x32_bf16 v[80:83], v[168:171], v[48:51], v[80:83]\n"
"v_cvt_pk_bf16_f32 v11, v2, v3\n"
"v_accvgpr_read_b32 v14, a200\n"
"v_mfma_f32_16x16x32_bf16 v[80:83], v[172:175], v[52:55], v[80:83]\n"
"ds_read_b128 v[152:155], v221 offset:51200\n"
"v_accvgpr_read_b32 v15, a201\n"
"v_mfma_f32_16x16x32_bf16 v[80:83], v[176:179], a[128:131], v[80:83]\n"
"buffer_store_dwordx4 v[8:11], v7, s[36:39], s68 offen offset:1024 sc1\n"
"v_mfma_f32_16x16x32_bf16 v[80:83], v[180:183], a[132:135], v[80:83]\n"
"ds_read_b128 v[156:159], v222 offset:51200\n"
"v_cvt_pk_bf16_f32 v234, v14, v15\n"
"v_mfma_f32_16x16x32_bf16 v[84:87], v[168:171], v[56:59], v[84:87]\n"
"ds_read_b128 v[160:163], v221 offset:55296\n"
"v_accvgpr_read_b32 v14, a202\n"
"v_mfma_f32_16x16x32_bf16 v[84:87], v[172:175], v[60:63], v[84:87]\n"
"ds_read_b128 v[164:167], v222 offset:55296\n"
"v_accvgpr_read_b32 v15, a203\n"
"v_mfma_f32_16x16x32_bf16 v[84:87], v[176:179], a[136:139], v[84:87]\n"
"v_cvt_pk_bf16_f32 v235, v14, v15\n"
"v_accvgpr_read_b32 v14, a216\n"
"v_mfma_f32_16x16x32_bf16 v[84:87], v[180:183], a[140:143], v[84:87]\n"
"v_accvgpr_read_b32 v15, a217\n"
"v_cvt_pk_bf16_f32 v236, v14, v15\n"
"v_mfma_f32_16x16x32_bf16 v[88:91], v[168:171], v[64:67], v[88:91]\n"
"v_accvgpr_read_b32 v14, a218\n"
"v_accvgpr_read_b32 v15, a219\n"
"v_mfma_f32_16x16x32_bf16 v[88:91], v[172:175], v[68:71], v[88:91]\n"
"v_cvt_pk_bf16_f32 v237, v14, v15\n"
"v_accvgpr_read_b32 v2, a204\n"
"v_mfma_f32_16x16x32_bf16 v[88:91], v[176:179], a[144:147], v[88:91]\n"
"buffer_store_dwordx4 v[234:237], v7, s[36:39], s68 offen offset:2048 sc1\n"
"v_mfma_f32_16x16x32_bf16 v[88:91], v[180:183], a[148:151], v[88:91]\n"
"v_accvgpr_read_b32 v3, a205\n"
"v_cvt_pk_bf16_f32 v8, v2, v3\n"
"v_mfma_f32_16x16x32_bf16 v[92:95], v[168:171], v[72:75], v[92:95]\n"
"v_accvgpr_read_b32 v2, a206\n"
"v_accvgpr_read_b32 v3, a207\n"
"v_mfma_f32_16x16x32_bf16 v[92:95], v[172:175], v[76:79], v[92:95]\n"
"v_cvt_pk_bf16_f32 v9, v2, v3\n"
"v_accvgpr_read_b32 v2, a220\n"
"v_mfma_f32_16x16x32_bf16 v[92:95], v[176:179], a[152:155], v[92:95]\n"
"v_accvgpr_read_b32 v3, a221\n"
"v_cvt_pk_bf16_f32 v10, v2, v3\n"
"v_mfma_f32_16x16x32_bf16 v[92:95], v[180:183], a[156:159], v[92:95]\n"
"v_accvgpr_read_b32 v2, a222\n"
"s_waitcnt lgkmcnt(0)\n"
"v_mfma_f32_16x16x32_bf16 v[128:131], v[152:155], v[48:51], v[128:131]\n"
"v_accvgpr_read_b32 v3, a223\n"
"v_cvt_pk_bf16_f32 v11, v2, v3\n"
"v_mfma_f32_16x16x32_bf16 v[128:131], v[156:159], v[52:55], v[128:131]\n"
"ds_read_b128 v[168:171], v221 offset:57344\n"
"s_add_u32 s88, s57, 3\n"
"v_mfma_f32_16x16x32_bf16 v[128:131], v[160:163], a[128:131], v[128:131]\n"
"buffer_store_dwordx4 v[8:11], v7, s[36:39], s68 offen offset:3072 sc1\n"
"v_mfma_f32_16x16x32_bf16 v[128:131], v[164:167], a[132:135], v[128:131]\n"
"ds_read_b128 v[172:175], v222 offset:57344\n"
"s_and_b32 s88, s88, 3\n"
"v_mfma_f32_16x16x32_bf16 v[132:135], v[152:155], v[56:59], v[132:135]\n"
"ds_read_b128 v[176:179], v221 offset:61440\n"
"s_add_u32 s88, s88, s82\n"
"v_mfma_f32_16x16x32_bf16 v[132:135], v[156:159], v[60:63], v[132:135]\n"
"ds_read_b128 v[180:183], v222 offset:61440\n"
"s_mul_i32 s88, s88, 12\n"
"v_mfma_f32_16x16x32_bf16 v[132:135], v[160:163], a[136:139], v[132:135]\n"
"s_add_u32 s88, s88, s54\n"
"s_add_u32 s88, s88, 8\n"
"v_mfma_f32_16x16x32_bf16 v[132:135], v[164:167], a[140:143], v[132:135]\n"
"s_lshl_b32 s89, s88, 12\n"
"s_add_u32 s89, s89, 25165824\n"
"v_mfma_f32_16x16x32_bf16 v[136:139], v[152:155], v[64:67], v[136:139]\n"
"v_and_b32 v243, 0xffff0000, v242\n"
"v_lshlrev_b32 v242, 16, v242\n"
"v_mfma_f32_16x16x32_bf16 v[136:139], v[156:159], v[68:71], v[136:139]\n"
"v_add_f32 v233, 1.0, v192\n"
"v_mfma_f32_16x16x32_bf16 v[136:139], v[160:163], a[144:147], v[136:139]\n"
"v_rcp_f32 v233, v233\n"
"v_mfma_f32_16x16x32_bf16 v[136:139], v[164:167], a[148:151], v[136:139]\n"
"v_mul_f32 v192, v192, v233\n"
"v_mfma_f32_16x16x32_bf16 v[140:143], v[152:155], v[72:75], v[140:143]\n"
"s_waitcnt vmcnt(15)\n"
"v_lshlrev_b32 v226, 16, v228\n"
"v_mfma_f32_16x16x32_bf16 v[140:143], v[156:159], v[76:79], v[140:143]\n"
"v_and_b32 v227, 0xffff0000, v228\n"
"v_lshlrev_b32 v228, 16, v229\n"
"v_mfma_f32_16x16x32_bf16 v[140:143], v[160:163], a[152:155], v[140:143]\n"
"v_and_b32 v229, 0xffff0000, v229\n"
"v_mul_f32 v226, v238, v226\n"
"v_mfma_f32_16x16x32_bf16 v[140:143], v[164:167], a[156:159], v[140:143]\n"
"v_mul_f32 v227, v239, v227\n"
"s_waitcnt lgkmcnt(0)\n"
"v_mfma_f32_16x16x32_bf16 v[80:83], v[168:171], v[96:99], v[80:83]\n"
"v_mul_f32 v228, v240, v228\n"
"v_mul_f32 v229, v241, v229\n"
"v_mfma_f32_16x16x32_bf16 v[80:83], v[172:175], v[100:103], v[80:83]\n"
"ds_read_b128 v[152:155], v221 offset:59392\n"
"v_mul_f32 v226, v192, v226\n"
"v_mfma_f32_16x16x32_bf16 v[80:83], v[176:179], a[0:3], v[80:83]\n"
"ds_read_b128 v[156:159], v222 offset:59392\n"
"v_mul_f32 v227, v192, v227\n"
"v_mfma_f32_16x16x32_bf16 v[80:83], v[180:183], a[4:7], v[80:83]\n"
"ds_read_b128 v[160:163], v221 offset:63488\n"
"v_mul_f32 v228, v192, v228\n"
"v_mfma_f32_16x16x32_bf16 v[84:87], v[168:171], v[104:107], v[84:87]\n"
"ds_read_b128 v[164:167], v222 offset:63488\n"
"v_mul_f32 v229, v192, v229\n"
"v_mfma_f32_16x16x32_bf16 v[84:87], v[172:175], v[108:111], v[84:87]\n"
"v_fmac_f32 v226, v233, v242\n"
"v_fmac_f32 v227, v233, v243\n"
"v_mfma_f32_16x16x32_bf16 v[84:87], v[176:179], a[8:11], v[84:87]\n"
"s_waitcnt vmcnt(6)\n"
"buffer_store_dword v219, v220, s[36:39], s90 offen sc1\n"
"v_mfma_f32_16x16x32_bf16 v[84:87], v[180:183], a[12:15], v[84:87]\n"
"s_lshl_b32 s90, s88, 7\n"
"s_add_u32 s90, s90, 39976960\n"
"v_mfma_f32_16x16x32_bf16 v[88:91], v[168:171], v[112:115], v[88:91]\n"
"v_cvt_pk_bf16_f32 v80, v80, v81\n"
"v_cvt_pk_bf16_f32 v81, v82, v83\n"
"v_mfma_f32_16x16x32_bf16 v[88:91], v[172:175], v[116:119], v[88:91]\n"
"v_fmac_f32 v228, v233, v244\n"
"v_fmac_f32 v229, v233, v245\n"
"v_mfma_f32_16x16x32_bf16 v[88:91], v[176:179], a[16:19], v[88:91]\n"
"v_add_f32 v216, 1.0, v193\n"
"v_mfma_f32_16x16x32_bf16 v[88:91], v[180:183], a[20:23], v[88:91]\n"
"v_rcp_f32 v216, v216\n"
"v_mfma_f32_16x16x32_bf16 v[92:95], v[168:171], v[120:123], v[92:95]\n"
"v_mul_f32 v193, v193, v216\n"
"v_lshlrev_b32 v248, 16, v250\n"
"v_mfma_f32_16x16x32_bf16 v[92:95], v[172:175], v[124:127], v[92:95]\n"
"v_and_b32 v249, 0xffff0000, v250\n"
"v_lshlrev_b32 v250, 16, v251\n"
"v_mfma_f32_16x16x32_bf16 v[92:95], v[176:179], a[24:27], v[92:95]\n"
"v_and_b32 v251, 0xffff0000, v251\n"
"v_mul_f32 v248, v238, v248\n"
"v_mfma_f32_16x16x32_bf16 v[92:95], v[180:183], a[28:31], v[92:95]\n"
"v_mul_f32 v249, v239, v249\n"
"s_waitcnt lgkmcnt(0)\n"
"v_mfma_f32_16x16x32_bf16 v[128:131], v[152:155], v[96:99], v[128:131]\n"
"s_barrier\n"
"v_mul_f32 v250, v240, v250\n"
"v_mfma_f32_16x16x32_bf16 v[128:131], v[156:159], v[100:103], v[128:131]\n"
"ds_read_b128 v[168:171], v223 offset:0\n"
"v_mul_f32 v251, v241, v251\n"
"v_mfma_f32_16x16x32_bf16 v[128:131], v[160:163], a[0:3], v[128:131]\n"
"ds_read_b128 v[172:175], v224 offset:0\n"
"v_mul_f32 v248, v193, v248\n"
"v_mfma_f32_16x16x32_bf16 v[128:131], v[164:167], a[4:7], v[128:131]\n"
"ds_read_b128 v[176:179], v223 offset:4096\n"
"v_mul_f32 v249, v193, v249\n"
"v_mfma_f32_16x16x32_bf16 v[132:135], v[152:155], v[104:107], v[132:135]\n"
"ds_read_b128 v[180:183], v224 offset:4096\n"
"v_mul_f32 v250, v193, v250\n"
"v_mfma_f32_16x16x32_bf16 v[132:135], v[156:159], v[108:111], v[132:135]\n"
"v_mul_f32 v251, v193, v251\n"
"v_fmac_f32 v248, v216, v242\n"
"v_mfma_f32_16x16x32_bf16 v[132:135], v[160:163], a[8:11], v[132:135]\n"
"v_cvt_pk_bf16_f32 v82, v128, v129\n"
"v_cvt_pk_bf16_f32 v83, v130, v131\n"
"v_mfma_f32_16x16x32_bf16 v[132:135], v[164:167], a[12:15], v[132:135]\n"
"buffer_store_dwordx4 v[80:83], v7, s[36:39], s89 offen offset:0 sc1\n"
"v_mfma_f32_16x16x32_bf16 v[136:139], v[152:155], v[112:115], v[136:139]\n"
"v_cvt_pk_bf16_f32 v84, v84, v85\n"
"v_cvt_pk_bf16_f32 v85, v86, v87\n"
"v_mfma_f32_16x16x32_bf16 v[136:139], v[156:159], v[116:119], v[136:139]\n"
"v_fmac_f32 v249, v216, v243\n"
"v_fmac_f32 v250, v216, v244\n"
"v_mfma_f32_16x16x32_bf16 v[136:139], v[160:163], a[16:19], v[136:139]\n"
"v_cvt_pk_bf16_f32 v86, v132, v133\n"
"v_cvt_pk_bf16_f32 v87, v134, v135\n"
"v_mfma_f32_16x16x32_bf16 v[136:139], v[164:167], a[20:23], v[136:139]\n"
"buffer_store_dwordx4 v[84:87], v7, s[36:39], s89 offen offset:1024 sc1\n"
"v_mfma_f32_16x16x32_bf16 v[140:143], v[152:155], v[120:123], v[140:143]\n"
"v_cvt_pk_bf16_f32 v88, v88, v89\n"
"v_cvt_pk_bf16_f32 v89, v90, v91\n"
"v_mfma_f32_16x16x32_bf16 v[140:143], v[156:159], v[124:127], v[140:143]\n"
"v_fmac_f32 v251, v216, v245\n"
"v_add_f32 v217, 1.0, v194\n"
"v_mfma_f32_16x16x32_bf16 v[140:143], v[160:163], a[24:27], v[140:143]\n"
"v_cvt_pk_bf16_f32 v90, v136, v137\n"
"v_cvt_pk_bf16_f32 v91, v138, v139\n"
"v_mfma_f32_16x16x32_bf16 v[140:143], v[164:167], a[28:31], v[140:143]\n"
"v_cvt_pk_bf16_f32 v92, v92, v93\n"
"s_waitcnt lgkmcnt(0)\n"
"v_mfma_f32_16x16x32_bf16 a[224:227], v[168:171], a[32:35], a[224:227]\n"
"buffer_store_dwordx4 v[88:91], v7, s[36:39], s89 offen offset:2048 sc1\n"
"v_mfma_f32_16x16x32_bf16 a[224:227], v[172:175], a[36:39], a[224:227]\n"
"v_rcp_f32 v217, v217\n"
"v_mfma_f32_16x16x32_bf16 a[224:227], v[176:179], v[16:19], a[224:227]\n"
"ds_read_b128 v[152:155], v223 offset:2048\n"
"v_cvt_pk_bf16_f32 v93, v94, v95\n"
"v_mfma_f32_16x16x32_bf16 a[224:227], v[180:183], v[20:23], a[224:227]\n"
"ds_read_b128 v[156:159], v224 offset:2048\n"
"v_cvt_pk_bf16_f32 v94, v140, v141\n"
"v_mfma_f32_16x16x32_bf16 a[228:231], v[168:171], a[40:43], a[228:231]\n"
"ds_read_b128 v[160:163], v223 offset:6144\n"
"v_cvt_pk_bf16_f32 v95, v142, v143\n"
"v_mfma_f32_16x16x32_bf16 a[228:231], v[172:175], a[44:47], a[228:231]\n"
"ds_read_b128 v[164:167], v224 offset:6144\n"
"v_mul_f32 v194, v194, v217\n"
"v_mfma_f32_16x16x32_bf16 a[228:231], v[176:179], v[24:27], a[228:231]\n"
"buffer_store_dwordx4 v[92:95], v7, s[36:39], s89 offen offset:3072 sc1\n"
"v_mfma_f32_16x16x32_bf16 a[228:231], v[180:183], v[28:31], a[228:231]\n"
"v_lshlrev_b32 v252, 16, v254\n"
"v_and_b32 v253, 0xffff0000, v254\n"
"v_mfma_f32_16x16x32_bf16 a[232:235], v[168:171], a[48:51], a[232:235]\n"
"v_lshlrev_b32 v254, 16, v255\n"
"v_and_b32 v255, 0xffff0000, v255\n"
"v_mfma_f32_16x16x32_bf16 a[232:235], v[172:175], a[52:55], a[232:235]\n"
"v_mul_f32 v252, v238, v252\n"
"v_mul_f32 v253, v239, v253\n"
"v_mfma_f32_16x16x32_bf16 a[232:235], v[176:179], v[32:35], a[232:235]\n"
"v_mul_f32 v254, v240, v254\n"
"v_mul_f32 v255, v241, v255\n"
"v_mfma_f32_16x16x32_bf16 a[232:235], v[180:183], v[36:39], a[232:235]\n"
"v_mul_f32 v252, v194, v252\n"
"v_mul_f32 v253, v194, v253\n"
"v_mfma_f32_16x16x32_bf16 a[236:239], v[168:171], a[56:59], a[236:239]\n"
"v_mul_f32 v254, v194, v254\n"
"v_mul_f32 v255, v194, v255\n"
"v_mfma_f32_16x16x32_bf16 a[236:239], v[172:175], a[60:63], a[236:239]\n"
"v_fmac_f32 v252, v217, v242\n"
"v_fmac_f32 v253, v217, v243\n"
"v_mfma_f32_16x16x32_bf16 a[236:239], v[176:179], v[40:43], a[236:239]\n"
"v_fmac_f32 v254, v217, v244\n"
"v_fmac_f32 v255, v217, v245\n"
"v_mfma_f32_16x16x32_bf16 a[236:239], v[180:183], v[44:47], a[236:239]\n"
"v_add_f32 v218, 1.0, v195\n"
"s_waitcnt lgkmcnt(0)\n"
"v_mfma_f32_16x16x32_bf16 a[240:243], v[152:155], a[32:35], a[240:243]\n"
"v_rcp_f32 v218, v218\n"
"v_mfma_f32_16x16x32_bf16 a[240:243], v[156:159], a[36:39], a[240:243]\n"
"ds_read_b128 v[168:171], v223 offset:8192\n"
"v_mul_f32 v195, v195, v218\n"
"v_mfma_f32_16x16x32_bf16 a[240:243], v[160:163], v[16:19], a[240:243]\n"
"ds_read_b128 v[172:175], v224 offset:8192\n"
"v_lshlrev_b32 v212, 16, v214\n"
"v_mfma_f32_16x16x32_bf16 a[240:243], v[164:167], v[20:23], a[240:243]\n"
"ds_read_b128 v[176:179], v223 offset:12288\n"
"v_and_b32 v213, 0xffff0000, v214\n"
"v_mfma_f32_16x16x32_bf16 a[244:247], v[152:155], a[40:43], a[244:247]\n"
"ds_read_b128 v[180:183], v224 offset:12288\n"
"v_lshlrev_b32 v214, 16, v215\n"
"v_mfma_f32_16x16x32_bf16 a[244:247], v[156:159], a[44:47], a[244:247]\n"
"v_and_b32 v215, 0xffff0000, v215\n"
"v_mul_f32 v212, v238, v212\n"
"v_mfma_f32_16x16x32_bf16 a[244:247], v[160:163], v[24:27], a[244:247]\n"
"v_mul_f32 v213, v239, v213\n"
"v_mul_f32 v214, v240, v214\n"
"v_mfma_f32_16x16x32_bf16 a[244:247], v[164:167], v[28:31], a[244:247]\n"
"v_mul_f32 v215, v241, v215\n"
"v_mul_f32 v212, v195, v212\n"
"v_mfma_f32_16x16x32_bf16 a[248:251], v[152:155], a[48:51], a[248:251]\n"
"s_waitcnt vmcnt(5)\n"
"buffer_store_dword v219, v220, s[36:39], s69 offen sc1\n"
"v_mfma_f32_16x16x32_bf16 a[248:251], v[156:159], a[52:55], a[248:251]\n"
"v_mul_f32 v213, v195, v213\n"
"v_mul_f32 v214, v195, v214\n"
"v_mfma_f32_16x16x32_bf16 a[248:251], v[160:163], v[32:35], a[248:251]\n"
"v_mul_f32 v215, v195, v215\n"
"v_fmac_f32 v212, v218, v242\n"
"v_mfma_f32_16x16x32_bf16 a[248:251], v[164:167], v[36:39], a[248:251]\n"
"v_fmac_f32 v213, v218, v243\n"
"v_fmac_f32 v214, v218, v244\n"
"v_mfma_f32_16x16x32_bf16 a[252:255], v[152:155], a[56:59], a[252:255]\n"
"v_fmac_f32 v215, v218, v245\n"
"v_add_f32 v206, 1.0, v196\n"
"v_mfma_f32_16x16x32_bf16 a[252:255], v[156:159], a[60:63], a[252:255]\n"
"v_rcp_f32 v206, v206\n"
"v_mfma_f32_16x16x32_bf16 a[252:255], v[160:163], v[40:43], a[252:255]\n"
"v_mul_f32 v196, v196, v206\n"
"v_lshlrev_b32 v144, 16, v146\n"
"v_mfma_f32_16x16x32_bf16 a[252:255], v[164:167], v[44:47], a[252:255]\n"
"v_and_b32 v145, 0xffff0000, v146\n"
"s_waitcnt lgkmcnt(0)\n"
"v_mfma_f32_16x16x32_bf16 a[224:227], v[168:171], a[64:67], a[224:227]\n"
"v_lshlrev_b32 v146, 16, v147\n"
"v_and_b32 v147, 0xffff0000, v147\n"
"v_mfma_f32_16x16x32_bf16 a[224:227], v[172:175], a[68:71], a[224:227]\n"
"ds_read_b128 v[152:155], v223 offset:10240\n"
"v_mul_f32 v144, v238, v144\n"
"v_mfma_f32_16x16x32_bf16 a[224:227], v[176:179], a[96:99], a[224:227]\n"
"ds_read_b128 v[156:159], v224 offset:10240\n"
"v_mul_f32 v145, v239, v145\n"
"v_mfma_f32_16x16x32_bf16 a[224:227], v[180:183], a[100:103], a[224:227]\n"
"ds_read_b128 v[160:163], v223 offset:14336\n"
"v_mul_f32 v146, v240, v146\n"
"v_mfma_f32_16x16x32_bf16 a[228:231], v[168:171], a[72:75], a[228:231]\n"
"ds_read_b128 v[164:167], v224 offset:14336\n"
"v_mul_f32 v147, v241, v147\n"
"v_mfma_f32_16x16x32_bf16 a[228:231], v[172:175], a[76:79], a[228:231]\n"
"v_mul_f32 v144, v196, v144\n"
"v_mul_f32 v145, v196, v145\n"
"v_mfma_f32_16x16x32_bf16 a[228:231], v[176:179], a[104:107], a[228:231]\n"
"v_mul_f32 v146, v196, v146\n"
"v_mul_f32 v147, v196, v147\n"
"v_mfma_f32_16x16x32_bf16 a[228:231], v[180:183], a[108:111], a[228:231]\n"
"v_fmac_f32 v144, v206, v242\n"
"v_fmac_f32 v145, v206, v243\n"
"v_mfma_f32_16x16x32_bf16 a[232:235], v[168:171], a[80:83], a[232:235]\n"
"v_fmac_f32 v146, v206, v244\n"
"v_fmac_f32 v147, v206, v245\n"
"v_mfma_f32_16x16x32_bf16 a[232:235], v[172:175], a[84:87], a[232:235]\n"
"v_add_f32 v207, 1.0, v197\n"
"v_mfma_f32_16x16x32_bf16 a[232:235], v[176:179], a[112:115], a[232:235]\n"
"v_rcp_f32 v207, v207\n"
"v_mfma_f32_16x16x32_bf16 a[232:235], v[180:183], a[116:119], a[232:235]\n"
"v_mul_f32 v197, v197, v207\n"
"v_lshlrev_b32 v148, 16, v150\n"
"v_mfma_f32_16x16x32_bf16 a[236:239], v[168:171], a[88:91], a[236:239]\n"
"v_and_b32 v149, 0xffff0000, v150\n"
"v_lshlrev_b32 v150, 16, v151\n"
"v_mfma_f32_16x16x32_bf16 a[236:239], v[172:175], a[92:95], a[236:239]\n"
"v_and_b32 v151, 0xffff0000, v151\n"
"v_mul_f32 v148, v238, v148\n"
"v_mfma_f32_16x16x32_bf16 a[236:239], v[176:179], a[120:123], a[236:239]\n"
"v_mul_f32 v149, v239, v149\n"
"v_mul_f32 v150, v240, v150\n"
"v_mfma_f32_16x16x32_bf16 a[236:239], v[180:183], a[124:127], a[236:239]\n"
"v_mul_f32 v151, v241, v151\n"
"s_waitcnt lgkmcnt(0)\n"
"v_mfma_f32_16x16x32_bf16 a[240:243], v[152:155], a[64:67], a[240:243]\n"
"s_barrier\n"
"v_mul_f32 v148, v197, v148\n"
"v_mfma_f32_16x16x32_bf16 a[240:243], v[156:159], a[68:71], a[240:243]\n"
"ds_read_b128 v[168:171], v223 offset:16384\n"
"v_mul_f32 v149, v197, v149\n"
"v_mfma_f32_16x16x32_bf16 a[240:243], v[160:163], a[96:99], a[240:243]\n"
"ds_read_b128 v[172:175], v224 offset:16384\n"
"v_mul_f32 v150, v197, v150\n"
"v_mfma_f32_16x16x32_bf16 a[240:243], v[164:167], a[100:103], a[240:243]\n"
"buffer_load_dword v232, off, s[36:39], s71 offset:0 sc1\n"
"v_mfma_f32_16x16x32_bf16 a[244:247], v[152:155], a[72:75], a[244:247]\n"
"ds_read_b128 v[176:179], v223 offset:20480\n"
"v_mul_f32 v151, v197, v151\n"
"v_mfma_f32_16x16x32_bf16 a[244:247], v[156:159], a[76:79], a[244:247]\n"
"ds_read_b128 v[180:183], v224 offset:20480\n"
"v_fmac_f32 v148, v207, v242\n"
"v_mfma_f32_16x16x32_bf16 a[244:247], v[160:163], a[104:107], a[244:247]\n"
"v_fmac_f32 v149, v207, v243\n"
"v_fmac_f32 v150, v207, v244\n"
"v_mfma_f32_16x16x32_bf16 a[244:247], v[164:167], a[108:111], a[244:247]\n"
"v_fmac_f32 v151, v207, v245\n"
"v_add_f32 v208, 1.0, v198\n"
"v_mfma_f32_16x16x32_bf16 a[248:251], v[152:155], a[80:83], a[248:251]\n"
"s_waitcnt vmcnt(2)\n"
"buffer_store_dword v219, v220, s[36:39], s90 offen sc1\n"
"v_mfma_f32_16x16x32_bf16 a[248:251], v[156:159], a[84:87], a[248:251]\n"
"v_rcp_f32 v208, v208\n"
"v_mfma_f32_16x16x32_bf16 a[248:251], v[160:163], a[112:115], a[248:251]\n"
"v_mul_f32 v198, v198, v208\n"
"v_lshlrev_b32 v184, 16, v186\n"
"v_mfma_f32_16x16x32_bf16 a[248:251], v[164:167], a[116:119], a[248:251]\n"
"v_and_b32 v185, 0xffff0000, v186\n"
"v_lshlrev_b32 v186, 16, v187\n"
"v_mfma_f32_16x16x32_bf16 a[252:255], v[152:155], a[88:91], a[252:255]\n"
"v_and_b32 v187, 0xffff0000, v187\n"
"v_mul_f32 v184, v238, v184\n"
"v_mfma_f32_16x16x32_bf16 a[252:255], v[156:159], a[92:95], a[252:255]\n"
"v_mul_f32 v185, v239, v185\n"
"v_mul_f32 v186, v240, v186\n"
"v_mfma_f32_16x16x32_bf16 a[252:255], v[160:163], a[120:123], a[252:255]\n"
"v_mul_f32 v187, v241, v187\n"
"v_mul_f32 v184, v198, v184\n"
"v_mfma_f32_16x16x32_bf16 a[252:255], v[164:167], a[124:127], a[252:255]\n"
"v_mul_f32 v185, v198, v185\n"
"s_waitcnt lgkmcnt(0)\n"
"v_mfma_f32_16x16x32_bf16 a[224:227], v[168:171], v[48:51], a[224:227]\n"
"v_mul_f32 v186, v198, v186\n"
"v_mul_f32 v187, v198, v187\n"
"v_mfma_f32_16x16x32_bf16 a[224:227], v[172:175], v[52:55], a[224:227]\n"
"ds_read_b128 v[152:155], v223 offset:18432\n"
"v_fmac_f32 v184, v208, v242\n"
"v_mfma_f32_16x16x32_bf16 a[224:227], v[176:179], a[128:131], a[224:227]\n"
"ds_read_b128 v[156:159], v224 offset:18432\n"
"v_fmac_f32 v185, v208, v243\n"
"v_mfma_f32_16x16x32_bf16 a[224:227], v[180:183], a[132:135], a[224:227]\n"
"ds_read_b128 v[160:163], v223 offset:22528\n"
"v_fmac_f32 v186, v208, v244\n"
"v_mfma_f32_16x16x32_bf16 a[228:231], v[168:171], v[56:59], a[228:231]\n"
"ds_read_b128 v[164:167], v224 offset:22528\n"
"v_fmac_f32 v187, v208, v245\n"
"v_mfma_f32_16x16x32_bf16 a[228:231], v[172:175], v[60:63], a[228:231]\n"
"v_add_f32 v4, 1.0, v199\n"
"v_mfma_f32_16x16x32_bf16 a[228:231], v[176:179], a[136:139], a[228:231]\n"
"v_rcp_f32 v4, v4\n"
"v_mfma_f32_16x16x32_bf16 a[228:231], v[180:183], a[140:143], a[228:231]\n"
"s_waitcnt vmcnt(1)\n"
"v_cmp_ne_u32 vcc, s61, v232\n"
"s_cbranch_vccnz .Lk_slow5\n"
".Lk_back5:\n"
"v_mfma_f32_16x16x32_bf16 a[232:235], v[168:171], v[64:67], a[232:235]\n"
"buffer_load_dwordx4 a[160:163], v7, s[36:39], s70 offen offset:0 sc1\n"
"v_mfma_f32_16x16x32_bf16 a[232:235], v[172:175], v[68:71], a[232:235]\n"
"v_mul_f32 v199, v199, v4\n"
"v_lshlrev_b32 v188, 16, v190\n"
"v_mfma_f32_16x16x32_bf16 a[232:235], v[176:179], a[144:147], a[232:235]\n"
"v_and_b32 v189, 0xffff0000, v190\n"
"v_lshlrev_b32 v190, 16, v191\n"
"v_mfma_f32_16x16x32_bf16 a[232:235], v[180:183], a[148:151], a[232:235]\n"
"v_and_b32 v191, 0xffff0000, v191\n"
"v_mul_f32 v188, v238, v188\n"
"v_mfma_f32_16x16x32_bf16 a[236:239], v[168:171], v[72:75], a[236:239]\n"
"buffer_load_dwordx4 a[168:171], v7, s[36:39], s70 offen offset:2048 sc1\n"
"v_mfma_f32_16x16x32_bf16 a[236:239], v[172:175], v[76:79], a[236:239]\n"
"buffer_load_dword v246, off, s[36:39], s71 offset:512 sc1\n"
"v_mfma_f32_16x16x32_bf16 a[236:239], v[176:179], a[152:155], a[236:239]\n"
"v_mul_f32 v189, v239, v189\n"
"v_mul_f32 v190, v240, v190\n"
"v_mfma_f32_16x16x32_bf16 a[236:239], v[180:183], a[156:159], a[236:239]\n"
"v_mul_f32 v191, v241, v191\n"
"s_waitcnt lgkmcnt(0)\n"
"v_mfma_f32_16x16x32_bf16 a[240:243], v[152:155], v[48:51], a[240:243]\n"
"buffer_load_dwordx4 a[164:167], v7, s[36:39], s70 offen offset:1024 sc1\n"
"v_mfma_f32_16x16x32_bf16 a[240:243], v[156:159], v[52:55], a[240:243]\n"
"ds_read_b128 v[168:171], v223 offset:24576\n"
"v_mul_f32 v188, v199, v188\n"
"v_mfma_f32_16x16x32_bf16 a[240:243], v[160:163], a[128:131], a[240:243]\n"
"ds_read_b128 v[172:175], v224 offset:24576\n"
"v_mul_f32 v189, v199, v189\n"
"v_mfma_f32_16x16x32_bf16 a[240:243], v[164:167], a[132:135], a[240:243]\n"
"ds_read_b128 v[176:179], v223 offset:28672\n"
"v_mul_f32 v190, v199, v190\n"
"v_mfma_f32_16x16x32_bf16 a[244:247], v[152:155], v[56:59], a[244:247]\n"
"ds_read_b128 v[180:183], v224 offset:28672\n"
"v_mul_f32 v191, v199, v191\n"
"v_mfma_f32_16x16x32_bf16 a[244:247], v[156:159], v[60:63], a[244:247]\n"
"buffer_load_dwordx4 a[172:175], v7, s[36:39], s70 offen offset:3072 sc1\n"
"v_mfma_f32_16x16x32_bf16 a[244:247], v[160:163], a[136:139], a[244:247]\n"
"v_fmac_f32 v188, v4, v242\n"
"v_fmac_f32 v189, v4, v243\n"
"v_mfma_f32_16x16x32_bf16 a[244:247], v[164:167], a[140:143], a[244:247]\n"
"buffer_load_dword v247, off, s[36:39], s71 offset:1024 sc1\n"
"v_mfma_f32_16x16x32_bf16 a[248:251], v[152:155], v[64:67], a[248:251]\n"
"v_fmac_f32 v190, v4, v244\n"
"v_fmac_f32 v191, v4, v245\n"
"v_mfma_f32_16x16x32_bf16 a[248:251], v[156:159], v[68:71], a[248:251]\n"
"s_add_u32 s72, s63, 114688\n"
"v_lshrrev_b32 v5, 2, v209\n"
"v_mfma_f32_16x16x32_bf16 a[248:251], v[160:163], a[144:147], a[248:251]\n"
"v_and_b32 v6, 3, v209\n"
"v_lshl_add_u32 v5, v6, 3, v5\n"
"v_mfma_f32_16x16x32_bf16 a[248:251], v[164:167], a[148:151], a[248:251]\n"
"v_and_b32 v6, 7, v209\n"
"v_xor_b32 v6, v6, v210\n"
"v_mfma_f32_16x16x32_bf16 a[252:255], v[152:155], v[72:75], a[252:255]\n"
"v_lshlrev_b32 v6, 4, v6\n"
"v_lshl_add_u32 v5, v5, 7, v6\n"
"v_mfma_f32_16x16x32_bf16 a[252:255], v[156:159], v[76:79], a[252:255]\n"
"v_add_u32 v5, s72, v5\n"
"v_xor_b32 v6, 64, v5\n"
"v_mfma_f32_16x16x32_bf16 a[252:255], v[160:163], a[152:155], a[252:255]\n"
"v_and_b32 v192, 1, v225\n"
"v_lshlrev_b32 v192, 2, v192\n"
"v_mfma_f32_16x16x32_bf16 a[252:255], v[164:167], a[156:159], a[252:255]\n"
"v_xor_b32 v192, v192, v211\n"
"s_waitcnt lgkmcnt(0)\n"
"v_mfma_f32_16x16x32_bf16 a[224:227], v[168:171], v[96:99], a[224:227]\n"
"v_lshlrev_b32 v192, 4, v192\n"
"v_lshl_add_u32 v192, v225, 7, v192\n"
"v_mfma_f32_16x16x32_bf16 a[224:227], v[172:175], v[100:103], a[224:227]\n"
"ds_read_b128 v[152:155], v223 offset:26624\n"
"v_add_u32 v192, s72, v192\n"
"v_mfma_f32_16x16x32_bf16 a[224:227], v[176:179], a[0:3], a[224:227]\n"
"ds_read_b128 v[156:159], v224 offset:26624\n"
"v_xor_b32 v193, 16, v192\n"
"v_mfma_f32_16x16x32_bf16 a[224:227], v[180:183], a[4:7], a[224:227]\n"
"s_waitcnt vmcnt(3)\n"
"v_cmp_ne_u32 vcc, s61, v246\n"
"s_cbranch_vccnz .Lk_slow6\n"
".Lk_back6:\n"
"v_mfma_f32_16x16x32_bf16 a[228:231], v[168:171], v[104:107], a[228:231]\n"
"ds_read_b128 v[160:163], v223 offset:30720\n"
"v_xor_b32 v194, 32, v192\n"
"v_mfma_f32_16x16x32_bf16 a[228:231], v[172:175], v[108:111], a[228:231]\n"
"ds_read_b128 v[164:167], v224 offset:30720\n"
"v_xor_b32 v195, 48, v192\n"
"v_mfma_f32_16x16x32_bf16 a[228:231], v[176:179], a[8:11], a[228:231]\n"
"buffer_load_dwordx4 a[176:179], v7, s[36:39], s16 offen offset:0 sc1\n"
"v_mfma_f32_16x16x32_bf16 a[228:231], v[180:183], a[12:15], a[228:231]\n"
"s_lshl_b32 s72, s56, 6\n"
"s_lshl_b32 s73, s55, 2\n"
"v_mfma_f32_16x16x32_bf16 a[232:235], v[168:171], v[112:115], a[232:235]\n"
"s_add_u32 s72, s72, s73\n"
"s_add_u32 s72, s72, s54\n"
"v_mfma_f32_16x16x32_bf16 a[232:235], v[172:175], v[116:119], a[232:235]\n"
"s_mov_b32 s73, 40370176\n"
"v_mfma_f32_16x16x32_bf16 a[232:235], v[176:179], a[16:19], a[232:235]\n"
"buffer_load_dwordx4 a[184:187], v7, s[36:39], s16 offen offset:2048 sc1\n"
"v_mfma_f32_16x16x32_bf16 a[232:235], v[180:183], a[20:23], a[232:235]\n"
"s_cmp_eq_u32 s72, 63\n"
"s_cselect_b32 s83, s73, 1879048192\n"
"v_mfma_f32_16x16x32_bf16 a[236:239], v[168:171], v[120:123], a[236:239]\n"
"v_mfma_f32_16x16x32_bf16 a[236:239], v[172:175], v[124:127], a[236:239]\n"
"v_mfma_f32_16x16x32_bf16 a[236:239], v[176:179], a[24:27], a[236:239]\n"
"buffer_load_dwordx4 a[180:183], v7, s[36:39], s16 offen offset:1024 sc1\n"
"v_mfma_f32_16x16x32_bf16 a[236:239], v[180:183], a[28:31], a[236:239]\n"
"s_waitcnt lgkmcnt(0)\n"
"v_mfma_f32_16x16x32_bf16 a[240:243], v[152:155], v[96:99], a[240:243]\n"
"v_mfma_f32_16x16x32_bf16 a[240:243], v[156:159], v[100:103], a[240:243]\n"
"v_mfma_f32_16x16x32_bf16 a[240:243], v[160:163], a[0:3], a[240:243]\n"
"buffer_load_dwordx4 a[188:191], v7, s[36:39], s16 offen offset:3072 sc1\n"
"v_mfma_f32_16x16x32_bf16 a[240:243], v[164:167], a[4:7], a[240:243]\n"
"v_mfma_f32_16x16x32_bf16 a[244:247], v[152:155], v[104:107], a[244:247]\n"
"v_mfma_f32_16x16x32_bf16 a[244:247], v[156:159], v[108:111], a[244:247]\n"
"s_waitcnt vmcnt(4)\n"
"v_cmp_ne_u32 vcc, s61, v247\n"
"s_cbranch_vccnz .Lk_slow7\n"
".Lk_back7:\n"
"v_mfma_f32_16x16x32_bf16 a[244:247], v[160:163], a[8:11], a[244:247]\n"
"buffer_load_dwordx4 a[192:195], v7, s[36:39], s17 offen offset:0 sc1\n"
"v_mfma_f32_16x16x32_bf16 a[244:247], v[164:167], a[12:15], a[244:247]\n"
"v_mfma_f32_16x16x32_bf16 a[248:251], v[152:155], v[112:115], a[248:251]\n"
"v_mfma_f32_16x16x32_bf16 a[248:251], v[156:159], v[116:119], a[248:251]\n"
"v_mfma_f32_16x16x32_bf16 a[248:251], v[160:163], a[16:19], a[248:251]\n"
"buffer_load_dwordx4 a[200:203], v7, s[36:39], s17 offen offset:2048 sc1\n"
"v_mfma_f32_16x16x32_bf16 a[248:251], v[164:167], a[20:23], a[248:251]\n"
"v_mfma_f32_16x16x32_bf16 a[252:255], v[152:155], v[120:123], a[252:255]\n"
"v_mfma_f32_16x16x32_bf16 a[252:255], v[156:159], v[124:127], a[252:255]\n"
"v_mfma_f32_16x16x32_bf16 a[252:255], v[160:163], a[24:27], a[252:255]\n"
"buffer_load_dwordx4 a[196:199], v7, s[36:39], s17 offen offset:1024 sc1\n"
"v_mfma_f32_16x16x32_bf16 a[252:255], v[164:167], a[28:31], a[252:255]\n"
"v_mfma_f32_16x16x32_bf16 a[224:227], v[202:205], a[160:163], a[224:227]\n"
"v_mfma_f32_16x16x32_bf16 a[240:243], v[200:203], a[160:163], a[240:243]\n"
"v_mfma_f32_16x16x32_bf16 a[232:235], v[202:205], a[168:171], a[232:235]\n"
"buffer_load_dwordx4 a[204:207], v7, s[36:39], s17 offen offset:3072 sc1\n"
"v_mfma_f32_16x16x32_bf16 a[248:251], v[200:203], a[168:171], a[248:251]\n"
"v_mfma_f32_16x16x32_bf16 a[228:231], v[202:205], a[164:167], a[228:231]\n"
"v_mfma_f32_16x16x32_bf16 a[244:247], v[200:203], a[164:167], a[244:247]\n"
"v_mfma_f32_16x16x32_bf16 a[236:239], v[202:205], a[172:175], a[236:239]\n"
"v_mfma_f32_16x16x32_bf16 a[252:255], v[200:203], a[172:175], a[252:255]\n"
"s_waitcnt vmcnt(7)\n"
"v_mfma_f32_16x16x32_bf16 a[224:227], v[202:205], a[176:179], a[224:227]\n"
"v_mfma_f32_16x16x32_bf16 a[240:243], v[200:203], a[176:179], a[240:243]\n"
"s_waitcnt vmcnt(6)\n"
"v_mfma_f32_16x16x32_bf16 a[232:235], v[202:205], a[184:187], a[232:235]\n"
"v_mfma_f32_16x16x32_bf16 a[248:251], v[200:203], a[184:187], a[248:251]\n"
"s_waitcnt vmcnt(5)\n"
"v_mfma_f32_16x16x32_bf16 a[228:231], v[202:205], a[180:183], a[228:231]\n"
"v_mfma_f32_16x16x32_bf16 a[244:247], v[200:203], a[180:183], a[244:247]\n"
"s_waitcnt vmcnt(4)\n"
"v_mfma_f32_16x16x32_bf16 a[236:239], v[202:205], a[188:191], a[236:239]\n"
"v_mfma_f32_16x16x32_bf16 a[252:255], v[200:203], a[188:191], a[252:255]\n"
".Lk_fin:\n"
"s_waitcnt vmcnt(3)\n"
"v_mfma_f32_16x16x32_bf16 a[224:227], v[202:205], a[192:195], a[224:227]\n"
"v_mfma_f32_16x16x32_bf16 a[240:243], v[200:203], a[192:195], a[240:243]\n"
"s_waitcnt vmcnt(2)\n"
"v_mfma_f32_16x16x32_bf16 a[232:235], v[202:205], a[200:203], a[232:235]\n"
"v_mfma_f32_16x16x32_bf16 a[248:251], v[200:203], a[200:203], a[248:251]\n"
"s_waitcnt vmcnt(1)\n"
"v_mfma_f32_16x16x32_bf16 a[228:231], v[202:205], a[196:199], a[228:231]\n"
"v_mfma_f32_16x16x32_bf16 a[244:247], v[200:203], a[196:199], a[244:247]\n"
"s_waitcnt vmcnt(0)\n"
"v_mfma_f32_16x16x32_bf16 a[236:239], v[202:205], a[204:207], a[236:239]\n"
"v_mfma_f32_16x16x32_bf16 a[252:255], v[200:203], a[204:207], a[252:255]\n"
"buffer_store_dword v219, v220, s[36:39], s83 offen sc1\n"
"ds_write_b128 v5, a[224:227] offset:0\n"
"ds_write_b128 v6, a[240:243] offset:0\n"
"ds_write_b128 v5, a[232:235] offset:512\n"
"ds_write_b128 v6, a[248:251] offset:512\n"
"ds_read_b128 v[152:155], v192 offset:0\n"
"ds_read_b128 v[156:159], v193 offset:1024\n"
"ds_read_b128 v[160:163], v194 offset:2048\n"
"ds_read_b128 v[164:167], v195 offset:3072\n"
"ds_write_b128 v5, a[228:231] offset:16384\n"
"ds_write_b128 v6, a[244:247] offset:16384\n"
"ds_write_b128 v5, a[236:239] offset:16896\n"
"ds_write_b128 v6, a[252:255] offset:16896\n"
"ds_read_b128 v[168:171], v192 offset:16384\n"
"ds_read_b128 v[172:175], v193 offset:17408\n"
"ds_read_b128 v[176:179], v194 offset:18432\n"
"ds_read_b128 v[180:183], v195 offset:19456\n"
"s_waitcnt lgkmcnt(11)\n"
"v_fmac_f32 v226, v233, v152\n"
"v_fmac_f32 v227, v233, v153\n"
"v_fmac_f32 v228, v233, v154\n"
"v_fmac_f32 v229, v233, v155\n"
"buffer_store_dwordx4 v[226:229], v231, s[32:35], s74 offen sc1\n"
"s_waitcnt lgkmcnt(10)\n"
"v_fmac_f32 v248, v216, v156\n"
"v_fmac_f32 v249, v216, v157\n"
"v_fmac_f32 v250, v216, v158\n"
"v_fmac_f32 v251, v216, v159\n"
"buffer_store_dwordx4 v[248:251], v231, s[32:35], s75 offen sc1\n"
"s_waitcnt lgkmcnt(9)\n"
"v_fmac_f32 v252, v217, v160\n"
"v_fmac_f32 v253, v217, v161\n"
"v_fmac_f32 v254, v217, v162\n"
"v_fmac_f32 v255, v217, v163\n"
"buffer_store_dwordx4 v[252:255], v231, s[32:35], s76 offen sc1\n"
"s_waitcnt lgkmcnt(8)\n"
"v_fmac_f32 v212, v218, v164\n"
"v_fmac_f32 v213, v218, v165\n"
"v_fmac_f32 v214, v218, v166\n"
"v_fmac_f32 v215, v218, v167\n"
"buffer_store_dwordx4 v[212:215], v231, s[32:35], s77 offen sc1\n"
"s_waitcnt lgkmcnt(3)\n"
"v_fmac_f32 v144, v206, v168\n"
"v_fmac_f32 v145, v206, v169\n"
"v_fmac_f32 v146, v206, v170\n"
"v_fmac_f32 v147, v206, v171\n"
".Lk_h13:\n"
"s_cbranch_execz .Lk_hend\n"
"buffer_store_dwordx4 v[144:147], v231, s[32:35], s78 offen sc1\n"
"s_waitcnt lgkmcnt(2)\n"
"v_fmac_f32 v148, v207, v172\n"
"v_fmac_f32 v149, v207, v173\n"
"v_fmac_f32 v150, v207, v174\n"
"v_fmac_f32 v151, v207, v175\n"
"buffer_store_dwordx4 v[148:151], v231, s[32:35], s79 offen sc1\n"
"s_waitcnt lgkmcnt(1)\n"
"v_fmac_f32 v184, v208, v176\n"
"v_fmac_f32 v185, v208, v177\n"
"v_fmac_f32 v186, v208, v178\n"
"v_fmac_f32 v187, v208, v179\n"
"buffer_store_dwordx4 v[184:187], v231, s[32:35], s80 offen sc1\n"
"s_waitcnt lgkmcnt(0)\n"
"v_fmac_f32 v188, v4, v180\n"
"v_fmac_f32 v189, v4, v181\n"
"v_fmac_f32 v190, v4, v182\n"
"v_fmac_f32 v191, v4, v183\n"
"buffer_store_dwordx4 v[188:191], v231, s[32:35], s81 offen sc1\n"
".Lk_done:\n"
"s_branch .Lk_end\n"
".Lk_slow0:\n"
"s_sleep 1\n"
"buffer_load_dword v232, v233, s[36:39], s67 offen sc1\n"
"s_waitcnt vmcnt(0)\n"
"v_cmp_ne_u32 vcc, s61, v232\n"
"s_cbranch_vccz .Lk_back0\n"
"s_sub_u32 s62, s62, 1\n"
"s_cmp_gt_i32 s62, 0\n"
"s_cbranch_scc1 .Lk_slow0\n"
"s_branch .Lk_back0\n"
".Lk_slow1:\n"
"s_sleep 1\n"
"buffer_load_dwordx4 v[184:187], v7, s[36:39], s100 offen sc1\n"
"buffer_load_dwordx4 v[188:191], v7, s[36:39], s16 offen sc1\n"
"buffer_load_dwordx4 v[192:195], v7, s[36:39], s89 offen sc1\n"
"buffer_load_dwordx4 v[196:199], v7, s[36:39], s90 offen sc1\n"
"s_waitcnt vmcnt(0)\n"
"v_xor_b32 v185, s94, v185\n"
"v_xor_b32 v187, s94, v187\n"
"v_xor_b32 v189, s95, v189\n"
"v_xor_b32 v191, s95, v191\n"
"v_xor_b32 v193, s96, v193\n"
"v_xor_b32 v195, s96, v195\n"
"v_xor_b32 v197, s97, v197\n"
"v_xor_b32 v199, s97, v199\n"
"v_or3_b32 v208, v185, v187, v189\n"
"v_or3_b32 v6, v191, v193, v195\n"
"v_or3_b32 v208, v208, v197, v199\n"
"v_or_b32 v208, v208, v6\n"
"v_cmp_ne_u32 vcc, 0, v208\n"
"s_cbranch_vccz .Lk_back1\n"
"s_sub_u32 s62, s62, 1\n"
"s_cmp_gt_i32 s62, 0\n"
"s_cbranch_scc1 .Lk_slow1\n"
"s_branch .Lk_back1\n"
".Lk_slow2:\n"
"s_sleep 1\n"
"buffer_load_dword v232, v233, s[36:39], s67 offen sc1\n"
"s_waitcnt vmcnt(0)\n"
"v_cmp_ne_u32 vcc, s61, v232\n"
"s_cbranch_vccz .Lk_back2\n"
"s_sub_u32 s62, s62, 1\n"
"s_cmp_gt_i32 s62, 0\n"
"s_cbranch_scc1 .Lk_slow2\n"
"s_branch .Lk_back2\n"
".Lk_slow3:\n"
"s_sleep 1\n"
"buffer_load_dword v232, v233, s[36:39], s67 offen sc1\n"
"s_waitcnt vmcnt(0)\n"
"v_cmp_ne_u32 vcc, s61, v232\n"
"s_cbranch_vccz .Lk_back3\n"
"s_sub_u32 s62, s62, 1\n"
"s_cmp_gt_i32 s62, 0\n"
"s_cbranch_scc1 .Lk_slow3\n"
"s_branch .Lk_back3\n"
".Lk_slow4:\n"
"s_sleep 1\n"
"buffer_load_dword v232, v233, s[36:39], s67 offen sc1\n"
"s_waitcnt vmcnt(0)\n"
"v_cmp_ne_u32 vcc, s61, v232\n"
"s_cbranch_vccz .Lk_back4\n"
"s_sub_u32 s62, s62, 1\n"
"s_cmp_gt_i32 s62, 0\n"
"s_cbranch_scc1 .Lk_slow4\n"
"s_branch .Lk_back4\n"
".Lk_slow5:\n"
"s_sleep 1\n"
"buffer_load_dword v232, off, s[36:39], s71 offset:0 sc1\n"
"s_waitcnt vmcnt(0)\n"
"v_cmp_ne_u32 vcc, s61, v232\n"
"s_cbranch_vccz .Lk_back5\n"
"s_sub_u32 s62, s62, 1\n"
"s_cmp_gt_i32 s62, 0\n"
"s_cbranch_scc1 .Lk_slow5\n"
"s_branch .Lk_back5\n"
".Lk_slow6:\n"
"s_sleep 1\n"
"buffer_load_dword v246, off, s[36:39], s71 offset:512 sc1\n"
"s_waitcnt vmcnt(0)\n"
"v_cmp_ne_u32 vcc, s61, v246\n"
"s_cbranch_vccz .Lk_back6\n"
"s_sub_u32 s62, s62, 1\n"
"s_cmp_gt_i32 s62, 0\n"
"s_cbranch_scc1 .Lk_slow6\n"
"s_branch .Lk_back6\n"
".Lk_slow7:\n"
"s_sleep 1\n"
"buffer_load_dword v247, off, s[36:39], s71 offset:1024 sc1\n"
"s_waitcnt vmcnt(0)\n"
"v_cmp_ne_u32 vcc, s61, v247\n"
"s_cbranch_vccz .Lk_back7\n"
"s_sub_u32 s62, s62, 1\n"
"s_cmp_gt_i32 s62, 0\n"
"s_cbranch_scc1 .Lk_slow7\n"
"s_branch .Lk_back7\n"
".Lk_end:\n"
  : "+v"(tid) : "s"(__builtin_amdgcn_kernarg_segment_ptr()), "s"(blockIdx.x), "s"((unsigned)(size_t)lds)
  : "v1","v2","v3","v4","v5","v6","v7","v8","v9","v10","v11","v12","v13","v14","v15","v16","v17","v18","v19","v20","v21","v22","v23","v24","v25","v26","v27","v28","v29","v30","v31","v32","v33","v34","v35","v36","v37","v38","v39","v40","v41","v42","v43","v44","v45","v46","v47","v48","v49","v50","v51","v52","v53","v54","v55","v56","v57","v58","v59","v60","v61","v62","v63","v64","v65","v66","v67","v68","v69","v70","v71","v72","v73","v74","v75","v76","v77","v78","v79","v80","v81","v82","v83","v84","v85","v86","v87","v88","v89","v90","v91","v92","v93","v94","v95","v96","v97","v98","v99","v100","v101","v102","v103","v104","v105","v106","v107","v108","v109","v110","v111","v112","v113","v114","v115","v116","v117","v118","v119","v120","v121","v122","v123","v124","v125","v126","v127","v128","v129","v130","v131","v132","v133","v134","v135","v136","v137","v138","v139","v140","v141","v142","v143","v144","v145","v146","v147","v148","v149","v150","v151","v152","v153","v154","v155","v156","v157","v158","v159","v160","v161","v162","v163","v164","v165","v166","v167","v168","v169","v170","v171","v172","v173","v174","v175","v176","v177","v178","v179","v180","v181","v182","v183","v184","v185","v186","v187","v188","v189","v190","v191","v192","v193","v194","v195","v196","v197","v198","v199","v200","v201","v202","v203","v204","v205","v206","v207","v208","v209","v210","v211","v212","v213","v214","v215","v216","v217","v218","v219","v220","v221","v222","v223","v224","v225","v226","v227","v228","v229","v230","v231","v232","v233","v234","v235","v236","v237","v238","v239","v240","v241","v242","v243","v244","v245","v246","v247","v248","v249","v250","v251","v252","v253","v254","v255","a0","a1","a2","a3","a4","a5","a6","a7","a8","a9","a10","a11","a12","a13","a14","a15","a16","a17","a18","a19","a20","a21","a22","a23","a24","a25","a26","a27","a28","a29","a30","a31","a32","a33","a34","a35","a36","a37","a38","a39","a40","a41","a42","a43","a44","a45","a46","a47","a48","a49","a50","a51","a52","a53","a54","a55","a56","a57","a58","a59","a60","a61","a62","a63","a64","a65","a66","a67","a68","a69","a70","a71","a72","a73","a74","a75","a76","a77","a78","a79","a80","a81","a82","a83","a84","a85","a86","a87","a88","a89","a90","a91","a92","a93","a94","a95","a96","a97","a98","a99","a100","a101","a102","a103","a104","a105","a106","a107","a108","a109","a110","a111","a112","a113","a114","a115","a116","a117","a118","a119","a120","a121","a122","a123","a124","a125","a126","a127","a128","a129","a130","a131","a132","a133","a134","a135","a136","a137","a138","a139","a140","a141","a142","a143","a144","a145","a146","a147","a148","a149","a150","a151","a152","a153","a154","a155","a156","a157","a158","a159","a160","a161","a162","a163","a164","a165","a166","a167","a168","a169","a170","a171","a172","a173","a174","a175","a176","a177","a178","a179","a180","a181","a182","a183","a184","a185","a186","a187","a188","a189","a190","a191","a192","a193","a194","a195","a196","a197","a198","a199","a200","a201","a202","a203","a204","a205","a206","a207","a208","a209","a210","a211","a212","a213","a214","a215","a216","a217","a218","a219","a220","a221","a222","a223","a224","a225","a226","a227","a228","a229","a230","a231","a232","a233","a234","a235","a236","a237","a238","a239","a240","a241","a242","a243","a244","a245","a246","a247","a248","a249","a250","a251","a252","a253","a254","a255","s4","s5","s6","s7","s8","s9","s10","s11","s12","s13","s14","s15","s16","s17","s18","s19","s20","s21","s22","s23","s24","s25","s26","s27","s28","s29","s30","s31","s32","s33","s34","s35","s36","s37","s38","s39","s40","s41","s42","s43","s44","s45","s46","s47","s48","s49","s50","s51","s52","s53","s54","s55","s56","s57","s58","s59","s60","s61","s62","s63","s64","s65","s66","s67","s68","s69","s70","s71","s72","s73","s74","s75","s76","s77","s78","s79","s80","s81","s82","s83","s84","s85","s86","s87","s88","s89","s90","s91","s92","s93","s94","s95","s96","s97","s98","s99","s100","s101","vcc","memory");
}

extern "C" void kernel_launch(void* const* d_in, const int* in_sizes, int n_in, void* d_out, int out_size,
                              void* d_ws, size_t ws_size, hipStream_t stream) {
  const unsigned F = 0x00020000u;
  gated_mix_s5_ffn_kernel<<<256, 256, 0, stream>>>(
      d_in[0], 4194304u, F, d_in[10], 7340032u + 4096u, F, d_in[3], 2097152u, F, d_in[4], 2097152u, F,
      d_in[12], 7340032u, F, d_in[5], 2097152u, F, d_in[6], 2097152u, F, d_out, 8388608u, F,
      d_ws, 40636416u, F,
      d_in[14], d_in[11], d_in[1], d_in[2], d_in[7], d_in[8], d_in[13]);
}
```
